```python
import torch

def _softmax(x, axis=-1):
    return torch.softmax(x, dim=axis)

def _gelu(x):
    return torch.nn.functional.gelu(x, approximate='tanh')

def _silu(x):
    return torch.nn.functional.silu(x)

def _sigmoid(x):
    return torch.sigmoid(x)

def _layernorm(x, axis=-1, eps=1e-05):
    m = x.mean(dim=axis, keepdim=True)
    v = x.var(dim=axis, keepdim=True, unbiased=False)
    return (x - m) / torch.sqrt(v + eps)

def _scatter_add(tgt, idx, src, axis=0):
    return tgt.clone().index_add_(axis, idx, src)

def _segment_sum(src, seg, n):
    out = torch.zeros((n,) + src.shape[1:], dtype=src.dtype)
    return out.index_add_(0, seg, src)

def _pad(x, pw):
    flat = tuple((v for pair in reversed(pw) for v in pair))
    return torch.nn.functional.pad(x, flat)
kernel_name = 'adaLN_cond__sliding_block__dense_activation_relu__prompt'

def setup_inputs():

    _w = lambda a: (lambda t: t.to(torch.bfloat16) if t.is_floating_point() else t)(torch.as_tensor(a))

    def _A_setup():
        B, S, D = (1, 2048, 448)
        return {'x': _w((lambda *s: torch.randn(*s, dtype=torch.bfloat16) * 0.05)(B, S, D)), 'wqkv': _w((lambda *s: torch.randn(*s, dtype=torch.bfloat16) * 0.05)(D, 3 * D)), 'wo': _w((lambda *s: torch.randn(*s, dtype=torch.bfloat16) * 0.05)(D, D))}
    _A_in = _A_setup()

    def _B_setup():
        B, S, D, Dff = (1, 2048, 448, 1792)
        return {'x': _w((lambda *s: torch.randn(*s, dtype=torch.bfloat16) * 0.05)(B, S, D)), 'w1': _w((lambda *s: torch.randn(*s, dtype=torch.bfloat16) * 0.05)(D, Dff)), 'w2': _w((lambda *s: torch.randn(*s, dtype=torch.bfloat16) * 0.05)(Dff, D))}
    _B_in = _B_setup()
    return {'A_x': _A_in['x'], 'A_wqkv': _A_in['wqkv'], 'A_wo': _A_in['wo'], 'B_x': _B_in['x'], 'B_w1': _B_in['w1'], 'B_w2': _B_in['w2'], 'c': _w((lambda *s: torch.randn(*s, dtype=torch.bfloat16) * 0.1)(1, 448) * 8.0), 'w_mod': _w((lambda *s: torch.randn(*s, dtype=torch.bfloat16) * 0.1)(448, 6 * 448) * 448 ** (-0.5) * 8.0)}

def reference(A_x, A_wqkv, A_wo, B_x, B_w1, B_w2, c, w_mod):

    def _A_ref(x, wqkv, wo):
        B, S, D = x.shape
        H = 16
        Dh = D // H
        W = 256
        qkv = (lambda x, *d: x.permute(*d))((x @ wqkv).reshape(B, S, 3, H, Dh), 2, 0, 3, 1, 4)
        q, k, v = (qkv[0], qkv[1], qkv[2])
        scores = torch.einsum('bhqd,bhkd->bhqk', q, k) * Dh ** (-0.5)
        idx = torch.arange(S)
        mask = (idx[:, None] - idx[None, :] >= 0) & (idx[:, None] - idx[None, :] < W)
        scores = torch.where(mask[None, None], scores, -1000000000.0)
        p = _softmax(scores, axis=-1)
        o = torch.einsum('bhqk,bhkd->bhqd', p, v)
        return (lambda x, *d: x.permute(*d))(o, 0, 2, 1, 3).reshape(B, S, D) @ wo

    def _B_ref(x, w1, w2):
        h = torch.einsum('bsd,df->bsf', x, w1)
        zero = torch.zeros_like(h)
        h = torch.maximum(h, zero)
        return torch.einsum('bsf,fd->bsd', h, w2)
    x = A_x
    mod = (_silu(c) @ w_mod).reshape(c.shape[0], 6, 1, -1)
    sa, ca, ga, sb, cb, gb = (mod[:, 0].clone(), mod[:, 1].clone(), mod[:, 2].clone(), mod[:, 3].clone(), mod[:, 4].clone(), mod[:, 5].clone())
    h = _layernorm(x, axis=-1) * (1 + ca) + sa
    x = x + ga * _A_ref(h, A_wqkv, A_wo)
    h = _layernorm(x, axis=-1) * (1 + cb) + sb
    x = x + gb * _B_ref(h, B_w1, B_w2)
    return x
```

```cpp
#include <hip/hip_runtime.h>
#include <hip/hip_bf16.h>
#include <stdint.h>

constexpr int S = 2048, D = 448, H = 16, DH = 28, W = 256, DFF = 1792;

__device__ __forceinline__ float bf2f(unsigned short b) { return __uint_as_float((unsigned)b << 16); }
__device__ __forceinline__ unsigned short f2bf(float f) {
  unsigned u = __float_as_uint(f);
  u += 0x7FFFu + ((u >> 16) & 1u);
  return (unsigned short)(u >> 16);
}
__device__ __forceinline__ float rbf(float f) { return bf2f(f2bf(f)); }

__global__ void k_mod(const unsigned short* c, const unsigned short* wmod, float* mod) {
  int n = blockIdx.x * blockDim.x + threadIdx.x;
  if (n >= 6 * D) return;
  float acc = 0.f;
  for (int k = 0; k < D; ++k) {
    float cv = bf2f(c[k]);
    float s = rbf(cv / (1.f + __expf(-cv)));
    acc += s * bf2f(wmod[(size_t)k * 6 * D + n]);
  }
  mod[n] = rbf(acc);
}

__global__ void k_ln(const unsigned short* x, const float* shift, const float* scale, unsigned short* h) {
  int row = blockIdx.x * 4 + (threadIdx.x >> 6), lane = threadIdx.x & 63;
  float v[7];
  float s = 0.f;
  for (int i = 0; i < 7; ++i) { v[i] = bf2f(x[(size_t)row * D + lane + 64 * i]); s += v[i]; }
  for (int m = 32; m >= 1; m >>= 1) s += __shfl_xor(s, m);
  float mean = s / D;
  float q = 0.f;
  for (int i = 0; i < 7; ++i) { float d = v[i] - mean; q += d * d; }
  for (int m = 32; m >= 1; m >>= 1) q += __shfl_xor(q, m);
  float r = rsqrtf(q / D + 1e-5f);
  for (int i = 0; i < 7; ++i) {
    int k = lane + 64 * i;
    float xn = rbf((v[i] - mean) * r);
    h[(size_t)row * D + k] = f2bf(xn * rbf(1.f + scale[k]) + shift[k]);
  }
}

__global__ void k_gemm(const unsigned short* A, const unsigned short* B, unsigned short* C, int M, int N, int K, int mode,
                       const unsigned short* res, const float* gate) {
  int n = blockIdx.x * 64 + (threadIdx.x & 63);
  int m = blockIdx.y * 4 + (threadIdx.x >> 6);
  if (n >= N) return;
  float acc = 0.f;
  for (int k = 0; k < K; ++k) acc += bf2f(A[(size_t)m * K + k]) * bf2f(B[(size_t)k * N + n]);
  if (mode == 1) acc = fmaxf(acc, 0.f);
  if (mode == 2) acc = bf2f(res[(size_t)m * N + n]) + rbf(gate[n] * rbf(acc));
  C[(size_t)m * N + n] = f2bf(acc);
}

__global__ void k_attn(const unsigned short* qkv, unsigned short* o) {
  int gid = blockIdx.x * 4 + (threadIdx.x >> 6), lane = threadIdx.x & 63;
  int q = gid / H, h = gid % H;
  float qv[DH];
  for (int d = 0; d < DH; ++d) qv[d] = bf2f(qkv[(size_t)q * 3 * D + h * DH + d]);
  float sc[4];
  float mx = -1e30f;
  for (int i = 0; i < 4; ++i) {
    int key = q - (lane + 64 * i);
    float s = -1e30f;
    if (key >= 0) {
      s = 0.f;
      for (int d = 0; d < DH; ++d) s += qv[d] * bf2f(qkv[(size_t)key * 3 * D + D + h * DH + d]);
      s *= 0.18898223650461362f;
    }
    sc[i] = s; mx = fmaxf(mx, s);
  }
  for (int m = 32; m >= 1; m >>= 1) mx = fmaxf(mx, __shfl_xor(mx, m));
  float sum = 0.f;
  for (int i = 0; i < 4; ++i) { sc[i] = __expf(sc[i] - mx); sum += sc[i]; }
  for (int m = 32; m >= 1; m >>= 1) sum += __shfl_xor(sum, m);
  for (int d = 0; d < DH; ++d) {
    float a = 0.f;
    for (int i = 0; i < 4; ++i) {
      int key = q - (lane + 64 * i);
      if (key >= 0) a += sc[i] * bf2f(qkv[(size_t)key * 3 * D + 2 * D + h * DH + d]);
    }
    for (int m = 32; m >= 1; m >>= 1) a += __shfl_xor(a, m);
    if (lane == 0) o[(size_t)q * D + h * DH + d] = f2bf(a / sum);
  }
}

extern "C" void kernel_launch(void* const* d_in, const int* in_sizes, int n_in, void* d_out, int out_size, void* d_ws,
                              size_t ws_size, hipStream_t stream) {
  const unsigned short* x = (const unsigned short*)d_in[0];
  const unsigned short* wqkv = (const unsigned short*)d_in[1];
  const unsigned short* wo = (const unsigned short*)d_in[2];
  const unsigned short* w1 = (const unsigned short*)d_in[4];
  const unsigned short* w2 = (const unsigned short*)d_in[5];
  const unsigned short* c = (const unsigned short*)d_in[6];
  const unsigned short* wmod = (const unsigned short*)d_in[7];
  unsigned short* out = (unsigned short*)d_out;
  char* ws = (char*)d_ws;
  float* mod = (float*)ws;
  unsigned short* h = (unsigned short*)(ws + 65536);
  unsigned short* qkv = h + S * D;
  unsigned short* o = qkv + S * 3 * D;
  unsigned short* x1 = o + S * D;
  unsigned short* hid = x1 + S * D;
  k_mod<<<(6 * D + 255) / 256, 256, 0, stream>>>(c, wmod, mod);
  k_ln<<<S / 4, 256, 0, stream>>>(x, mod, mod + D, h);
  k_gemm<<<dim3(3 * D / 64, S / 4), 256, 0, stream>>>(h, wqkv, qkv, S, 3 * D, D, 0, nullptr, nullptr);
  k_attn<<<S * H / 4, 256, 0, stream>>>(qkv, o);
  k_gemm<<<dim3(D / 64, S / 4), 256, 0, stream>>>(o, wo, x1, S, D, D, 2, x, mod + 2 * D);
  k_ln<<<S / 4, 256, 0, stream>>>(x1, mod + 3 * D, mod + 4 * D, h);
  k_gemm<<<dim3(DFF / 64, S / 4), 256, 0, stream>>>(h, w1, hid, S, DFF, D, 1, nullptr, nullptr);
  k_gemm<<<dim3(D / 64, S / 4), 256, 0, stream>>>(hid, w2, out, S, D, DFF, 2, x1, mod + 5 * D);
}
```

```cpp
#include <hip/hip_runtime.h>
#include <stdint.h>

typedef unsigned int u32;
typedef unsigned long long u64;
typedef unsigned short u16;
typedef u32 v4u __attribute__((ext_vector_type(4)));
typedef u32 v2u __attribute__((ext_vector_type(2)));
typedef float f4 __attribute__((ext_vector_type(4)));
typedef float f2 __attribute__((ext_vector_type(2)));
typedef __bf16 bf2v __attribute__((ext_vector_type(2)));
typedef __bf16 bf8v __attribute__((ext_vector_type(8)));
#define GAS __attribute__((address_space(1)))
#define LAS __attribute__((address_space(3)))
typedef GAS u64 gu64;
#define RLX_AGENT __ATOMIC_RELAXED, __HIP_MEMORY_SCOPE_AGENT
typedef __amdgpu_buffer_rsrc_t rsrc_t;

constexpr int S_ = 2048, D_ = 448, DFF_ = 1792;
constexpr int WS_GRAN = 4096;
constexpr int WS_KV = 65536;
constexpr int WS_O = WS_KV + 4194304;
constexpr int WS_X1 = WS_O + 2097152;
constexpr int WS_HID = WS_X1 + 1835008;
constexpr int WS_END = WS_HID + 7340032;
constexpr int WS_DBG = WS_END;
constexpr int L_WQ = 0;
constexpr int L_K = 0, L_V = 24576;
constexpr int L_WO = 86016;
constexpr int L_SM = 151552;
constexpr int L_AS1 = L_SM;
constexpr int L_A2 = L_AS1;
constexpr int L_SB = L_A2 + 1792;
constexpr int L_GA = L_SB + 896;
constexpr int L_GB = L_GA + 256;
constexpr int L_RED = L_GB + 256;
constexpr int L_X1S = L_RED + 1344;
constexpr int L_END = L_X1S + 7168;
static_assert(L_END <= 163840, "lds");
constexpr int LDS_BYTES = 163840;
constexpr u32 OOB = 0x80000000u;
constexpr unsigned SPIN_CAP = 1u << 15;

__device__ __forceinline__ u32 pk(float lo, float hi) { f2 v = {lo, hi}; return __builtin_bit_cast(u32, __builtin_convertvector(v, bf2v)); }
__device__ __forceinline__ float dot2(u32 a, u32 b, float c) { return __builtin_amdgcn_fdot2_f32_bf16(__builtin_bit_cast(bf2v, a), __builtin_bit_cast(bf2v, b), c, false); }
__device__ __forceinline__ f4 mfma(v4u a, v4u b, f4 c) { return __builtin_amdgcn_mfma_f32_16x16x32_bf16(__builtin_bit_cast(bf8v, a), __builtin_bit_cast(bf8v, b), c, 0, 0, 0); }
__device__ __forceinline__ float lo2f(u32 v) { return __builtin_bit_cast(float, v << 16); }
__device__ __forceinline__ float hi2f(u32 v) { return __builtin_bit_cast(float, v & 0xffff0000u); }
__device__ __forceinline__ u32 plo(u32 r0, u32 r1) { return __builtin_amdgcn_perm(r1, r0, 0x05040100u); }
__device__ __forceinline__ u32 phi(u32 r0, u32 r1) { return __builtin_amdgcn_perm(r1, r0, 0x07060302u); }
__device__ __forceinline__ u32 nosent(u32 v) { return v < 0xFFFFFFFEu ? v : 0xFFFFFFFEu; }
__device__ __forceinline__ v4u nosent4(v4u v) { v.x = nosent(v.x); v.y = nosent(v.y); v.z = nosent(v.z); v.w = nosent(v.w); return v; }
__device__ __forceinline__ u32 umax3(u32 a, u32 b, u32 c) { u32 m = a > b ? a : b; return m > c ? m : c; }
__device__ __forceinline__ u32 chk(u32 m, v4u v) { return umax3(umax3(m, v.x, v.y), v.z, v.w); }
template <int C> __device__ __forceinline__ float dppf(float v) { return __builtin_bit_cast(float, __builtin_amdgcn_update_dpp(0, __builtin_bit_cast(int, v), C, 0xf, 0xf, false)); }
__device__ __forceinline__ float rowsum16(float v) { v += dppf<0xB1>(v); v += dppf<0x4E>(v); v += dppf<0x141>(v); v += dppf<0x140>(v); return v; }
__device__ __forceinline__ v4u ld4(rsrc_t r, u32 vo, u32 so) { return __builtin_amdgcn_raw_buffer_load_b128(r, vo, so, 0); }
__device__ __forceinline__ v4u ld4s(rsrc_t r, u32 vo, u32 so) { return __builtin_amdgcn_raw_buffer_load_b128(r, vo, so, 16); }
__device__ __forceinline__ v2u ld2(rsrc_t r, u32 vo, u32 so) { return __builtin_amdgcn_raw_buffer_load_b64(r, vo, so, 0); }
__device__ __forceinline__ u32 ld1(rsrc_t r, u32 vo, u32 so) { return __builtin_amdgcn_raw_buffer_load_b32(r, vo, so, 0); }
__device__ __forceinline__ void st4s(rsrc_t r, u32 vo, u32 so, v4u v) { __builtin_amdgcn_raw_buffer_store_b128(v, r, vo + so, 0, 16); }
__device__ __forceinline__ void st2s(rsrc_t r, u32 vo, u32 so, v2u v) { __builtin_amdgcn_raw_buffer_store_b64(v, r, vo, so, 16); }
#define LDS4(off) (*(LAS v4u*)(lds + (off)))
#define LDS2(off) (*(LAS v2u*)(lds + (off)))
#define LDSF4(off) (*(LAS f4*)(lds + (off)))
#define LDSF(off) (*(LAS float*)(lds + (off)))
#define LDSH(off) (*(LAS u16*)(lds + (off)))
#define CBAR() asm volatile("" ::: "memory")
#define PIN(v) asm volatile("" : "+v"(v))
#define DRAIN() asm volatile("s_waitcnt vmcnt(0)" ::: "memory")
#define STAMPT(i)

__global__ __launch_bounds__(512, 2) void mega(const u16* __restrict__ x, const u16* __restrict__ wqkv, const u16* __restrict__ wo,
                                               const u16* __restrict__ w1, const u16* __restrict__ w2, const u16* __restrict__ cvec,
                                               const u16* __restrict__ wmod, u16* __restrict__ out, char* ws) {
  extern __shared__ __attribute__((aligned(16))) char lds_g[];
  LAS char* lds = (LAS char*)lds_g;
  const int tid = threadIdx.x, lane = tid & 63, w = __builtin_amdgcn_readfirstlane(tid >> 6);
  const int l15 = lane & 15, g = lane >> 4;
  const int b = blockIdx.x, xcd = b & 7, slot = b >> 3;
  const int g16 = xcd * 2 + (slot >> 4), head = slot & 15;
  const int g32 = xcd * 4 + (slot >> 3), c8 = slot & 7;
  const rsrc_t rws = __builtin_amdgcn_make_buffer_rsrc(ws, 0, WS_END, 0x00020000);
  const rsrc_t rx = __builtin_amdgcn_make_buffer_rsrc((void*)x, 0, S_ * D_ * 2, 0x00020000);
  const rsrc_t rwq = __builtin_amdgcn_make_buffer_rsrc((void*)wqkv, 0, D_ * 3 * D_ * 2, 0x00020000);
  const rsrc_t rwo = __builtin_amdgcn_make_buffer_rsrc((void*)wo, 0, D_ * D_ * 2, 0x00020000);
  const rsrc_t rw1 = __builtin_amdgcn_make_buffer_rsrc((void*)w1, 0, D_ * DFF_ * 2, 0x00020000);
  const rsrc_t rw2 = __builtin_amdgcn_make_buffer_rsrc((void*)w2, 0, DFF_ * D_ * 2, 0x00020000);
  const rsrc_t rwm = __builtin_amdgcn_make_buffer_rsrc((void*)wmod, 0, D_ * 6 * D_ * 2, 0x00020000);
  const v4u SENT = {~0u, ~0u, ~0u, ~0u};
  STAMPT(0);

  if (w == 7) {
#pragma unroll
    for (int i = 0; i < 16; ++i) st4s(rws, lane * 16, WS_KV + (g16 * 16 + head) * 16384 + i * 1024, SENT);
#pragma unroll
    for (int i = 0; i < 8; ++i) st4s(rws, lane * 16, WS_O + ((8 * g16 + i) * 16 + head) * 1024, SENT);
    DRAIN();
  }
  const u64 epoch = __hip_atomic_load((gu64*)ws, RLX_AGENT);
  v2u wA = {0, 0};
  v4u wB = {0, 0, 0, 0};
  float cval = 0.f;
  if (w < 7 && slot < 28) {
    cval = lo2f(cvec[tid]);
    wA = ld2(rwm, tid * 5376, (112 * xcd + 4 * slot) * 2);
    wB = ld4(rwm, tid * 5376, (896 + 224 * xcd + 8 * slot) * 2);
  }
  v4u xr[14];
  {
    const u32 vo = (u32)(l15 * D_ + 8 * g) * 2;
#pragma unroll
    for (int s = 0; s < 14; ++s) xr[s] = ld4(rx, vo, (u32)((128 * g16 + 16 * w) * D_ + 32 * s) * 2);
  }
  u32 rq[6][8];
  {
    const u32 vo = l15 < 14 ? (u32)(8 * g * 1344 + 2 * l15) * 2 : OOB;
#pragma unroll
    for (int i = 0; i < 6; ++i) {
      const int u = w + 8 * i;
      if (u < 42) {
        const int mat = u / 14, s = u - 14 * mat;
#pragma unroll
        for (int j = 0; j < 8; ++j) rq[i][j] = ld1(rwq, vo, (u32)((32 * s + j) * 1344 + mat * 448 + 28 * head) * 2);
      }
    }
  }
  u32 ro[4][8];
  {
#pragma unroll
    for (int i = 0; i < 4; ++i) {
      const int u = w + 8 * i, nh = u >> 4, hh = u & 15;
      const u32 vo = (nh == 0 || l15 < 12) ? (u32)(8 * g * D_ + 2 * l15) * 2 : OOB;
      const u32 vo2 = g < 3 ? vo : OOB;
#pragma unroll
      for (int j = 0; j < 8; ++j) ro[i][j] = ld1(rwo, j < 4 ? vo : vo2, (u32)((28 * hh + j) * D_ + 56 * c8 + 32 * nh) * 2);
    }
  }
  const int mtB = w & 3, nhB = w >> 2;
  const v4u xres = ld4(rx, (nhB == 1 && g == 3) ? OOB : (u32)(l15 * D_ + 8 * g) * 2, (u32)((64 * g32 + 16 * mtB) * D_ + 56 * c8 + 32 * nhB) * 2);

  if (w < 7 && slot < 28) {
    float sc = cval / (1.f + __expf(-cval));
    sc = lo2f(pk(sc, 0.f));
    float p[12];
    p[0] = lo2f(wA.x) * sc; p[1] = hi2f(wA.x) * sc; p[2] = lo2f(wA.y) * sc; p[3] = hi2f(wA.y) * sc;
    p[4] = lo2f(wB.x) * sc; p[5] = hi2f(wB.x) * sc; p[6] = lo2f(wB.y) * sc; p[7] = hi2f(wB.y) * sc;
    p[8] = lo2f(wB.z) * sc; p[9] = hi2f(wB.z) * sc; p[10] = lo2f(wB.w) * sc; p[11] = hi2f(wB.w) * sc;
#pragma unroll
    for (int j = 0; j < 12; ++j) p[j] = rowsum16(p[j]);
    if (l15 == 0) {
      const int o = L_RED + (w * 4 + g) * 48;
      LDSF4(o) = (f4){p[0], p[1], p[2], p[3]};
      LDSF4(o + 16) = (f4){p[4], p[5], p[6], p[7]};
      LDSF4(o + 32) = (f4){p[8], p[9], p[10], p[11]};
    }
  }
  __syncthreads();
  const u64 tag = (epoch + 1) & 0xFFFFFFFFFFFFull;
  if (slot < 28) {
    if (tid < 12) {
      float sum = 0.f;
#pragma unroll
      for (int i = 0; i < 28; ++i) sum += LDSF(L_RED + i * 48 + tid * 4);
      const int col = tid < 4 ? 112 * xcd + 4 * slot + tid : 896 + 224 * xcd + 8 * slot + (tid - 4);
      __hip_atomic_store((gu64*)(ws + WS_GRAN) + col, (tag << 16) | (u64)(pk(sum, 0.f) & 0xffffu), RLX_AGENT);
    }
  } else if (tid == 0) {
    __hip_atomic_store((gu64*)(ws + WS_GRAN) + 2688 + (slot - 28) * 8 + xcd, tag << 16, RLX_AGENT);
  }
  STAMPT(1);

#pragma unroll
  for (int i = 0; i < 6; ++i) {
    const int u = w + 8 * i;
    if (u < 42) {
      v4u e = {plo(rq[i][0], rq[i][1]), plo(rq[i][2], rq[i][3]), plo(rq[i][4], rq[i][5]), plo(rq[i][6], rq[i][7])};
      v4u o = {phi(rq[i][0], rq[i][1]), phi(rq[i][2], rq[i][3]), phi(rq[i][4], rq[i][5]), phi(rq[i][6], rq[i][7])};
      LDS4(L_WQ + u * 2048 + lane * 16) = e;
      LDS4(L_WQ + u * 2048 + 1024 + lane * 16) = o;
    }
  }
#pragma unroll
  for (int i = 0; i < 4; ++i) {
    const int u = w + 8 * i;
    v4u e = {plo(ro[i][0], ro[i][1]), plo(ro[i][2], ro[i][3]), plo(ro[i][4], ro[i][5]), plo(ro[i][6], ro[i][7])};
    v4u o = {phi(ro[i][0], ro[i][1]), phi(ro[i][2], ro[i][3]), phi(ro[i][4], ro[i][5]), phi(ro[i][6], ro[i][7])};
    LDS4(L_WO + u * 2048 + lane * 16) = e;
    LDS4(L_WO + u * 2048 + 1024 + lane * 16) = o;
  }
  u32 xh[14][8];
  {
    float sum = 0.f;
#pragma unroll
    for (int s = 0; s < 14; ++s)
#pragma unroll
      for (int j = 0; j < 4; ++j) sum += lo2f(xr[s][j]) + hi2f(xr[s][j]);
    sum += __shfl_xor(sum, 16);
    sum += __shfl_xor(sum, 32);
    const float mean = sum * (1.f / D_);
    float sq = 0.f;
#pragma unroll
    for (int s = 0; s < 14; ++s)
#pragma unroll
      for (int j = 0; j < 4; ++j) { float a = lo2f(xr[s][j]) - mean, c = hi2f(xr[s][j]) - mean; sq += a * a + c * c; }
    sq += __shfl_xor(sq, 16);
    sq += __shfl_xor(sq, 32);
    const float rstd = rsqrtf(sq * (1.f / D_) + 1e-5f);
#pragma unroll
    for (int s = 0; s < 14; ++s)
#pragma unroll
      for (int j = 0; j < 4; ++j) {
        xh[s][2 * j] = pk((lo2f(xr[s][j]) - mean) * rstd, 1.f);
        xh[s][2 * j + 1] = pk((hi2f(xr[s][j]) - mean) * rstd, 1.f);
        PIN(xh[s][2 * j]); PIN(xh[s][2 * j + 1]);
      }
  }
  STAMPT(2);
  {
    gu64* gr = (gu64*)(ws + WS_GRAN);
    const int i1 = tid < 384 ? 512 + tid : 2688 + (tid - 384);
    const bool has1 = tid < 416;
    u64 v0, v1;
    for (unsigned spins = 0;; ++spins) {
      v0 = __hip_atomic_load(gr + tid, RLX_AGENT);
      v1 = has1 ? __hip_atomic_load(gr + i1, RLX_AGENT) : (tag << 16);
      const bool ok = (v0 >> 16) == tag && (v1 >> 16) == tag;
      if (__all(ok) || spins > SPIN_CAP) break;
      __builtin_amdgcn_s_sleep(1);
    }
    if (tid < 448) LDSH(L_AS1 + tid * 4 + 2) = (u16)v0;
    else LDSH(L_AS1 + (tid - 448) * 4) = (u16)pk(1.f + lo2f((u32)v0), 0.f);
    if (tid < 384) LDSH(L_AS1 + (64 + tid) * 4) = (u16)pk(1.f + lo2f((u32)v1), 0.f);
  }
  __syncthreads();
  STAMPT(3);
  if (b == 0 && tid == 0) __hip_atomic_store((gu64*)ws, epoch + 1, RLX_AGENT);
  {
#pragma unroll
    for (int i = 0; i < 4; ++i) {
      const int rt = 4 * g32 + i;
      if (w < 7) st4s(rws, lane * 16, WS_HID + rt * 57344 + c8 * 7168 + w * 1024, SENT);
      else {
        st4s(rws, lane * 16, WS_X1 + rt * 14336 + c8 * 1792, SENT);
        st4s(rws, lane < 48 ? lane * 16 : OOB, WS_X1 + rt * 14336 + c8 * 1792 + 1024, SENT);
      }
    }
  }
  u32 warm0, warm1;
  {
    warm0 = ld1(rw1, tid < 392 ? tid * 128 : OOB, slot * 50176);
    warm1 = ld1(rw2, tid < 392 ? tid * 128 : OOB, slot * 50176);
  }

  f4 aq[6];
#pragma unroll
  for (int t = 0; t < 6; ++t) aq[t] = (f4){0, 0, 0, 0};
#pragma unroll
  for (int s = 0; s < 14; ++s) {
    const v4u as0 = LDS4(L_AS1 + (32 * s + 8 * g) * 4), as1 = LDS4(L_AS1 + (32 * s + 8 * g) * 4 + 16);
    v4u hf;
    hf.x = pk(dot2(xh[s][0], as0.x, 0.f), dot2(xh[s][1], as0.y, 0.f));
    hf.y = pk(dot2(xh[s][2], as0.z, 0.f), dot2(xh[s][3], as0.w, 0.f));
    hf.z = pk(dot2(xh[s][4], as1.x, 0.f), dot2(xh[s][5], as1.y, 0.f));
    hf.w = pk(dot2(xh[s][6], as1.z, 0.f), dot2(xh[s][7], as1.w, 0.f));
#pragma unroll
    for (int t = 0; t < 4; ++t) aq[t] = mfma(LDS4(L_WQ + ((t >> 1) * 14 + s) * 2048 + (t & 1) * 1024 + lane * 16), hf, aq[t]);
#pragma unroll
    for (int t = 4; t < 6; ++t) aq[t] = mfma(hf, LDS4(L_WQ + (28 + s) * 2048 + (t & 1) * 1024 + lane * 16), aq[t]);
  }
  asm volatile("" ::"v"(warm0), "v"(warm1));
  DRAIN();
  __syncthreads();
  STAMPT(4);
  const v4u qf = {pk(aq[0][0], aq[1][0]), pk(aq[0][1], aq[1][1]), pk(aq[0][2], aq[1][2]), pk(aq[0][3], aq[1][3])};
  {
    const v4u kf = nosent4((v4u){pk(aq[2][0], aq[3][0]), pk(aq[2][1], aq[3][1]), pk(aq[2][2], aq[3][2]), pk(aq[2][3], aq[3][3])});
    const int tau = 16 * (w & 1) + l15;
    const u32 koff = (w >> 1) * 2048 + ((tau >> 2) & 1) * 1024 + (g * 16 + 4 * (tau >> 3) + (tau & 3)) * 16;
    v2u ve = {pk(aq[4][0], aq[4][1]), pk(aq[4][2], aq[4][3])}, vo_ = {pk(aq[5][0], aq[5][1]), pk(aq[5][2], aq[5][3])};
    if (l15 == 14) ve = (v2u){0x3F803F80u, 0x3F803F80u};
    ve.x = nosent(ve.x); ve.y = nosent(ve.y); vo_.x = nosent(vo_.x); vo_.y = nosent(vo_.y);
    const u32 voff = (w >> 1) * 2048 + ((2 * (w & 1) + (g >> 1)) * 16 + l15) * 16 + (g & 1) * 8;
    if (g16 < 15) {
      const u32 base = WS_KV + (g16 * 16 + head) * 16384;
      st4s(rws, koff, base, kf);
      st2s(rws, voff, base + 8192, ve);
      st2s(rws, voff, base + 8192 + 1024, vo_);
    }
    LDS4(L_K + 16384 + koff) = kf;
    LDS2(L_V + 16384 + voff) = ve;
    LDS2(L_V + 16384 + 1024 + voff) = vo_;
  }
  {
    gu64* gr = (gu64*)(ws + WS_GRAN);
    const int j = tid - 448;
    const int i0 = tid < 448 ? 1344 + tid : 896 + 56 * c8 + (j < 56 ? j : 0);
    const int i1 = tid < 448 ? 1792 + tid : 2240 + 56 * c8 + (j < 56 ? j : 0);
    u64 v0, v1;
    for (unsigned spins = 0;; ++spins) {
      v0 = __hip_atomic_load(gr + i0, RLX_AGENT);
      v1 = __hip_atomic_load(gr + i1, RLX_AGENT);
      const bool ok = (v0 >> 16) == tag && (v1 >> 16) == tag;
      if (__all(ok) || spins > SPIN_CAP) break;
      __builtin_amdgcn_s_sleep(1);
    }
    if (tid < 448) {
      LDSH(L_SB + tid * 2) = (u16)v0;
      LDSF(L_A2 + tid * 4) = lo2f(pk(1.f + lo2f((u32)v1), 0.f));
    } else {
      LDSF(L_GA + j * 4) = lo2f((u32)v0);
      LDSF(L_GB + j * 4) = lo2f((u32)v1);
    }
  }
  {
#pragma unroll
    for (int hb = 0; hb < 2; ++hb) {
      const int gs = g16 - 2 + hb;
      v4u a = {0, 0, 0, 0}, c = {0, 0, 0, 0};
      if (gs >= 0) {
        const u32 base = WS_KV + (gs * 16 + head) * 16384;
        for (unsigned spins = 0;; ++spins) {
          CBAR();
          a = ld4s(rws, tid * 16, base);
          c = ld4s(rws, tid * 16, base + 8192);
          const bool bad = chk(chk(0u, a), c) == ~0u;
          if (!__any(bad) || spins > SPIN_CAP) break;
          __builtin_amdgcn_s_sleep(1);
        }
      }
      LDS4(L_K + hb * 8192 + tid * 16) = a;
      LDS4(L_V + hb * 8192 + tid * 16) = c;
    }
  }
  __syncthreads();
  STAMPT(5);

  u32 w1f[14][8];
  v4u w1e[14], w1o[14];
  const u32 vo1 = w < 7 ? (u32)(8 * g * DFF_ + 2 * l15) * 2 : OOB;
#pragma unroll
  for (int s = 0; s < 7; ++s)
#pragma unroll
    for (int j = 0; j < 8; ++j) w1f[s][j] = ld1(rw1, vo1, (u32)((32 * s + j) * DFF_ + 224 * c8 + 32 * (w % 7)) * 2);

  {
    const int blk0 = w >> 1;
    f4 sc[18];
#pragma unroll
    for (int t = 0; t < 18; ++t) sc[t] = mfma(LDS4(L_K + (blk0 + (t >> 1)) * 2048 + (t & 1) * 1024 + lane * 16), qf, (f4){0, 0, 0, 0});
    const int ql = 256 + 16 * w + l15;
    const int kmin = (256 - 128 * g16) > (ql - 255) ? (256 - 128 * g16) : (ql - 255);
    const float NEG = -1e30f;
    if (g16 < 2) {
#pragma unroll
      for (int t = 2; t < 16; ++t)
#pragma unroll
        for (int r = 0; r < 4; ++r) { const int kl = 32 * (blk0 + (t >> 1)) + 8 * g + 4 * (t & 1) + r; sc[t][r] = kl >= kmin ? sc[t][r] : NEG; }
    }
#pragma unroll
    for (int t = 0; t < 2; ++t)
#pragma unroll
      for (int r = 0; r < 4; ++r) { const int kl = 32 * blk0 + 8 * g + 4 * t + r; sc[t][r] = kl >= kmin ? sc[t][r] : NEG; }
#pragma unroll
    for (int t = 16; t < 18; ++t)
#pragma unroll
      for (int r = 0; r < 4; ++r) { const int kl = 32 * (blk0 + 8) + 8 * g + 4 * (t & 1) + r; sc[t][r] = (kl <= ql && kl >= kmin) ? sc[t][r] : NEG; }
    float mx = NEG;
#pragma unroll
    for (int t = 0; t < 18; ++t) mx = fmaxf(fmaxf(mx, fmaxf(sc[t][0], sc[t][1])), fmaxf(sc[t][2], sc[t][3]));
    mx = fmaxf(mx, __shfl_xor(mx, 16));
    mx = fmaxf(mx, __shfl_xor(mx, 32));
    const float cs = 0.18898223650461362f * 1.4426950408889634f;
    const float mc = mx * cs;
    f4 oe = {0, 0, 0, 0}, oo = {0, 0, 0, 0};
#pragma unroll
    for (int bb = 0; bb < 9; ++bb) {
      float p[8];
#pragma unroll
      for (int r = 0; r < 4; ++r) {
        p[r] = __builtin_amdgcn_exp2f(sc[2 * bb][r] * cs - mc);
        p[4 + r] = __builtin_amdgcn_exp2f(sc[2 * bb + 1][r] * cs - mc);
      }
      const v4u pf = {pk(p[0], p[1]), pk(p[2], p[3]), pk(p[4], p[5]), pk(p[6], p[7])};
      oe = mfma(LDS4(L_V + (blk0 + bb) * 2048 + lane * 16), pf, oe);
      oo = mfma(LDS4(L_V + (blk0 + bb) * 2048 + 1024 + lane * 16), pf, oo);
    }
    const float den = __shfl(oe[2], 48 + l15);
    const float inv = 1.f / den;
    const v4u of = nosent4((v4u){pk(oe[0] * inv, oo[0] * inv), pk(oe[1] * inv, oo[1] * inv), pk(oe[2] * inv, oo[2] * inv), pk(oe[3] * inv, oo[3] * inv)});
    st4s(rws, lane * 16, WS_O + ((8 * g16 + w) * 16 + head) * 1024, of);
  }
  STAMPT(6);
  f4 au[2] = {{0, 0, 0, 0}, {0, 0, 0, 0}}, av[2] = {{0, 0, 0, 0}, {0, 0, 0, 0}};
  const v4u ONES = {0x3F803F80u, 0x3F803F80u, 0x3F803F80u, 0x3F803F80u};
#define PROC_W1(s)                                                                                                              \
  {                                                                                                                             \
    const v4u e = {plo(w1f[s][0], w1f[s][1]), plo(w1f[s][2], w1f[s][3]), plo(w1f[s][4], w1f[s][5]), plo(w1f[s][6], w1f[s][7])}; \
    const v4u o = {phi(w1f[s][0], w1f[s][1]), phi(w1f[s][2], w1f[s][3]), phi(w1f[s][4], w1f[s][5]), phi(w1f[s][6], w1f[s][7])}; \
    const v4u sbf = LDS4(L_SB + (32 * s + 8 * g) * 2);                                                                          \
    av[0] = mfma(e, sbf, av[0]);                                                                                                \
    av[1] = mfma(o, sbf, av[1]);                                                                                                \
    const f4 a0 = LDSF4(L_A2 + (32 * s + 8 * g) * 4), a1 = LDSF4(L_A2 + (32 * s + 8 * g) * 4 + 16);                             \
    v4u e2, o2;                                                                                                                 \
    e2.x = pk(lo2f(w1f[s][0]) * a0.x, lo2f(w1f[s][1]) * a0.y); o2.x = pk(hi2f(w1f[s][0]) * a0.x, hi2f(w1f[s][1]) * a0.y);       \
    e2.y = pk(lo2f(w1f[s][2]) * a0.z, lo2f(w1f[s][3]) * a0.w); o2.y = pk(hi2f(w1f[s][2]) * a0.z, hi2f(w1f[s][3]) * a0.w);       \
    e2.z = pk(lo2f(w1f[s][4]) * a1.x, lo2f(w1f[s][5]) * a1.y); o2.z = pk(hi2f(w1f[s][4]) * a1.x, hi2f(w1f[s][5]) * a1.y);       \
    e2.w = pk(lo2f(w1f[s][6]) * a1.z, lo2f(w1f[s][7]) * a1.w); o2.w = pk(hi2f(w1f[s][6]) * a1.z, hi2f(w1f[s][7]) * a1.w);       \
    au[0] = mfma(e2, ONES, au[0]);                                                                                              \
    au[1] = mfma(o2, ONES, au[1]);                                                                                              \
    PIN(e2); PIN(o2);                                                                                                           \
    w1e[s] = e2; w1o[s] = o2;                                                                                                   \
  }
#pragma unroll
  for (int s = 0; s < 7; ++s) PROC_W1(s)
  PIN(au[0]); PIN(au[1]); PIN(av[0]); PIN(av[1]);
  __syncthreads();

  {
    v4u of[16];
    const u32 base = WS_O + (4 * g32 + mtB) * 16384;
    for (unsigned spins = 0;; ++spins) {
      CBAR();
      u32 m = 0;
#pragma unroll
      for (int h = 0; h < 16; ++h) of[h] = ld4s(rws, lane * 16, base + h * 1024);
#pragma unroll
      for (int h = 0; h < 16; ++h) m = chk(m, of[h]);
      if (!__any(m == ~0u) || spins > SPIN_CAP) break;
      __builtin_amdgcn_s_sleep(1);
    }
    STAMPT(7);
    f4 e = {0, 0, 0, 0}, o = {0, 0, 0, 0};
#pragma unroll
    for (int h = 0; h < 16; ++h) {
      e = mfma(LDS4(L_WO + (nhB * 16 + h) * 2048 + lane * 16), of[h], e);
      o = mfma(LDS4(L_WO + (nhB * 16 + h) * 2048 + 1024 + lane * 16), of[h], o);
      if ((h & 1) == 1 && h >= 3) {
        __builtin_amdgcn_sched_barrier(0);
        const int s = 7 + ((h - 3) >> 1);
#pragma unroll
        for (int j = 0; j < 8; ++j) w1f[s][j] = ld1(rw1, vo1, (u32)((32 * s + j) * DFF_ + 224 * c8 + 32 * (w % 7)) * 2);
        __builtin_amdgcn_sched_barrier(0);
      }
    }
    const f4 g0 = LDSF4(L_GA + (32 * nhB + 8 * g) * 4), g1 = LDSF4(L_GA + (32 * nhB + 8 * g) * 4 + 16);
    v4u x1;
    x1.x = pk(lo2f(xres.x) + g0.x * e[0], hi2f(xres.x) + g0.y * o[0]);
    x1.y = pk(lo2f(xres.y) + g0.z * e[1], hi2f(xres.y) + g0.w * o[1]);
    x1.z = pk(lo2f(xres.z) + g1.x * e[2], hi2f(xres.z) + g1.y * o[2]);
    x1.w = pk(lo2f(xres.w) + g1.z * e[3], hi2f(xres.w) + g1.w * o[3]);
    x1 = nosent4(x1);
    if (!(nhB == 1 && g == 3)) {
      st4s(rws, lane * 16, WS_X1 + (4 * g32 + mtB) * 14336 + (7 * c8 + 4 * nhB) * 256, x1);
      LDS4(L_X1S + (mtB * 7 + 4 * nhB) * 256 + lane * 16) = x1;
    }
  }
  STAMPT(8);
#pragma unroll
  for (int s = 7; s < 14; ++s) PROC_W1(s)
  PIN(au[0]); PIN(au[1]); PIN(av[0]); PIN(av[1]);
  {
    v4u t[7];
    const u32 base = WS_X1 + 4 * g32 * 14336 + w * 7168;
    for (unsigned spins = 0;; ++spins) {
      CBAR();
      u32 m = 0;
#pragma unroll
      for (int i = 0; i < 7; ++i) t[i] = ld4s(rws, lane * 16, base + i * 1024);
#pragma unroll
      for (int i = 0; i < 7; ++i) m = chk(m, t[i]);
      if (!__any(m == ~0u) || spins > SPIN_CAP) break;
      __builtin_amdgcn_s_sleep(1);
    }
    float s1 = 0.f, s2 = 0.f;
#pragma unroll
    for (int i = 0; i < 7; ++i) {
      LDS4(w * 7168 + i * 1024 + lane * 16) = t[i];
#pragma unroll
      for (int j = 0; j < 4; ++j) { s1 = dot2(t[i][j], 0x3F803F80u, s1); s2 = dot2(t[i][j], t[i][j], s2); }
    }
    s1 += __shfl_xor(s1, 16); s2 += __shfl_xor(s2, 16);
    s1 += __shfl_xor(s1, 32); s2 += __shfl_xor(s2, 32);
    if (g == 0) *(LAS f2*)(lds + L_RED + ((w & 1) * 64 + (w >> 1) * 16 + l15) * 8) = (f2){s1, s2};
  }
  __syncthreads();
  STAMPT(9);
  v2u w2r[7][8];
  const u32 vo2 = l15 < 14 ? (u32)(8 * g * D_ + 4 * l15) * 2 : OOB;
  {
    f4 acc[4][2];
#pragma unroll
    for (int m = 0; m < 4; ++m) { acc[m][0] = (f4){0, 0, 0, 0}; acc[m][1] = (f4){0, 0, 0, 0}; }
#pragma unroll
    for (int s = 0; s < 14; ++s) {
      const v4u e = w1e[s], o = w1o[s];
#pragma unroll
      for (int m = 0; m < 4; ++m) {
        const v4u xf = LDS4(m * 14336 + s * 1024 + lane * 16);
        acc[m][0] = mfma(e, xf, acc[m][0]);
        acc[m][1] = mfma(o, xf, acc[m][1]);
      }
      __builtin_amdgcn_sched_barrier(0);
#pragma unroll
      for (int j = 0; j < 4; ++j) w2r[s >> 1][(s & 1) * 4 + j] = ld2(rw2, vo2, (u32)((224 * w + 32 * (s >> 1) + (s & 1) * 4 + j) * D_ + 56 * c8) * 2);
      __builtin_amdgcn_sched_barrier(0);
    }
#pragma unroll
    for (int m = 0; m < 4; ++m) {
      const f2 st0 = *(LAS f2*)(lds + L_RED + (m * 16 + l15) * 8), st1 = *(LAS f2*)(lds + L_RED + (64 + m * 16 + l15) * 8);
      const float mean = (st0.x + st1.x) * (1.f / D_);
      const float var = fmaxf((st0.y + st1.y) * (1.f / D_) - mean * mean, 0.f);
      const float rstd = rsqrtf(var + 1e-5f);
      v4u hv;
#pragma unroll
      for (int r = 0; r < 4; ++r) {
        const float he = fmaxf(rstd * (acc[m][0][r] - mean * au[0][r]) + av[0][r], 0.f);
        const float ho = fmaxf(rstd * (acc[m][1][r] - mean * au[1][r]) + av[1][r], 0.f);
        hv[r] = nosent(pk(he, ho));
      }
      st4s(rws, w < 7 ? lane * 16 : OOB, WS_HID + (4 * g32 + m) * 57344 + (28 * c8 + 4 * (w % 7)) * 256, hv);
    }
  }
  STAMPT(10);
  __syncthreads();
  {
    v4u wf[7][4];
#pragma unroll
    for (int s = 0; s < 7; ++s) {
      wf[s][0] = (v4u){plo(w2r[s][0].x, w2r[s][1].x), plo(w2r[s][2].x, w2r[s][3].x), plo(w2r[s][4].x, w2r[s][5].x), plo(w2r[s][6].x, w2r[s][7].x)};
      wf[s][1] = (v4u){phi(w2r[s][0].x, w2r[s][1].x), phi(w2r[s][2].x, w2r[s][3].x), phi(w2r[s][4].x, w2r[s][5].x), phi(w2r[s][6].x, w2r[s][7].x)};
      wf[s][2] = (v4u){plo(w2r[s][0].y, w2r[s][1].y), plo(w2r[s][2].y, w2r[s][3].y), plo(w2r[s][4].y, w2r[s][5].y), plo(w2r[s][6].y, w2r[s][7].y)};
      wf[s][3] = (v4u){phi(w2r[s][0].y, w2r[s][1].y), phi(w2r[s][2].y, w2r[s][3].y), phi(w2r[s][4].y, w2r[s][5].y), phi(w2r[s][6].y, w2r[s][7].y)};
      PIN(wf[s][0]); PIN(wf[s][1]); PIN(wf[s][2]); PIN(wf[s][3]);
    }
#pragma unroll
    for (int mp = 0; mp < 2; ++mp) {
      v4u hf[2][7];
      const u32 base = WS_HID + (4 * g32 + 2 * mp) * 57344 + w * 7168;
      for (unsigned spins = 0;; ++spins) {
        CBAR();
        u32 m = 0;
#pragma unroll
        for (int i = 0; i < 2; ++i)
#pragma unroll
          for (int s = 0; s < 7; ++s) hf[i][s] = ld4s(rws, lane * 16, base + i * 57344 + s * 1024);
#pragma unroll
        for (int i = 0; i < 2; ++i)
#pragma unroll
          for (int s = 0; s < 7; ++s) m = chk(m, hf[i][s]);
        if (!__any(m == ~0u) || spins > SPIN_CAP) break;
        __builtin_amdgcn_s_sleep(1);
      }
      if (mp == 0) STAMPT(11);
#pragma unroll
      for (int i = 0; i < 2; ++i) {
        f4 acc[4] = {{0, 0, 0, 0}, {0, 0, 0, 0}, {0, 0, 0, 0}, {0, 0, 0, 0}};
#pragma unroll
        for (int s = 0; s < 7; ++s)
#pragma unroll
          for (int t = 0; t < 4; ++t) acc[t] = mfma(wf[s][t], hf[i][s], acc[t]);
#pragma unroll
        for (int r = 0; r < 4; ++r) LDSF4(((w * 4 + 2 * mp + i) * 4 + r) * 1024 + lane * 16) = (f4){acc[0][r], acc[1][r], acc[2][r], acc[3][r]};
      }
    }
  }
  __syncthreads();
  STAMPT(12);
  {
    const int mt = tid >> 7, rp = (tid >> 6) & 1;
    if (!(g == 3 && rp == 1)) {
      f4 s0 = {0, 0, 0, 0}, s1 = {0, 0, 0, 0};
#pragma unroll
      for (int ww = 0; ww < 8; ++ww) {
        s0 += LDSF4(((ww * 4 + mt) * 4 + 2 * rp) * 1024 + lane * 16);
        s1 += LDSF4(((ww * 4 + mt) * 4 + 2 * rp + 1) * 1024 + lane * 16);
      }
      const v4u x1 = LDS4(L_X1S + (mt * 7 + 2 * g + rp) * 256 + l15 * 16);
      const f4 g0 = LDSF4(L_GB + (16 * g + 8 * rp) * 4), g1 = LDSF4(L_GB + (16 * g + 8 * rp) * 4 + 16);
      v4u o;
      o.x = pk(lo2f(x1.x) + g0.x * s0.x, hi2f(x1.x) + g0.y * s0.y);
      o.y = pk(lo2f(x1.y) + g0.z * s0.z, hi2f(x1.y) + g0.w * s0.w);
      o.z = pk(lo2f(x1.z) + g1.x * s1.x, hi2f(x1.z) + g1.y * s1.y);
      o.w = pk(lo2f(x1.w) + g1.z * s1.z, hi2f(x1.w) + g1.w * s1.w);
      *(v4u*)(out + (size_t)(64 * g32 + 16 * mt + l15) * D_ + 56 * c8 + 16 * g + 8 * rp) = o;
    }
  }
  STAMPT(13);
}

extern "C" void kernel_launch(void* const* d_in, const int* in_sizes, int n_in, void* d_out, int out_size, void* d_ws,
                              size_t ws_size, hipStream_t stream) {
  (void)hipFuncSetAttribute((const void*)mega, hipFuncAttributeMaxDynamicSharedMemorySize, LDS_BYTES);
  mega<<<256, 512, LDS_BYTES, stream>>>((const u16*)d_in[0], (const u16*)d_in[1], (const u16*)d_in[2], (const u16*)d_in[4],
                                        (const u16*)d_in[5], (const u16*)d_in[6], (const u16*)d_in[7], (u16*)d_out, (char*)d_ws);
}
```

```cpp
#include <hip/hip_runtime.h>
#include <stdint.h>

typedef unsigned int u32;
typedef unsigned long long u64;
typedef unsigned short u16;
typedef u32 v4u __attribute__((ext_vector_type(4)));
typedef u32 v2u __attribute__((ext_vector_type(2)));
typedef float f4 __attribute__((ext_vector_type(4)));
typedef float f2 __attribute__((ext_vector_type(2)));
typedef __bf16 bf2v __attribute__((ext_vector_type(2)));
typedef __bf16 bf8v __attribute__((ext_vector_type(8)));
#define GAS __attribute__((address_space(1)))
#define LAS __attribute__((address_space(3)))
typedef GAS u64 gu64;
#define RLX_AGENT __ATOMIC_RELAXED, __HIP_MEMORY_SCOPE_AGENT
typedef __amdgpu_buffer_rsrc_t rsrc_t;

constexpr int S_ = 2048, D_ = 448, DFF_ = 1792;
constexpr int WS_GRAN = 4096;
constexpr int G_PREP = 2688;
constexpr int WS_KV = 65536;
constexpr int WS_O = WS_KV + 4194304;
constexpr int WS_X1 = WS_O + 2097152;
constexpr int WS_ST = WS_X1 + 1835008;
constexpr int WS_HID = WS_ST + 262144;
constexpr int WS_W2T = WS_HID + 7340032;
constexpr int WS_W1T = WS_W2T + 1835008;
constexpr int WS_WOT = WS_W1T + 1605632;
constexpr int WS_END = WS_WOT + 524288;
constexpr int WS_DBG = WS_END;
constexpr int L_WQ = 0;
constexpr int L_K = 0, L_V = 24576;
constexpr int L_PR = 49152;
constexpr int L_WO = 86016;
constexpr int L_SM = 151552;
constexpr int L_AS1 = L_SM;
constexpr int L_A2 = L_AS1 + 1792;
constexpr int L_SB = L_A2 + 896;
constexpr int L_GA = L_SB + 896;
constexpr int L_GB = L_GA + 256;
constexpr int L_MR = L_GB + 256;
constexpr int L_RED = L_MR + 512;
constexpr int L_X1S = L_RED;
constexpr int L_END = L_X1S + 7168;
static_assert(L_END <= 163840, "lds");
constexpr int LDS_BYTES = 163840;
constexpr u32 OOB = 0x80000000u;
constexpr unsigned SPIN_CAP = 1u << 15;

__device__ __forceinline__ u32 pk(float lo, float hi) { f2 v = {lo, hi}; return __builtin_bit_cast(u32, __builtin_convertvector(v, bf2v)); }
__device__ __forceinline__ float dot2(u32 a, u32 b, float c) { return __builtin_amdgcn_fdot2_f32_bf16(__builtin_bit_cast(bf2v, a), __builtin_bit_cast(bf2v, b), c, false); }
__device__ __forceinline__ f4 mfma(v4u a, v4u b, f4 c) { return __builtin_amdgcn_mfma_f32_16x16x32_bf16(__builtin_bit_cast(bf8v, a), __builtin_bit_cast(bf8v, b), c, 0, 0, 0); }
__device__ __forceinline__ float lo2f(u32 v) { return __builtin_bit_cast(float, v << 16); }
__device__ __forceinline__ float hi2f(u32 v) { return __builtin_bit_cast(float, v & 0xffff0000u); }
__device__ __forceinline__ u32 plo(u32 r0, u32 r1) { return __builtin_amdgcn_perm(r1, r0, 0x05040100u); }
__device__ __forceinline__ u32 phi(u32 r0, u32 r1) { return __builtin_amdgcn_perm(r1, r0, 0x07060302u); }
__device__ __forceinline__ u32 nosent(u32 v) { return v < 0xFFFFFFFEu ? v : 0xFFFFFFFEu; }
__device__ __forceinline__ v4u nosent4(v4u v) { v.x = nosent(v.x); v.y = nosent(v.y); v.z = nosent(v.z); v.w = nosent(v.w); return v; }
__device__ __forceinline__ u32 umax3(u32 a, u32 b, u32 c) { u32 m = a > b ? a : b; return m > c ? m : c; }
__device__ __forceinline__ u32 chk(u32 m, v4u v) { return umax3(umax3(m, v.x, v.y), v.z, v.w); }
template <int C> __device__ __forceinline__ float dppf(float v) { return __builtin_bit_cast(float, __builtin_amdgcn_update_dpp(0, __builtin_bit_cast(int, v), C, 0xf, 0xf, false)); }
__device__ __forceinline__ float rowsum16(float v) { v += dppf<0xB1>(v); v += dppf<0x4E>(v); v += dppf<0x141>(v); v += dppf<0x140>(v); return v; }
__device__ __forceinline__ v4u ld4(rsrc_t r, u32 vo, u32 so) { return __builtin_amdgcn_raw_buffer_load_b128(r, vo, so, 0); }
__device__ __forceinline__ v4u ld4s(rsrc_t r, u32 vo, u32 so) { return __builtin_amdgcn_raw_buffer_load_b128(r, vo, so, 16); }
__device__ __forceinline__ v2u ld2(rsrc_t r, u32 vo, u32 so) { return __builtin_amdgcn_raw_buffer_load_b64(r, vo, so, 0); }
__device__ __forceinline__ u32 ld1(rsrc_t r, u32 vo, u32 so) { return __builtin_amdgcn_raw_buffer_load_b32(r, vo, so, 0); }
__device__ __forceinline__ void st4s(rsrc_t r, u32 vo, u32 so, v4u v) { __builtin_amdgcn_raw_buffer_store_b128(v, r, vo + so, 0, 16); }
__device__ __forceinline__ void st2s(rsrc_t r, u32 vo, u32 so, v2u v) { __builtin_amdgcn_raw_buffer_store_b64(v, r, vo, so, 16); }
__device__ __forceinline__ v4u modulate(const u32 (&xh)[8], v4u a0, v4u a1) {
  v4u h;
  float d0, d1, d2, d3, d4, d5, d6, d7;
  asm volatile(
      "v_dot2_f32_bf16 %4, %12, %20, 0\n\tv_dot2_f32_bf16 %5, %13, %21, 0\n\tv_dot2_f32_bf16 %6, %14, %22, 0\n\tv_dot2_f32_bf16 %7, %15, %23, 0\n\t"
      "v_dot2_f32_bf16 %8, %16, %24, 0\n\tv_dot2_f32_bf16 %9, %17, %25, 0\n\tv_dot2_f32_bf16 %10, %18, %26, 0\n\tv_dot2_f32_bf16 %11, %19, %27, 0\n\t"
      "v_cvt_pk_bf16_f32 %0, %4, %5\n\tv_cvt_pk_bf16_f32 %1, %6, %7\n\tv_cvt_pk_bf16_f32 %2, %8, %9\n\tv_cvt_pk_bf16_f32 %3, %10, %11\n\ts_nop 1"
      : "=&v"(h.x), "=&v"(h.y), "=&v"(h.z), "=&v"(h.w), "=&v"(d0), "=&v"(d1), "=&v"(d2), "=&v"(d3), "=&v"(d4), "=&v"(d5), "=&v"(d6), "=&v"(d7)
      : "v"(xh[0]), "v"(xh[1]), "v"(xh[2]), "v"(xh[3]), "v"(xh[4]), "v"(xh[5]), "v"(xh[6]), "v"(xh[7]),
        "v"(a0.x), "v"(a0.y), "v"(a0.z), "v"(a0.w), "v"(a1.x), "v"(a1.y), "v"(a1.z), "v"(a1.w));
  return h;
}
__device__ __forceinline__ v4u qstep(f4 (&aq)[6], const v4u (&fb)[8], v4u hf, const u32 (&xh)[8], v4u a0, v4u a1) {
  v4u h;
  float d0, d1, d2, d3, d4, d5, d6, d7;
  asm volatile(
      "v_mfma_f32_16x16x32_bf16 %12, %18, %24, %12\n\tv_dot2_f32_bf16 %4, %25, %33, 0\n\tv_dot2_f32_bf16 %5, %26, %34, 0\n\t"
      "v_mfma_f32_16x16x32_bf16 %13, %19, %24, %13\n\tv_dot2_f32_bf16 %6, %27, %35, 0\n\tv_dot2_f32_bf16 %7, %28, %36, 0\n\t"
      "v_mfma_f32_16x16x32_bf16 %14, %20, %24, %14\n\tv_dot2_f32_bf16 %8, %29, %37, 0\n\tv_dot2_f32_bf16 %9, %30, %38, 0\n\t"
      "v_mfma_f32_16x16x32_bf16 %15, %21, %24, %15\n\tv_dot2_f32_bf16 %10, %31, %39, 0\n\tv_dot2_f32_bf16 %11, %32, %40, 0\n\t"
      "v_mfma_f32_16x16x32_bf16 %16, %24, %22, %16\n\tv_cvt_pk_bf16_f32 %0, %4, %5\n\tv_cvt_pk_bf16_f32 %1, %6, %7\n\t"
      "v_mfma_f32_16x16x32_bf16 %17, %24, %23, %17\n\tv_cvt_pk_bf16_f32 %2, %8, %9\n\tv_cvt_pk_bf16_f32 %3, %10, %11\n\ts_nop 1"
      : "=&v"(h.x), "=&v"(h.y), "=&v"(h.z), "=&v"(h.w), "=&v"(d0), "=&v"(d1), "=&v"(d2), "=&v"(d3), "=&v"(d4), "=&v"(d5), "=&v"(d6), "=&v"(d7),
        "+v"(aq[0]), "+v"(aq[1]), "+v"(aq[2]), "+v"(aq[3]), "+v"(aq[4]), "+v"(aq[5])
      : "v"(fb[2]), "v"(fb[3]), "v"(fb[4]), "v"(fb[5]), "v"(fb[6]), "v"(fb[7]), "v"(hf),
        "v"(xh[0]), "v"(xh[1]), "v"(xh[2]), "v"(xh[3]), "v"(xh[4]), "v"(xh[5]), "v"(xh[6]), "v"(xh[7]),
        "v"(a0.x), "v"(a0.y), "v"(a0.z), "v"(a0.w), "v"(a1.x), "v"(a1.y), "v"(a1.z), "v"(a1.w));
  return h;
}
typedef short s4v __attribute__((ext_vector_type(4)));
__device__ __forceinline__ v2u trrd(LAS char* p) { return __builtin_bit_cast(v2u, __builtin_amdgcn_ds_read_tr16_b64_v4i16((LAS s4v*)p)); }
__device__ __forceinline__ v4u mksrd(const void* p, u32 bytes) {
  const u64 a = (u64)p;
  return (v4u){(u32)__builtin_amdgcn_readfirstlane((u32)a), (u32)__builtin_amdgcn_readfirstlane((u32)(a >> 32)) & 0xffffu, bytes, 0x00020000u};
}
template <bool SC1> __device__ __forceinline__ void dma16(v4u srd, u32 vo, u32 so, u32 ldsaddr) {
  u32 keep;
  so = __builtin_amdgcn_readfirstlane(so);
  ldsaddr = __builtin_amdgcn_readfirstlane(ldsaddr);
  if (SC1)
    asm volatile("s_mov_b32 %0, m0\n\ts_mov_b32 m0, %4\n\ts_nop 3\n\tbuffer_load_dwordx4 %1, %2, %3 offen sc1 lds\n\ts_mov_b32 m0, %0"
                 : "=&s"(keep) : "v"(vo), "s"(srd), "s"(so), "s"(ldsaddr) : "memory");
  else
    asm volatile("s_mov_b32 %0, m0\n\ts_mov_b32 m0, %4\n\ts_nop 3\n\tbuffer_load_dwordx4 %1, %2, %3 offen lds\n\ts_mov_b32 m0, %0"
                 : "=&s"(keep) : "v"(vo), "s"(srd), "s"(so), "s"(ldsaddr) : "memory");
}
#define LDS4(off) (*(LAS v4u*)(lds + (off)))
#define LDS2(off) (*(LAS v2u*)(lds + (off)))
#define LDSF4(off) (*(LAS f4*)(lds + (off)))
#define LDSF(off) (*(LAS float*)(lds + (off)))
#define LDSH(off) (*(LAS u16*)(lds + (off)))
#define CBAR() asm volatile("" ::: "memory")
#define PIN(v) asm volatile("" : "+v"(v))
#define SB0() __builtin_amdgcn_sched_barrier(0)
#define BACKOFF() do { __builtin_amdgcn_s_setprio(0); __builtin_amdgcn_s_sleep(2); } while (0)
#define RUNPRIO() __builtin_amdgcn_s_setprio(1)
#define DRAIN() asm volatile("s_waitcnt vmcnt(0)" ::: "memory")
#define STAMPT(i)

__global__ __launch_bounds__(512, 2) void mega(const u16* __restrict__ x, const u16* __restrict__ wqkv, const u16* __restrict__ wo,
                                               const u16* __restrict__ w1, const u16* __restrict__ w2, const u16* __restrict__ cvec,
                                               const u16* __restrict__ wmod, u16* __restrict__ out, char* ws) {
  extern __shared__ __attribute__((aligned(16))) char lds_g[];
  LAS char* lds = (LAS char*)lds_g;
  const int tid = threadIdx.x, lane = tid & 63, w = __builtin_amdgcn_readfirstlane(tid >> 6) & 7;
  const int l15 = lane & 15, g = lane >> 4;
  const int b = blockIdx.x, xcd = b & 7, slot = b >> 3;
  const int g16 = xcd * 2 + (slot >> 4), head = slot & 15;
  const int g32 = xcd * 4 + (slot >> 3), c8 = slot & 7;
  const rsrc_t rws = __builtin_amdgcn_make_buffer_rsrc(ws, 0, WS_END, 0x00020000);
  const rsrc_t rx = __builtin_amdgcn_make_buffer_rsrc((void*)x, 0, S_ * D_ * 2, 0x00020000);
  const rsrc_t rwq = __builtin_amdgcn_make_buffer_rsrc((void*)wqkv, 0, D_ * 3 * D_ * 2, 0x00020000);
  const rsrc_t rwm = __builtin_amdgcn_make_buffer_rsrc((void*)wmod, 0, D_ * 6 * D_ * 2, 0x00020000);
  const rsrc_t rc = __builtin_amdgcn_make_buffer_rsrc((void*)cvec, 0, D_ * 2, 0x00020000);
  const v4u SENT = {~0u, ~0u, ~0u, ~0u};
  STAMPT(0);
  RUNPRIO();
  u32 cpf;
  {
    u64 pc;
    u32 len;
    asm volatile("s_getpc_b64 %0\n.Lmega_start:\n\ts_mov_b32 %1, .Lmega_end-.Lmega_start" : "=s"(pc), "=s"(len));
    const rsrc_t rcode = __builtin_amdgcn_make_buffer_rsrc((void*)pc, 0, len, 0x00020000);
    cpf = ld1(rcode, tid < 40 ? (tid * 8 + (slot & 7)) * 128 : OOB, 0);
  }
  u32 mpf;
  {
    const int idx = slot * 84 + tid - 64, n = idx / 448, row = idx - 448 * n, j = xcd + 8 * n;
    mpf = ld1(rwm, (tid >= 64 && tid < 148 && j < 42) ? (u32)(row * 5376 + j * 128) : OOB, 0);
  }
  const u64 epoch = __hip_atomic_load((gu64*)ws, RLX_AGENT);
  gu64* gr = (gu64*)(ws + WS_GRAN);
  const int mj = xcd + 8 * (slot / 3);
  const bool modw = (slot % 3 == 0) && mj < 42 && w < 4;
  if (tid == 0) *(volatile LAS u32*)(lds + L_RED + 3072) = 0u;
  v2u mr[4][8];
  u32 craw;
  if (modw) {
    craw = ld1(rc, w + 4 * g < 14 ? (32 * (w + 4 * g) + 2 * l15) * 2 : OOB, 0);
#pragma unroll
    for (int i = 0; i < 4; ++i) {
      const u32 vo = (i < 3 || w < 2) ? (u32)(8 * g * 2688 + 4 * l15) * 2 : OOB;
#pragma unroll
      for (int j = 0; j < 8; ++j) mr[i][j] = ld2(rwm, vo, (u32)((32 * ((w + 4 * i) % 14) + j) * 2688 + 64 * mj) * 2);
    }
  }
  asm volatile("s_waitcnt lgkmcnt(0)\n\ts_barrier" ::: "memory");
  const u64 tag = (epoch + 1) & 0xFFFFFFFFFFFFull;
  if (modw) {
    float c0 = lo2f(craw), c1 = hi2f(craw);
    c0 = c0 * __builtin_amdgcn_rcpf(1.f + __expf(-c0));
    c1 = c1 * __builtin_amdgcn_rcpf(1.f + __expf(-c1));
    *(LAS u32*)(lds + L_RED + w * 256 + lane * 4) = pk(c0, c1);
    f4 acc[4] = {{0, 0, 0, 0}, {0, 0, 0, 0}, {0, 0, 0, 0}, {0, 0, 0, 0}};
#pragma unroll
    for (int i = 0; i < 4; ++i) {
      const v4u sf = LDS4(L_RED + w * 256 + i * 64 + g * 16);
      acc[0] = mfma((v4u){plo(mr[i][0].x, mr[i][1].x), plo(mr[i][2].x, mr[i][3].x), plo(mr[i][4].x, mr[i][5].x), plo(mr[i][6].x, mr[i][7].x)}, sf, acc[0]);
      acc[1] = mfma((v4u){phi(mr[i][0].x, mr[i][1].x), phi(mr[i][2].x, mr[i][3].x), phi(mr[i][4].x, mr[i][5].x), phi(mr[i][6].x, mr[i][7].x)}, sf, acc[1]);
      acc[2] = mfma((v4u){plo(mr[i][0].y, mr[i][1].y), plo(mr[i][2].y, mr[i][3].y), plo(mr[i][4].y, mr[i][5].y), plo(mr[i][6].y, mr[i][7].y)}, sf, acc[2]);
      acc[3] = mfma((v4u){phi(mr[i][0].y, mr[i][1].y), phi(mr[i][2].y, mr[i][3].y), phi(mr[i][4].y, mr[i][5].y), phi(mr[i][6].y, mr[i][7].y)}, sf, acc[3]);
    }
    if (l15 == 0) {
#pragma unroll
      for (int r = 0; r < 4; ++r) LDSF4(L_RED + 1024 + w * 256 + (16 * g + 4 * r) * 4) = (f4){acc[0][r], acc[1][r], acc[2][r], acc[3][r]};
    }
    asm volatile("s_waitcnt lgkmcnt(0)" ::: "memory");
    u32 old = 0;
    if (lane == 0) old = __hip_atomic_fetch_add((LAS u32*)(lds + L_RED + 3072), 1u, __ATOMIC_RELAXED, __HIP_MEMORY_SCOPE_WORKGROUP);
    if (__builtin_amdgcn_readfirstlane(old) == 3u) {
      CBAR();
      const float sum = (LDSF(L_RED + 1024 + lane * 4) + LDSF(L_RED + 1280 + lane * 4)) + (LDSF(L_RED + 1536 + lane * 4) + LDSF(L_RED + 1792 + lane * 4));
      __hip_atomic_store(gr + 64 * mj + lane, (tag << 16) | (u64)(pk(sum, 0.f) & 0xffffu), RLX_AGENT);
    }
  }
  STAMPT(14);
  STAMPT(15);
  v4u xr[14];
  {
    const v4u sx = mksrd(x, S_ * D_ * 2);
    const u32 vo = (u32)((lane >> 3) * D_ * 2 + (lane & 7) * 16);
    const u32 so = (u32)((128 * g16 + 16 * w) * D_ * 2);
    asm volatile(
        "s_nop 4\n\t"
        "buffer_load_dwordx4 %0, %14, %15, %16 offen\n\tbuffer_load_dwordx4 %1, %14, %15, %17 offen\n\t"
        "buffer_load_dwordx4 %2, %14, %15, %16 offen offset:128\n\tbuffer_load_dwordx4 %3, %14, %15, %17 offen offset:128\n\t"
        "buffer_load_dwordx4 %4, %14, %15, %16 offen offset:256\n\tbuffer_load_dwordx4 %5, %14, %15, %17 offen offset:256\n\t"
        "buffer_load_dwordx4 %6, %14, %15, %16 offen offset:384\n\tbuffer_load_dwordx4 %7, %14, %15, %17 offen offset:384\n\t"
        "buffer_load_dwordx4 %8, %14, %15, %16 offen offset:512\n\tbuffer_load_dwordx4 %9, %14, %15, %17 offen offset:512\n\t"
        "buffer_load_dwordx4 %10, %14, %15, %16 offen offset:640\n\tbuffer_load_dwordx4 %11, %14, %15, %17 offen offset:640\n\t"
        "buffer_load_dwordx4 %12, %14, %15, %16 offen offset:768\n\tbuffer_load_dwordx4 %13, %14, %15, %17 offen offset:768\n\t"
        "s_waitcnt vmcnt(0)"
        : "=&v"(xr[0]), "=&v"(xr[1]), "=&v"(xr[2]), "=&v"(xr[3]), "=&v"(xr[4]), "=&v"(xr[5]), "=&v"(xr[6]), "=&v"(xr[7]), "=&v"(xr[8]),
          "=&v"(xr[9]), "=&v"(xr[10]), "=&v"(xr[11]), "=&v"(xr[12]), "=&v"(xr[13])
        : "v"(vo), "s"(sx), "s"(so), "s"(so + 8 * D_ * 2)
        : "memory");
  }
  asm volatile("" ::"v"(cpf), "v"(mpf));
  STAMPT(16);
  STAMPT(1);
  const v4u sq_srd = mksrd(wqkv, D_ * 3 * D_ * 2);
  const u32 vq = (u32)((lane >> 2) * 2688 + (((lane & 3) ^ (2 * (lane >> 5))) * 16));
#define WQDMA(i)                                                                                                        \
  {                                                                                                                     \
    const int u = w + 8 * ((i) >> 1), d = 2 * u + ((i) & 1), mat = u / 14, kb = d - 28 * mat;                           \
    dma16<false>(sq_srd, u < 42 ? vq : OOB, (u32)(16 * kb * 2688 + (mat % 3) * 896 + 56 * head), u < 42 ? L_WQ + d * 1024 : L_RED + 4096); \
  }
  WQDMA(0) WQDMA(1)
  {
    const u32 scr = L_WO + w * 8192;
    const u32 wa = scr + ((lane & 7) >> 2) * 1024 + (lane & 3) * 256 + (((lane >> 3) ^ (lane & 7)) * 16);
    const u32 ra0 = scr + g * 256 + ((l15 ^ g) * 16), ra1 = scr + 1024 + g * 256 + ((l15 ^ (4 + g)) * 16);
    v4u xf[14];
#pragma unroll
    for (int q = 0; q < 8; ++q) LDS4(wa + (q >> 1) * 2048 + (q & 1) * 128) = xr[q];
#pragma unroll
    for (int s = 0; s < 8; ++s) xf[s] = LDS4(((s & 1) ? ra1 : ra0) + (s >> 1) * 2048);
    SB0();
    WQDMA(2) WQDMA(3)
#pragma unroll
    for (int q = 8; q < 14; ++q) LDS4(wa + ((q - 8) >> 1) * 2048 + (q & 1) * 128) = xr[q];
#pragma unroll
    for (int s = 8; s < 14; ++s) xf[s] = LDS4(((s & 1) ? ra1 : ra0) + ((s - 8) >> 1) * 2048);
#pragma unroll
    for (int s = 0; s < 14; ++s) xr[s] = xf[s];
  }
  STAMPT(17);
  u32 pr[8];
  u32 pdst, pvo, pvo2, pso, pld;
  rsrc_t rp;
  {
    const int u = b * 8 + w;
    const u16* base; u32 bytes, ld, so, vo, vo2;
    if (u < 896) {
      const int kb = u >> 4, cc = (u >> 1) & 7, hf = u & 1;
      base = w2; bytes = DFF_ * D_ * 2; ld = D_ * 2; so = (u32)(32 * kb * D_ + 56 * cc + 2 * hf) * 2;
      vo = l15 < 14 ? (u32)(8 * g * D_ + 4 * l15) * 2 : OOB; vo2 = vo;
      pdst = WS_W2T + ((cc * 56 + kb) * 4 + 2 * hf) * 1024;
    } else if (u < 1680) {
      const int v = u - 896, cb = v / 14, s = v - 14 * cb;
      base = w1; bytes = DFF_ * D_ * 2; ld = DFF_ * 2; so = (u32)(32 * s * DFF_ + 32 * cb) * 2;
      vo = (u32)(8 * g * DFF_ + 2 * l15) * 2; vo2 = vo;
      pdst = WS_W1T + (cb * 14 + s) * 2048;
    } else {
      const int v = u - 1680, hh = v & 15, nh = (v >> 4) & 1, cc = (v >> 5) & 7;
      base = wo; bytes = D_ * D_ * 2; ld = D_ * 2; so = (u32)(28 * hh * D_ + 56 * cc + 32 * nh) * 2;
      vo = (u < 1936 && (nh == 0 || l15 < 12)) ? (u32)(4 * g * D_ + 2 * l15) * 2 : OOB; vo2 = g < 3 ? vo + 12 * D_ * 2 : OOB;
      pdst = u < 1936 ? WS_WOT + (cc * 32 + nh * 16 + hh) * 2048 : OOB;
    }
    rp = __builtin_amdgcn_make_buffer_rsrc((void*)base, 0, bytes, 0x00020000);
    pvo = vo; pvo2 = vo2; pso = so; pld = ld;
  }
  const int mtB = w & 3, nhB = w >> 2;
  v4u xres;
  u32 xh[14][8];
  {
    float sum = 0.f, sq = 0.f;
#pragma unroll
    for (int s = 0; s < 14; ++s) {
      if (s < 8) WQDMA(4 + s)
#pragma unroll
      for (int j = 0; j < 4; ++j) { sum = dot2(xr[s][j], 0x3F803F80u, sum); sq = dot2(xr[s][j], xr[s][j], sq); }
      PIN(sum); PIN(sq);
    }
    sum += __shfl_xor(sum, 16); sq += __shfl_xor(sq, 16);
    sum += __shfl_xor(sum, 32); sq += __shfl_xor(sq, 32);
    const float mean = sum * (1.f / D_);
    const float rstd = __builtin_amdgcn_rsqf(fmaxf(sq * (1.f / D_) - mean * mean, 0.f) + 1e-5f);
    const f2 rs2 = {rstd, rstd}, nm2 = {-mean * rstd, -mean * rstd};
#pragma unroll
    for (int s = 0; s < 14; ++s) {
      if (s < 8) pr[s] = ld1(rp, s < 4 ? pvo : pvo2, pso + s * pld);
      else if (s == 8) xres = ld4(rx, (nhB == 1 && g == 3) ? OOB : (u32)(l15 * D_ + 8 * g) * 2, (u32)((64 * g32 + 16 * mtB) * D_ + 56 * c8 + 32 * nhB) * 2);
      else if (s < 11) st4s(rws, lane * 16, WS_KV + (g16 * 16 + head) * 16384 + (2 * w + s - 9) * 1024, SENT);
      else if (s == 11) st4s(rws, lane * 16, WS_O + ((8 * g16 + w) * 16 + head) * 1024, SENT);
#pragma unroll
      for (int j = 0; j < 4; ++j) {
        const f2 v = __builtin_elementwise_fma((f2){lo2f(xr[s][j]), hi2f(xr[s][j])}, rs2, nm2);
        xh[s][2 * j] = pk(v.x, 1.f);
        xh[s][2 * j + 1] = pk(v.y, 1.f);
        PIN(xh[s][2 * j]); PIN(xh[s][2 * j + 1]);
      }
      SB0();
    }
  }
  STAMPT(20);
  asm volatile("s_waitcnt vmcnt(12)" ::: "memory");
  const int i1 = tid < 384 ? 512 + tid : tid;
  u64 g10 = __hip_atomic_load(gr + tid, RLX_AGENT), g11 = __hip_atomic_load(gr + i1, RLX_AGENT);
  {
    LAS char* tb0 = lds + L_WQ + (8 * g + (l15 >> 2)) * 64 + (g & 1) * 32 + (l15 & 3) * 8;
    LAS char* tb1 = lds + L_WQ + (8 * g + (l15 >> 2)) * 64 + ((g & 1) ^ 1) * 32 + (l15 & 3) * 8;
    v2u r[6][4];
#pragma unroll
    for (int i = 0; i < 6; ++i) {
      const int u = (i < 5 || w < 2) ? w + 8 * i : w;
      r[i][0] = trrd(tb0 + u * 2048); r[i][1] = trrd(tb0 + u * 2048 + 256);
      r[i][2] = trrd(tb1 + u * 2048); r[i][3] = trrd(tb1 + u * 2048 + 256);
    }
    SB0();
#pragma unroll
    for (int i = 0; i < 6; ++i) {
      const int u = (i < 5 || w < 2) ? w + 8 * i : w;
      LDS4(L_WQ + u * 2048 + lane * 16) = (v4u){r[i][0].x, r[i][0].y, r[i][1].x, r[i][1].y};
      LDS4(L_WQ + u * 2048 + 1024 + lane * 16) = (v4u){r[i][2].x, r[i][2].y, r[i][3].x, r[i][3].y};
    }
  }
  {
    v4u e = {plo(pr[0], pr[1]), plo(pr[2], pr[3]), plo(pr[4], pr[5]), plo(pr[6], pr[7])};
    v4u o = {phi(pr[0], pr[1]), phi(pr[2], pr[3]), phi(pr[4], pr[5]), phi(pr[6], pr[7])};
    st4s(rws, lane * 16, pdst, e);
    st4s(rws, lane * 16, pdst + 1024, o);
  }
  STAMPT(2);
  {
    for (unsigned spins = 0;; ++spins) {
      const bool ok = (g10 >> 16) == tag && (g11 >> 16) == tag;
      if (__all(ok) || spins > SPIN_CAP) break;
      BACKOFF();
      g10 = __hip_atomic_load(gr + tid, RLX_AGENT);
      g11 = __hip_atomic_load(gr + i1, RLX_AGENT);
    }
    RUNPRIO();
    if (tid < 448) LDSH(L_AS1 + tid * 4 + 2) = (u16)g10;
    else LDSH(L_AS1 + (tid - 448) * 4) = (u16)pk(1.f + lo2f((u32)g10), 0.f);
    if (tid < 384) LDSH(L_AS1 + (64 + tid) * 4) = (u16)pk(1.f + lo2f((u32)g11), 0.f);
    if (tid == 0) *(volatile LAS u32*)(lds + L_RED + 3072) = 0u;
  }
  __syncthreads();
  STAMPT(3);
  const int jg = tid - 448;
  const int i20 = tid < 448 ? 1344 + tid : 896 + 56 * c8 + (jg < 56 ? jg : 0);
  const int i21 = tid < 448 ? 1792 + tid : 2240 + 56 * c8 + (jg < 56 ? jg : 0);
  u64 g20 = __hip_atomic_load(gr + i20, RLX_AGENT), g21 = __hip_atomic_load(gr + i21, RLX_AGENT);

  STAMPT(29);
  f4 aq[6];
#pragma unroll
  for (int t = 0; t < 6; ++t) aq[t] = (f4){0, 0, 0, 0};
  {
    v4u fb[2][8];
#define QLOAD(s, d)                                                                                         \
  {                                                                                                         \
    fb[d][0] = LDS4(L_AS1 + (32 * (s) + 8 * g) * 4); fb[d][1] = LDS4(L_AS1 + (32 * (s) + 8 * g) * 4 + 16);  \
    _Pragma("unroll") for (int t = 0; t < 6; ++t) fb[d][2 + t] = LDS4(L_WQ + ((t >> 1) * 14 + (s)) * 2048 + (t & 1) * 1024 + lane * 16); \
  }
    QLOAD(0, 0)
    QLOAD(1, 1)
    v4u hf = modulate(xh[0], fb[0][0], fb[0][1]);
#pragma unroll
    for (int s = 0; s < 14; ++s) {
      SB0();
      if (s < 13) hf = qstep(aq, fb[s & 1], hf, xh[s + 1], fb[(s + 1) & 1][0], fb[(s + 1) & 1][1]);
      else {
#pragma unroll
        for (int t = 0; t < 4; ++t) aq[t] = mfma(fb[s & 1][2 + t], hf, aq[t]);
#pragma unroll
        for (int t = 4; t < 6; ++t) aq[t] = mfma(hf, fb[s & 1][2 + t], aq[t]);
      }
      SB0();
      if (s < 12) QLOAD(s + 2, s & 1)
      if (s == 3) {
        DRAIN();
        u32 old = 0;
        if (lane == 0) old = __hip_atomic_fetch_add((LAS u32*)(lds + L_RED + 3072), 1u, __ATOMIC_RELAXED, __HIP_MEMORY_SCOPE_WORKGROUP);
        if (lane == 0 && old == 7u) __hip_atomic_store(gr + G_PREP + b, tag << 16, RLX_AGENT);
      }
      if (s == 5) st4s(rws, (w < 4 || lane < 48) ? lane * 16 : OOB, WS_X1 + (4 * g32 + (w & 3)) * 14336 + c8 * 1792 + (w >> 2) * 1024, SENT);
      if (s == 7 && w < 4) st4s(rws, lane < 16 ? lane * 16 : OOB, WS_ST + (4 * g32 + w) * 2048 + c8 * 256, SENT);
    }
  }
  STAMPT(30);
  u64 ff[4];
#pragma unroll
  for (int i = 0; i < 4; ++i) ff[i] = __hip_atomic_load(gr + G_PREP + lane + 64 * i, RLX_AGENT);
  STAMPT(31);
  __syncthreads();
  STAMPT(4);
  const v4u qf = {pk(aq[0][0], aq[0][1]), pk(aq[0][2], aq[0][3]), g < 3 ? pk(aq[1][0], aq[1][1]) : 0u, g < 3 ? pk(aq[1][2], aq[1][3]) : 0u};
  {
    const v4u kf = nosent4((v4u){pk(aq[2][0], aq[2][1]), pk(aq[2][2], aq[2][3]), pk(aq[3][0], aq[3][1]), pk(aq[3][2], aq[3][3])});
    const int tau = 16 * (w & 1) + l15;
    const u32 koff = (w >> 1) * 2048 + ((tau >> 2) & 1) * 1024 + (g * 16 + 4 * (tau >> 3) + (tau & 3)) * 16;
    v2u ve = {pk(aq[4][0], aq[4][1]), pk(aq[4][2], aq[4][3])}, vo_ = {pk(aq[5][0], aq[5][1]), pk(aq[5][2], aq[5][3])};
    if (l15 == 12) vo_ = (v2u){0x3F803F80u, 0x3F803F80u};
    ve.x = nosent(ve.x); ve.y = nosent(ve.y); vo_.x = nosent(vo_.x); vo_.y = nosent(vo_.y);
    const u32 voff = (w >> 1) * 2048 + ((2 * (w & 1) + (g >> 1)) * 16 + l15) * 16 + (g & 1) * 8;
    const u32 base = g16 < 15 ? WS_KV + (g16 * 16 + head) * 16384 : OOB;
    st4s(rws, koff, base, kf);
    st2s(rws, voff, base + 8192, ve);
    st2s(rws, voff, base + 8192 + 1024, vo_);
    LDS4(L_K + 16384 + koff) = kf;
    LDS2(L_V + 16384 + voff) = ve;
    LDS2(L_V + 16384 + 1024 + voff) = vo_;
  }
  STAMPT(21);
  {
    for (unsigned spins = 0;; ++spins) {
      bool ok = (g20 >> 16) == tag && (g21 >> 16) == tag;
#pragma unroll
      for (int i = 0; i < 4; ++i) ok &= (ff[i] >> 16) == tag;
      if (__all(ok) || spins > SPIN_CAP) break;
      BACKOFF();
      g20 = __hip_atomic_load(gr + i20, RLX_AGENT);
      g21 = __hip_atomic_load(gr + i21, RLX_AGENT);
#pragma unroll
      for (int i = 0; i < 4; ++i) ff[i] = __hip_atomic_load(gr + G_PREP + lane + 64 * i, RLX_AGENT);
    }
    RUNPRIO();
    CBAR();
    if (b == 0 && tid == 0) __hip_atomic_store((gu64*)ws, epoch + 1, RLX_AGENT);
    if (tid < 448) {
      LDSH(L_SB + tid * 2) = (u16)g20;
      LDSH(L_A2 + tid * 2) = (u16)pk(1.f + lo2f((u32)g21), 0.f);
    } else {
      LDSF(L_GA + jg * 4) = lo2f((u32)g20);
      LDSF(L_GB + jg * 4) = lo2f((u32)g21);
    }
  }
  STAMPT(22);
  v4u w1f[14][2];
  const u32 vo1 = w < 7 ? lane * 16 : OOB;
  {
    const v4u sws = mksrd(ws, WS_END);
#pragma unroll
    for (int i = 0; i < 8; ++i) dma16<true>(sws, lane * 16, WS_WOT + c8 * 65536 + (w * 8 + i) * 1024, L_WO + (w * 8 + i) * 1024);
#pragma unroll
    for (int i = 0; i < 6; ++i) w1f[i >> 1][i & 1] = ld4s(rws, vo1, WS_W1T + (7 * c8 + (w % 7)) * 28672 + i * 1024);
  }
  {
    const u32 b0 = g16 >= 2 ? WS_KV + ((g16 - 2) * 16 + head) * 16384 : OOB;
    const u32 b1 = g16 >= 1 ? WS_KV + ((g16 - 1) * 16 + head) * 16384 : OOB;
    v4u k0, v0, k1, v1;
    for (unsigned spins = 0;; ++spins) {
      CBAR();
      k0 = ld4s(rws, tid * 16, b0); v0 = ld4s(rws, tid * 16, b0 + 8192);
      k1 = ld4s(rws, tid * 16, b1); v1 = ld4s(rws, tid * 16, b1 + 8192);
      const bool bad = chk(chk(chk(chk(0u, k0), v0), k1), v1) == ~0u;
      if (!__any(bad) || spins > SPIN_CAP) break;
      BACKOFF();
    }
    RUNPRIO();
    STAMPT(24);
    LDS4(L_K + tid * 16) = k0; LDS4(L_V + tid * 16) = v0;
    LDS4(L_K + 8192 + tid * 16) = k1; LDS4(L_V + 8192 + tid * 16) = v1;
  }
  DRAIN();
  __syncthreads();
  STAMPT(5);
  {
    const int blk0 = w >> 1;
    f4 sc[18];
    {
      v4u kf[18];
#pragma unroll
      for (int t = 0; t < 6; ++t) kf[t] = LDS4(L_K + (blk0 + (t >> 1)) * 2048 + (t & 1) * 1024 + lane * 16);
#pragma unroll
      for (int t = 0; t < 18; ++t) {
        if (t + 6 < 18) kf[t + 6] = LDS4(L_K + (blk0 + ((t + 6) >> 1)) * 2048 + (t & 1) * 1024 + lane * 16);
        if (t & 1) w1f[(6 + (t >> 1)) >> 1][(6 + (t >> 1)) & 1] = ld4s(rws, vo1, WS_W1T + (7 * c8 + (w % 7)) * 28672 + (6 + (t >> 1)) * 1024);
        SB0();
        sc[t] = mfma(kf[t], qf, (f4){0, 0, 0, 0});
        SB0();
      }
    }
    STAMPT(25);
    v4u vf[18];
#pragma unroll
    for (int t = 0; t < 4; ++t) vf[t] = LDS4(L_V + (blk0 + (t >> 1)) * 2048 + (t & 1) * 1024 + lane * 16);
    const int ql = 256 + 16 * w + l15;
    const int kmin = (256 - 128 * g16) > (ql - 255) ? (256 - 128 * g16) : (ql - 255);
    const int kb0 = 32 * blk0 + 8 * g;
    const float NEG = -1e30f;
    if (g16 < 2) {
#pragma unroll
      for (int t = 2; t < 16; ++t)
#pragma unroll
        for (int r = 0; r < 4; ++r) sc[t][r] = (kb0 + 32 * (t >> 1) + 4 * (t & 1) + r) >= kmin ? sc[t][r] : NEG;
    }
#pragma unroll
    for (int t = 0; t < 2; ++t)
#pragma unroll
      for (int r = 0; r < 4; ++r) sc[t][r] = (kb0 + 4 * t + r) >= kmin ? sc[t][r] : NEG;
#pragma unroll
    for (int t = 16; t < 18; ++t)
#pragma unroll
      for (int r = 0; r < 4; ++r) sc[t][r] = (kb0 + 256 + 4 * (t & 1) + r) <= ql ? sc[t][r] : NEG;
    float mx = NEG;
#pragma unroll
    for (int t = 0; t < 18; ++t) mx = fmaxf(fmaxf(mx, sc[t][0]), fmaxf(sc[t][1], fmaxf(sc[t][2], sc[t][3])));
    mx = fmaxf(mx, __shfl_xor(mx, 16));
    mx = fmaxf(mx, __shfl_xor(mx, 32));
    STAMPT(26);
    STAMPT(18);
    const float cs = 0.18898223650461362f * 1.4426950408889634f;
    const float mc = mx * cs;
    f4 oe = {0, 0, 0, 0}, oo = {0, 0, 0, 0};
#pragma unroll
    for (int bb = 0; bb < 9; ++bb) {
      if (bb + 2 < 9) {
        vf[2 * bb + 4] = LDS4(L_V + (blk0 + bb + 2) * 2048 + lane * 16);
        vf[2 * bb + 5] = LDS4(L_V + (blk0 + bb + 2) * 2048 + 1024 + lane * 16);
      }
      w1f[(15 + bb) >> 1][(15 + bb) & 1] = ld4s(rws, vo1, WS_W1T + (7 * c8 + (w % 7)) * 28672 + (15 + bb) * 1024);
      if ((bb & 1) == 0 && bb < 8) st4s(rws, vo1, WS_HID + (4 * g32 + (bb >> 1)) * 57344 + c8 * 7168 + (w % 7) * 1024, SENT);
      SB0();
      float p[8];
#pragma unroll
      for (int r = 0; r < 4; ++r) {
        p[r] = __builtin_amdgcn_exp2f(sc[2 * bb][r] * cs - mc);
        p[4 + r] = __builtin_amdgcn_exp2f(sc[2 * bb + 1][r] * cs - mc);
      }
      const v4u pf = {pk(p[0], p[1]), pk(p[2], p[3]), pk(p[4], p[5]), pk(p[6], p[7])};
      oe = mfma(vf[2 * bb], pf, oe);
      oo = mfma(vf[2 * bb + 1], pf, oo);
      SB0();
    }
    const float den = __shfl(oo[0], 48 + l15);
    const float inv = __builtin_amdgcn_rcpf(den);
    const v4u of = nosent4((v4u){pk(oe[0] * inv, oe[1] * inv), pk(oe[2] * inv, oe[3] * inv), pk(oo[0] * inv, oo[1] * inv), pk(oo[2] * inv, oo[3] * inv)});
    st4s(rws, lane * 16, WS_O + ((8 * g16 + w) * 16 + head) * 1024, of);
  }
  STAMPT(6);
  SB0();
#pragma unroll
  for (int i = 24; i < 28; ++i) w1f[i >> 1][i & 1] = ld4s(rws, vo1, WS_W1T + (7 * c8 + (w % 7)) * 28672 + i * 1024);
  STAMPT(19);
  STAMPT(27);
  __syncthreads();
  STAMPT(28);

  {
    v4u of[8];
    const u32 base = WS_O + (4 * g32 + mtB) * 16384 + nhB * 8192;
    for (unsigned spins = 0;; ++spins) {
      CBAR();
      u32 m = 0;
#pragma unroll
      for (int h = 0; h < 8; ++h) of[h] = ld4s(rws, lane * 16, base + h * 1024);
#pragma unroll
      for (int h = 0; h < 8; ++h) m = chk(m, of[h]);
      if (!__any(m == ~0u) || spins > SPIN_CAP) break;
      BACKOFF();
    }
    RUNPRIO();
    STAMPT(7);
    f4 acc[4] = {{0, 0, 0, 0}, {0, 0, 0, 0}, {0, 0, 0, 0}, {0, 0, 0, 0}};
#pragma unroll
    for (int h = 0; h < 8; ++h)
#pragma unroll
      for (int t = 0; t < 4; ++t) acc[t] = mfma(LDS4(L_WO + ((t >> 1) * 16 + nhB * 8 + h) * 2048 + (t & 1) * 1024 + lane * 16), of[h], acc[t]);
    LDSF4(L_PR + w * 2048 + lane * 16) = nhB ? acc[0] : acc[2];
    LDSF4(L_PR + w * 2048 + 1024 + lane * 16) = nhB ? acc[1] : acc[3];
    DRAIN();
    __syncthreads();
    const f4 e = (nhB ? acc[2] : acc[0]) + LDSF4(L_PR + (w ^ 4) * 2048 + lane * 16);
    const f4 o = (nhB ? acc[3] : acc[1]) + LDSF4(L_PR + (w ^ 4) * 2048 + 1024 + lane * 16);
    const f4 g0 = LDSF4(L_GA + (32 * nhB + 8 * g) * 4), g1 = LDSF4(L_GA + (32 * nhB + 8 * g) * 4 + 16);
    const v4u a2 = LDS4(L_A2 + (56 * c8 + 32 * nhB + 8 * g) * 2);
    v4u x1;
    x1.x = pk(lo2f(xres.x) + g0.x * e[0], hi2f(xres.x) + g0.y * o[0]);
    x1.y = pk(lo2f(xres.y) + g0.z * e[1], hi2f(xres.y) + g0.w * o[1]);
    x1.z = pk(lo2f(xres.z) + g1.x * e[2], hi2f(xres.z) + g1.y * o[2]);
    x1.w = pk(lo2f(xres.w) + g1.z * e[3], hi2f(xres.w) + g1.w * o[3]);
    v4u y;
#pragma unroll
    for (int j = 0; j < 4; ++j) y[j] = nosent(pk(lo2f(x1[j]) * lo2f(a2[j]), hi2f(x1[j]) * hi2f(a2[j])));
    const bool vld = !(nhB == 1 && g == 3);
    st4s(rws, vld ? lane * 16 : OOB, WS_X1 + (4 * g32 + mtB) * 14336 + (7 * c8 + 4 * nhB) * 256, y);
    float s1 = 0.f, s2 = 0.f;
#pragma unroll
    for (int j = 0; j < 4; ++j) { s1 = dot2(x1[j], 0x3F803F80u, s1); s2 = dot2(x1[j], x1[j], s2); }
    s1 += __shfl_xor(s1, 16); s2 += __shfl_xor(s2, 16);
    s1 += __shfl_xor(s1, 32); s2 += __shfl_xor(s2, 32);
    st2s(rws, g == 0 ? l15 * 8 : OOB, WS_ST + (4 * g32 + mtB) * 2048 + (2 * c8 + nhB) * 128,
         (v2u){nosent(__builtin_bit_cast(u32, s1)), nosent(__builtin_bit_cast(u32, s2))});
    if (vld) LDS4(L_X1S + (mtB * 7 + 4 * nhB) * 256 + lane * 16) = x1;
  }
  STAMPT(8);
  f4 au[2] = {{0, 0, 0, 0}, {0, 0, 0, 0}}, av[2] = {{0, 0, 0, 0}, {0, 0, 0, 0}};
#pragma unroll
  for (int s = 0; s < 14; ++s) {
    const v4u a2 = LDS4(L_A2 + (32 * s + 8 * g) * 2), sb = LDS4(L_SB + (32 * s + 8 * g) * 2);
    au[0] = mfma(w1f[s][0], a2, au[0]); au[1] = mfma(w1f[s][1], a2, au[1]);
    av[0] = mfma(w1f[s][0], sb, av[0]); av[1] = mfma(w1f[s][1], sb, av[1]);
  }
  PIN(au[0]); PIN(au[1]); PIN(av[0]); PIN(av[1]);
  STAMPT(23);
  {
    v4u t[7], st[2];
    const u32 base = WS_X1 + 4 * g32 * 14336 + w * 7168;
    const u32 sto = w < 4 ? lane * 16 : OOB;
    for (unsigned spins = 0;; ++spins) {
      CBAR();
      u32 m = 0;
#pragma unroll
      for (int i = 0; i < 7; ++i) t[i] = ld4s(rws, lane * 16, base + i * 1024);
      st[0] = ld4s(rws, sto, WS_ST + (4 * g32 + (w & 3)) * 2048);
      st[1] = ld4s(rws, sto, WS_ST + (4 * g32 + (w & 3)) * 2048 + 1024);
#pragma unroll
      for (int i = 0; i < 7; ++i) m = chk(m, t[i]);
      m = chk(chk(m, st[0]), st[1]);
      if (!__any(m == ~0u) || spins > SPIN_CAP) break;
      BACKOFF();
    }
    RUNPRIO();
#pragma unroll
    for (int i = 0; i < 7; ++i) LDS4(w * 7168 + i * 1024 + lane * 16) = t[i];
    if (w < 4) {
      f4 a = __builtin_bit_cast(f4, st[0]) + __builtin_bit_cast(f4, st[1]);
#pragma unroll
      for (int j = 0; j < 4; ++j) { a[j] += dppf<0x128>(a[j]); a[j] += __shfl_xor(a[j], 16); a[j] += __shfl_xor(a[j], 32); }
      const float m0 = a.x * (1.f / D_), m1 = a.z * (1.f / D_);
      const float r0 = __builtin_amdgcn_rsqf(fmaxf(a.y * (1.f / D_) - m0 * m0, 0.f) + 1e-5f);
      const float r1 = __builtin_amdgcn_rsqf(fmaxf(a.w * (1.f / D_) - m1 * m1, 0.f) + 1e-5f);
      if (lane < 8) LDSF4(L_MR + w * 128 + lane * 16) = (f4){m0, r0, m1, r1};
    }
    __syncthreads();
  }
  STAMPT(9);
  v4u wf[7][4];
#pragma unroll
  for (int mp = 0; mp < 2; ++mp) {
    f4 acc[2][2] = {{{0, 0, 0, 0}, {0, 0, 0, 0}}, {{0, 0, 0, 0}, {0, 0, 0, 0}}};
    v4u xb[3][2];
#pragma unroll
    for (int m = 0; m < 2; ++m) { xb[0][m] = LDS4((2 * mp + m) * 14336 + lane * 16); xb[1][m] = LDS4((2 * mp + m) * 14336 + 1024 + lane * 16); }
#pragma unroll
    for (int s = 0; s < 14; ++s) {
      if (s < 12) {
#pragma unroll
        for (int m = 0; m < 2; ++m) xb[(s + 2) % 3][m] = LDS4((2 * mp + m) * 14336 + (s + 2) * 1024 + lane * 16);
      }
      SB0();
#pragma unroll
      for (int m = 0; m < 2; ++m) {
        acc[m][0] = mfma(w1f[s][0], xb[s % 3][m], acc[m][0]);
        acc[m][1] = mfma(w1f[s][1], xb[s % 3][m], acc[m][1]);
      }
      SB0();
      if (mp == 1) {
#pragma unroll
        for (int j = 0; j < 2; ++j) wf[s >> 1][(s & 1) * 2 + j] = ld4s(rws, lane * 16, WS_W2T + ((c8 * 56 + 7 * w + (s >> 1)) * 4 + (s & 1) * 2 + j) * 1024);
        SB0();
      }
    }
#pragma unroll
    for (int m = 0; m < 2; ++m) {
      const f2 mr2 = *(LAS f2*)(lds + L_MR + ((2 * mp + m) * 16 + l15) * 8);
      const f2 nm = {-mr2.x, -mr2.x}, rs = {mr2.y, mr2.y};
      v4u hv;
#pragma unroll
      for (int r = 0; r < 4; ++r) {
        f2 h = __builtin_elementwise_fma(nm, (f2){au[0][r], au[1][r]}, (f2){acc[m][0][r], acc[m][1][r]});
        h = __builtin_elementwise_fma(rs, h, (f2){av[0][r], av[1][r]});
        hv[r] = pk(fmaxf(h.x, 0.f), fmaxf(h.y, 0.f));
      }
      st4s(rws, w < 7 ? lane * 16 : OOB, WS_HID + (4 * g32 + 2 * mp + m) * 57344 + (28 * c8 + 4 * (w % 7)) * 256, hv);
    }
    SB0();
  }
  STAMPT(10);
  __syncthreads();
  {
#pragma unroll
    for (int mp = 0; mp < 2; ++mp) {
      v4u hf[2][7];
      const u32 base = WS_HID + (4 * g32 + 2 * mp) * 57344 + w * 7168;
      for (unsigned spins = 0;; ++spins) {
        CBAR();
        u32 m = 0;
#pragma unroll
        for (int i = 0; i < 2; ++i)
#pragma unroll
          for (int s = 0; s < 7; ++s) hf[i][s] = ld4s(rws, lane * 16, base + i * 57344 + s * 1024);
#pragma unroll
        for (int i = 0; i < 2; ++i)
#pragma unroll
          for (int s = 0; s < 7; ++s) m = chk(m, hf[i][s]);
        if (!__any(m == ~0u) || spins > SPIN_CAP) break;
        BACKOFF();
      }
      RUNPRIO();
      if (mp == 0) STAMPT(11);
#pragma unroll
      for (int i = 0; i < 2; ++i) {
        f4 acc[4] = {{0, 0, 0, 0}, {0, 0, 0, 0}, {0, 0, 0, 0}, {0, 0, 0, 0}};
#pragma unroll
        for (int s = 0; s < 7; ++s)
#pragma unroll
          for (int t = 0; t < 4; ++t) acc[t] = mfma(wf[s][t], hf[i][s], acc[t]);
#pragma unroll
        for (int r = 0; r < 4; ++r) LDSF4(((w * 4 + 2 * mp + i) * 4 + r) * 1024 + lane * 16) = (f4){acc[0][r], acc[1][r], acc[2][r], acc[3][r]};
      }
    }
  }
  __syncthreads();
  STAMPT(12);
  {
    const int mt = tid >> 7, rp = (tid >> 6) & 1;
    if (!(g == 3 && rp == 1)) {
      f4 s0 = {0, 0, 0, 0}, s1 = {0, 0, 0, 0};
#pragma unroll
      for (int ww = 0; ww < 8; ++ww) {
        s0 += LDSF4(((ww * 4 + mt) * 4 + 2 * rp) * 1024 + lane * 16);
        s1 += LDSF4(((ww * 4 + mt) * 4 + 2 * rp + 1) * 1024 + lane * 16);
      }
      const v4u x1 = LDS4(L_X1S + (mt * 7 + 2 * g + rp) * 256 + l15 * 16);
      const f4 g0 = LDSF4(L_GB + (16 * g + 8 * rp) * 4), g1 = LDSF4(L_GB + (16 * g + 8 * rp) * 4 + 16);
      v4u o;
      o.x = pk(lo2f(x1.x) + g0.x * s0.x, hi2f(x1.x) + g0.y * s0.y);
      o.y = pk(lo2f(x1.y) + g0.z * s0.z, hi2f(x1.y) + g0.w * s0.w);
      o.z = pk(lo2f(x1.z) + g1.x * s1.x, hi2f(x1.z) + g1.y * s1.y);
      o.w = pk(lo2f(x1.w) + g1.z * s1.z, hi2f(x1.w) + g1.w * s1.w);
      *(v4u*)(out + (size_t)(64 * g32 + 16 * mt + l15) * D_ + 56 * c8 + 16 * g + 8 * rp) = o;
    }
  }
  STAMPT(13);
  asm volatile(".Lmega_end:");
}

extern "C" void kernel_launch(void* const* d_in, const int* in_sizes, int n_in, void* d_out, int out_size, void* d_ws,
                              size_t ws_size, hipStream_t stream) {
  (void)hipFuncSetAttribute((const void*)mega, hipFuncAttributeMaxDynamicSharedMemorySize, LDS_BYTES);
  mega<<<256, 512, LDS_BYTES, stream>>>((const u16*)d_in[0], (const u16*)d_in[1], (const u16*)d_in[2], (const u16*)d_in[4],
                                        (const u16*)d_in[5], (const u16*)d_in[6], (const u16*)d_in[7], (u16*)d_out, (char*)d_ws);
}
```

```cpp
#include <hip/hip_runtime.h>
#include <stdint.h>

typedef unsigned int u32;
typedef unsigned long long u64;
typedef unsigned short u16;
typedef u32 v4u __attribute__((ext_vector_type(4)));
typedef u32 v2u __attribute__((ext_vector_type(2)));
typedef float f4 __attribute__((ext_vector_type(4)));
typedef float f2 __attribute__((ext_vector_type(2)));
typedef __bf16 bf2v __attribute__((ext_vector_type(2)));
typedef __bf16 bf8v __attribute__((ext_vector_type(8)));
#define GAS __attribute__((address_space(1)))
#define LAS __attribute__((address_space(3)))
typedef GAS u64 gu64;
#define RLX_AGENT __ATOMIC_RELAXED, __HIP_MEMORY_SCOPE_AGENT
typedef __amdgpu_buffer_rsrc_t rsrc_t;

constexpr int S_ = 2048, D_ = 448, DFF_ = 1792;
constexpr int WS_GRAN = 4096;
constexpr int G_PREP = 2688, G_FILL = 2944;
constexpr int G_REP = 4096;
constexpr int WS_KV = 524288;
constexpr int WS_O = WS_KV + 4194304;
constexpr int WS_X1 = WS_O + 2097152;
constexpr int WS_ST = WS_X1 + 1835008;
constexpr int WS_HID = WS_ST + 262144;
constexpr int WS_W2T = WS_HID + 7340032;
constexpr int WS_W1T = WS_W2T + 1835008;
constexpr int WS_WOT = WS_W1T + 1605632;
constexpr int WS_END = WS_WOT + 524288;
constexpr int WS_DBG = WS_END;
constexpr int L_WQ = 0;
constexpr int L_K = 0, L_V = 24576;
constexpr int L_PR = 49152;
constexpr int L_WO = 86016;
constexpr int L_X = 36864;
constexpr int L_UV = L_WO;
constexpr int L_SM = 151552;
constexpr int L_AS1 = L_SM;
constexpr int L_A2 = L_AS1 + 1792;
constexpr int L_SB = L_A2 + 896;
constexpr int L_GA = L_SB + 896;
constexpr int L_GB = L_GA + 256;
constexpr int L_MR = L_GB + 256;
constexpr int L_RED = L_MR + 512;
constexpr int L_X1S = L_RED;
constexpr int L_END = L_X1S + 7168;
static_assert(L_END <= 163840, "lds");
constexpr int LDS_BYTES = 163840;
constexpr u32 OOB = 0x80000000u;
constexpr unsigned SPIN_CAP = 1u << 15;

__device__ __forceinline__ u32 pk(float lo, float hi) { f2 v = {lo, hi}; return __builtin_bit_cast(u32, __builtin_convertvector(v, bf2v)); }
__device__ __forceinline__ float dot2(u32 a, u32 b, float c) { return __builtin_amdgcn_fdot2_f32_bf16(__builtin_bit_cast(bf2v, a), __builtin_bit_cast(bf2v, b), c, false); }
__device__ __forceinline__ f4 mfma(v4u a, v4u b, f4 c) { return __builtin_amdgcn_mfma_f32_16x16x32_bf16(__builtin_bit_cast(bf8v, a), __builtin_bit_cast(bf8v, b), c, 0, 0, 0); }
__device__ __forceinline__ float lo2f(u32 v) { return __builtin_bit_cast(float, v << 16); }
__device__ __forceinline__ float hi2f(u32 v) { return __builtin_bit_cast(float, v & 0xffff0000u); }
__device__ __forceinline__ u32 plo(u32 r0, u32 r1) { return __builtin_amdgcn_perm(r1, r0, 0x05040100u); }
__device__ __forceinline__ u32 phi(u32 r0, u32 r1) { return __builtin_amdgcn_perm(r1, r0, 0x07060302u); }
__device__ __forceinline__ u32 nosent(u32 v) { return v < 0xFFFFFFFEu ? v : 0xFFFFFFFEu; }
__device__ __forceinline__ v4u nosent4(v4u v) { v.x = nosent(v.x); v.y = nosent(v.y); v.z = nosent(v.z); v.w = nosent(v.w); return v; }
__device__ __forceinline__ u32 umax3(u32 a, u32 b, u32 c) { u32 m = a > b ? a : b; return m > c ? m : c; }
__device__ __forceinline__ u32 chk(u32 m, v4u v) { return umax3(umax3(m, v.x, v.y), v.z, v.w); }
template <int C> __device__ __forceinline__ float dppf(float v) { return __builtin_bit_cast(float, __builtin_amdgcn_update_dpp(0, __builtin_bit_cast(int, v), C, 0xf, 0xf, false)); }
__device__ __forceinline__ float rowsum16(float v) { v += dppf<0xB1>(v); v += dppf<0x4E>(v); v += dppf<0x141>(v); v += dppf<0x140>(v); return v; }
__device__ __forceinline__ v4u ld4(rsrc_t r, u32 vo, u32 so) { return __builtin_amdgcn_raw_buffer_load_b128(r, vo, so, 0); }
__device__ __forceinline__ v4u ld4s(rsrc_t r, u32 vo, u32 so) { return __builtin_amdgcn_raw_buffer_load_b128(r, vo, so, 16); }
__device__ __forceinline__ v2u ld2(rsrc_t r, u32 vo, u32 so) { return __builtin_amdgcn_raw_buffer_load_b64(r, vo, so, 0); }
__device__ __forceinline__ u32 ld1(rsrc_t r, u32 vo, u32 so) { return __builtin_amdgcn_raw_buffer_load_b32(r, vo, so, 0); }
__device__ __forceinline__ void st4s(rsrc_t r, u32 vo, u32 so, v4u v) { __builtin_amdgcn_raw_buffer_store_b128(v, r, vo + so, 0, 16); }
__device__ __forceinline__ void st2s(rsrc_t r, u32 vo, u32 so, v2u v) { __builtin_amdgcn_raw_buffer_store_b64(v, r, vo, so, 16); }
__device__ __forceinline__ v4u modulate(const u32 (&xh)[8], v4u a0, v4u a1) {
  v4u h;
  float d0, d1, d2, d3, d4, d5, d6, d7;
  asm volatile(
      "v_dot2_f32_bf16 %4, %12, %20, 0\n\tv_dot2_f32_bf16 %5, %13, %21, 0\n\tv_dot2_f32_bf16 %6, %14, %22, 0\n\tv_dot2_f32_bf16 %7, %15, %23, 0\n\t"
      "v_dot2_f32_bf16 %8, %16, %24, 0\n\tv_dot2_f32_bf16 %9, %17, %25, 0\n\tv_dot2_f32_bf16 %10, %18, %26, 0\n\tv_dot2_f32_bf16 %11, %19, %27, 0\n\t"
      "v_cvt_pk_bf16_f32 %0, %4, %5\n\tv_cvt_pk_bf16_f32 %1, %6, %7\n\tv_cvt_pk_bf16_f32 %2, %8, %9\n\tv_cvt_pk_bf16_f32 %3, %10, %11\n\ts_nop 1"
      : "=&v"(h.x), "=&v"(h.y), "=&v"(h.z), "=&v"(h.w), "=&v"(d0), "=&v"(d1), "=&v"(d2), "=&v"(d3), "=&v"(d4), "=&v"(d5), "=&v"(d6), "=&v"(d7)
      : "v"(xh[0]), "v"(xh[1]), "v"(xh[2]), "v"(xh[3]), "v"(xh[4]), "v"(xh[5]), "v"(xh[6]), "v"(xh[7]),
        "v"(a0.x), "v"(a0.y), "v"(a0.z), "v"(a0.w), "v"(a1.x), "v"(a1.y), "v"(a1.z), "v"(a1.w));
  return h;
}
typedef short s4v __attribute__((ext_vector_type(4)));
__device__ __forceinline__ v2u trrd(LAS char* p) { return __builtin_bit_cast(v2u, __builtin_amdgcn_ds_read_tr16_b64_v4i16((LAS s4v*)p)); }
__device__ __forceinline__ v4u mksrd(const void* p, u32 bytes) {
  const u64 a = (u64)p;
  return (v4u){(u32)__builtin_amdgcn_readfirstlane((u32)a), (u32)__builtin_amdgcn_readfirstlane((u32)(a >> 32)) & 0xffffu, bytes, 0x00020000u};
}
template <bool SC1> __device__ __forceinline__ void dma16(v4u srd, u32 vo, u32 so, u32 ldsaddr) {
  u32 keep;
  so = __builtin_amdgcn_readfirstlane(so);
  ldsaddr = __builtin_amdgcn_readfirstlane(ldsaddr);
  if (SC1)
    asm volatile("s_mov_b32 %0, m0\n\ts_mov_b32 m0, %4\n\ts_nop 3\n\tbuffer_load_dwordx4 %1, %2, %3 offen sc1 lds\n\ts_mov_b32 m0, %0"
                 : "=&s"(keep) : "v"(vo), "s"(srd), "s"(so), "s"(ldsaddr) : "memory");
  else
    asm volatile("s_mov_b32 %0, m0\n\ts_mov_b32 m0, %4\n\ts_nop 3\n\tbuffer_load_dwordx4 %1, %2, %3 offen lds\n\ts_mov_b32 m0, %0"
                 : "=&s"(keep) : "v"(vo), "s"(srd), "s"(so), "s"(ldsaddr) : "memory");
}
__device__ __forceinline__ void canary(rsrc_t r, u32 vo, u32 so, unsigned& spins) {
  __builtin_amdgcn_s_setprio(0);
  for (; spins <= SPIN_CAP; ++spins) {
    __builtin_amdgcn_s_sleep(2);
    asm volatile("" ::: "memory");
    if (!__any(__builtin_amdgcn_raw_buffer_load_b32(r, vo, so, 16) == ~0u)) break;
  }
}
#define LDS4(off) (*(LAS v4u*)(lds + (off)))
#define LDS2(off) (*(LAS v2u*)(lds + (off)))
#define LDSF4(off) (*(LAS f4*)(lds + (off)))
#define LDSF(off) (*(LAS float*)(lds + (off)))
#define LDSH(off) (*(LAS u16*)(lds + (off)))
#define CBAR() asm volatile("" ::: "memory")
#define PIN(v) asm volatile("" : "+v"(v))
#define SB0() __builtin_amdgcn_sched_barrier(0)
#define BACKOFF() do { __builtin_amdgcn_s_setprio(0); __builtin_amdgcn_s_sleep(6); } while (0)
#define RUNPRIO() __builtin_amdgcn_s_setprio(1)
#define DRAIN() asm volatile("s_waitcnt vmcnt(0)" ::: "memory")
#define STAMPT(i)

__global__ __launch_bounds__(512, 2) void mega(const u16* __restrict__ x, const u16* __restrict__ wqkv, const u16* __restrict__ wo,
                                               const u16* __restrict__ w1, const u16* __restrict__ w2, const u16* __restrict__ cvec,
                                               const u16* __restrict__ wmod, u16* __restrict__ out, char* ws) {
  extern __shared__ __attribute__((aligned(16))) char lds_g[];
  LAS char* lds = (LAS char*)lds_g;
  const int tid = threadIdx.x, lane = tid & 63, w = __builtin_amdgcn_readfirstlane(tid >> 6) & 7;
  const int l15 = lane & 15, g = lane >> 4;
  const int b = blockIdx.x, xcd = b & 7, slot = b >> 3;
  const int g16 = xcd * 2 + (slot >> 4), head = slot & 15;
  const int g32 = xcd * 4 + (slot >> 3), c8 = slot & 7;
  const rsrc_t rws = __builtin_amdgcn_make_buffer_rsrc(ws, 0, WS_END, 0x00020000);
  const rsrc_t rx = __builtin_amdgcn_make_buffer_rsrc((void*)x, 0, S_ * D_ * 2, 0x00020000);
  const rsrc_t rwq = __builtin_amdgcn_make_buffer_rsrc((void*)wqkv, 0, D_ * 3 * D_ * 2, 0x00020000);
  const rsrc_t rwm = __builtin_amdgcn_make_buffer_rsrc((void*)wmod, 0, D_ * 6 * D_ * 2, 0x00020000);
  const rsrc_t rc = __builtin_amdgcn_make_buffer_rsrc((void*)cvec, 0, D_ * 2, 0x00020000);
  const v4u SENT = {~0u, ~0u, ~0u, ~0u};
  STAMPT(0);
  RUNPRIO();
  u32 cpf, mpf;
  u64 epoch;
  {
    u64 pc;
    u32 len;
    asm volatile("s_getpc_b64 %0\n.Lmega_start:\n\ts_mov_b32 %1, .Lmega_end-.Lmega_start" : "=s"(pc), "=s"(len));
    const v4u scode = mksrd((const void*)pc, len), swm0 = mksrd(wmod, D_ * 6 * D_ * 2), sws0 = mksrd(ws, 64);
    const int idx = slot * 84 + tid - 320, n = idx / 448, row = idx - 448 * n, j = xcd + 8 * n;
    asm volatile("s_nop 4\n\tbuffer_load_dword %0, %3, %5, 0 offen\n\tbuffer_load_dword %1, %4, %6, 0 offen\n\tbuffer_load_dwordx2 %2, off, %7, 0 sc1"
                 : "=&v"(cpf), "=&v"(mpf), "=&v"(epoch)
                 : "v"(tid >= 448 ? (lane * 8 + (slot & 7)) * 128 : OOB), "v"((tid >= 320 && tid < 404 && j < 42) ? (u32)(row * 5376 + j * 128) : OOB),
                   "s"(scode), "s"(swm0), "s"(sws0)
                 : "memory");
  }
  gu64* gr0 = (gu64*)(ws + WS_GRAN);
  gu64* gr = gr0 + xcd * G_REP;
  const int mj = xcd + 8 * (slot / 3);
  const bool modw = (slot % 3 == 0) && mj < 42 && w < 4;
  if (tid < 4) *(volatile LAS u32*)(lds + L_RED + 3072 + tid * 4) = 0u;
  v2u mr[4][8];
  u32 craw;
  if (modw) {
    const v4u swm = mksrd(wmod, D_ * 6 * D_ * 2), sc_ = mksrd(cvec, D_ * 2);
    asm volatile("s_nop 4\n\tbuffer_load_dword %0, %1, %2, 0 offen" : "=&v"(craw) : "v"(w + 4 * g < 14 ? (32 * (w + 4 * g) + 2 * l15) * 2 : OOB), "s"(sc_) : "memory");
#pragma unroll
    for (int i = 0; i < 4; ++i) {
      const u32 vo = (i < 3 || w < 2) ? (u32)(8 * g * 2688 + 4 * l15) * 2 : OOB;
      const u32 so = (u32)((32 * ((w + 4 * i) % 14)) * 2688 + 64 * mj) * 2;
      asm volatile(
          "s_nop 4\n\t"
          "buffer_load_dwordx2 %0, %8, %9, %10 offen\n\tbuffer_load_dwordx2 %1, %8, %9, %11 offen\n\t"
          "buffer_load_dwordx2 %2, %8, %9, %12 offen\n\tbuffer_load_dwordx2 %3, %8, %9, %13 offen\n\t"
          "buffer_load_dwordx2 %4, %8, %9, %14 offen\n\tbuffer_load_dwordx2 %5, %8, %9, %15 offen\n\t"
          "buffer_load_dwordx2 %6, %8, %9, %16 offen\n\tbuffer_load_dwordx2 %7, %8, %9, %17 offen"
          : "=&v"(mr[i][0]), "=&v"(mr[i][1]), "=&v"(mr[i][2]), "=&v"(mr[i][3]), "=&v"(mr[i][4]), "=&v"(mr[i][5]), "=&v"(mr[i][6]), "=&v"(mr[i][7])
          : "v"(vo), "s"(swm), "s"(so), "s"(so + 5376), "s"(so + 2 * 5376), "s"(so + 3 * 5376), "s"(so + 4 * 5376), "s"(so + 5 * 5376),
            "s"(so + 6 * 5376), "s"(so + 7 * 5376)
          : "memory");
    }
  }
  asm volatile("s_waitcnt lgkmcnt(0)\n\ts_barrier" ::: "memory");
  STAMPT(14);
  const v4u sq_srd = mksrd(wqkv, D_ * 3 * D_ * 2);
  const u32 vq = (u32)((lane >> 2) * 2688 + (((lane & 3) ^ (2 * (lane >> 5))) * 16));
#define WQDMA(i)                                                                                                        \
  {                                                                                                                     \
    const int u = w + 8 * ((i) >> 1), d = 2 * u + ((i) & 1), mat = u / 14, kb = d - 28 * mat;                           \
    dma16<false>(sq_srd, u < 42 ? vq : OOB, (u32)(16 * kb * 2688 + (mat % 3) * 896 + 56 * head), u < 42 ? L_WQ + d * 1024 : L_RED + 4096); \
  }
  {
    const v4u sx = mksrd(x, S_ * D_ * 2);
    const u32 vo = (u32)((lane >> 3) * D_ * 2 + (((lane & 7) ^ (lane >> 3)) * 16));
#pragma unroll
    for (int q = 0; q < 14; ++q) dma16<false>(sx, vo, (u32)((128 * g16 + 16 * w + 8 * (q & 1)) * D_ * 2 + (q >> 1) * 128), L_X + w * 14336 + q * 1024);
  }
  WQDMA(0) WQDMA(1) WQDMA(2) WQDMA(3)
  SB0();
  asm volatile("s_waitcnt vmcnt(18)" : "+v"(epoch), "+v"(cpf), "+v"(mpf)::"memory");
  const u64 tag = (epoch + 1) & 0xFFFFFFFFFFFFull;
  if (modw) {
    __builtin_amdgcn_s_setprio(3);
    asm volatile(""
                 : "+v"(craw), "+v"(mr[0][0]), "+v"(mr[0][1]), "+v"(mr[0][2]), "+v"(mr[0][3]), "+v"(mr[0][4]), "+v"(mr[0][5]), "+v"(mr[0][6]), "+v"(mr[0][7]),
                   "+v"(mr[1][0]), "+v"(mr[1][1]), "+v"(mr[1][2]), "+v"(mr[1][3]), "+v"(mr[1][4]), "+v"(mr[1][5]), "+v"(mr[1][6]), "+v"(mr[1][7])::"memory");
    asm volatile(""
                 : "+v"(mr[2][0]), "+v"(mr[2][1]), "+v"(mr[2][2]), "+v"(mr[2][3]), "+v"(mr[2][4]), "+v"(mr[2][5]), "+v"(mr[2][6]), "+v"(mr[2][7]),
                   "+v"(mr[3][0]), "+v"(mr[3][1]), "+v"(mr[3][2]), "+v"(mr[3][3]), "+v"(mr[3][4]), "+v"(mr[3][5]), "+v"(mr[3][6]), "+v"(mr[3][7])::"memory");
    float c0 = lo2f(craw), c1 = hi2f(craw);
    c0 = c0 * __builtin_amdgcn_rcpf(1.f + __expf(-c0));
    c1 = c1 * __builtin_amdgcn_rcpf(1.f + __expf(-c1));
    *(LAS u32*)(lds + L_RED + w * 256 + lane * 4) = pk(c0, c1);
    f4 acc[4] = {{0, 0, 0, 0}, {0, 0, 0, 0}, {0, 0, 0, 0}, {0, 0, 0, 0}};
#pragma unroll
    for (int i = 0; i < 4; ++i) {
      const v4u sf = LDS4(L_RED + w * 256 + i * 64 + g * 16);
      acc[0] = mfma((v4u){plo(mr[i][0].x, mr[i][1].x), plo(mr[i][2].x, mr[i][3].x), plo(mr[i][4].x, mr[i][5].x), plo(mr[i][6].x, mr[i][7].x)}, sf, acc[0]);
      acc[1] = mfma((v4u){phi(mr[i][0].x, mr[i][1].x), phi(mr[i][2].x, mr[i][3].x), phi(mr[i][4].x, mr[i][5].x), phi(mr[i][6].x, mr[i][7].x)}, sf, acc[1]);
      acc[2] = mfma((v4u){plo(mr[i][0].y, mr[i][1].y), plo(mr[i][2].y, mr[i][3].y), plo(mr[i][4].y, mr[i][5].y), plo(mr[i][6].y, mr[i][7].y)}, sf, acc[2]);
      acc[3] = mfma((v4u){phi(mr[i][0].y, mr[i][1].y), phi(mr[i][2].y, mr[i][3].y), phi(mr[i][4].y, mr[i][5].y), phi(mr[i][6].y, mr[i][7].y)}, sf, acc[3]);
    }
    if (l15 == 0) {
#pragma unroll
      for (int r = 0; r < 4; ++r) LDSF4(L_RED + 1024 + w * 256 + (16 * g + 4 * r) * 4) = (f4){acc[0][r], acc[1][r], acc[2][r], acc[3][r]};
    }
    asm volatile("s_waitcnt lgkmcnt(0)" ::: "memory");
    u32 old = 0;
    if (lane == 0) old = __hip_atomic_fetch_add((LAS u32*)(lds + L_RED + 3072), 1u, __ATOMIC_RELAXED, __HIP_MEMORY_SCOPE_WORKGROUP);
    if (__builtin_amdgcn_readfirstlane(old) == 3u) {
      CBAR();
      const float sum = (LDSF(L_RED + 1024 + lane * 4) + LDSF(L_RED + 1280 + lane * 4)) + (LDSF(L_RED + 1536 + lane * 4) + LDSF(L_RED + 1792 + lane * 4));
      const u64 gval = (tag << 16) | (u64)(pk(sum, 0.f) & 0xffffu);
#pragma unroll
      for (int r = 0; r < 8; ++r) __hip_atomic_store(gr0 + r * G_REP + 64 * mj + lane, gval, RLX_AGENT);
    }
    RUNPRIO();
  }
  STAMPT(15);
  if (modw) DRAIN(); else asm volatile("s_waitcnt vmcnt(4)" ::: "memory");
  STAMPT(16);
  v4u xr[14];
  {
    const u32 ra0 = L_X + w * 14336 + (l15 >> 3) * 1024 + (l15 & 7) * 128 + ((g ^ (l15 & 7)) * 16);
    const u32 ra1 = L_X + w * 14336 + (l15 >> 3) * 1024 + (l15 & 7) * 128 + (((4 + g) ^ (l15 & 7)) * 16);
#pragma unroll
    for (int s = 0; s < 14; ++s) xr[s] = LDS4(((s & 1) ? ra1 : ra0) + (s >> 1) * 2048);
#pragma unroll
    for (int s = 0; s < 14; ++s) PIN(xr[s]);
  }
  asm volatile("s_waitcnt lgkmcnt(0)\n\ts_barrier" ::: "memory");
  STAMPT(17);
  u32 pr[8];
  u32 pdst, pvo, pvo2, pso, pld;
  rsrc_t rp;
  {
    const int u = b * 8 + w;
    const u16* base; u32 bytes, ld, so, vo, vo2;
    if (u < 896) {
      const int cc = u / 112, v = u - 112 * cc, kb = v >> 1, hf = v & 1;
      base = w2; bytes = DFF_ * D_ * 2; ld = D_ * 2; so = (u32)(32 * kb * D_ + 56 * cc + 2 * hf) * 2;
      vo = l15 < 14 ? (u32)(8 * g * D_ + 4 * l15) * 2 : OOB; vo2 = vo;
      pdst = WS_W2T + ((cc * 56 + kb) * 4 + 2 * hf) * 1024;
    } else if (u < 1680) {
      const int v = u - 896, cb = v / 14, s = v - 14 * cb;
      base = w1; bytes = DFF_ * D_ * 2; ld = DFF_ * 2; so = (u32)(32 * s * DFF_ + 32 * cb) * 2;
      vo = (u32)(8 * g * DFF_ + 2 * l15) * 2; vo2 = vo;
      pdst = WS_W1T + (cb * 14 + s) * 2048;
    } else {
      const int v = u - 1680, hh = v & 15, nh = (v >> 4) & 1, cc = (v >> 5) & 7;
      base = wo; bytes = D_ * D_ * 2; ld = D_ * 2; so = (u32)(28 * hh * D_ + 56 * cc + 32 * nh) * 2;
      vo = (u < 1936 && (nh == 0 || l15 < 12)) ? (u32)(4 * g * D_ + 2 * l15) * 2 : OOB; vo2 = g < 3 ? vo + 12 * D_ * 2 : OOB;
      pdst = u < 1936 ? WS_WOT + (cc * 32 + nh * 16 + hh) * 2048 : OOB;
    }
    rp = __builtin_amdgcn_make_buffer_rsrc((void*)base, 0, bytes, 0x00020000);
    pvo = vo; pvo2 = vo2; pso = so; pld = ld;
  }
  const int mtB = w & 3, nhB = w >> 2;
  v4u xres;
  const int tau = 16 * (w & 1) + l15;
  const u32 koff = (w >> 1) * 2048 + ((tau >> 2) & 1) * 1024 + (g * 16 + 4 * (tau >> 3) + (tau & 3)) * 16;
  const u32 voff = (w >> 1) * 2048 + ((2 * (w & 1) + (g >> 1)) * 16 + l15) * 16 + (g & 1) * 8;
  const u32 kvb = g16 < 15 ? WS_KV + (g16 * 16 + head) * 16384 : OOB;
  u32 xh[14][8];
  {
    const v4u ONES = {0x3F803F80u, 0x3F803F80u, 0x3F803F80u, 0x3F803F80u};
    f4 sm = {0, 0, 0, 0}, gm = {0, 0, 0, 0};
#pragma unroll
    for (int s = 0; s < 14; ++s) {
      if (s == 0 || s == 7) WQDMA(4 + s / 7)
      sm = mfma(ONES, xr[s], sm);
      gm = mfma(xr[s], xr[s], gm);
    }
    float sq = (l15 & 2) ? ((l15 & 1) ? gm[3] : gm[2]) : ((l15 & 1) ? gm[1] : gm[0]);
    sq = __shfl(sq, 16 * (l15 >> 2) + l15);
    const float mean = sm[0] * (1.f / D_);
    const float rstd = __builtin_amdgcn_rsqf(fmaxf(sq * (1.f / D_) - mean * mean, 0.f) + 1e-5f);
    const f2 rs2 = {rstd, rstd}, nm2 = {-mean * rstd, -mean * rstd};
#pragma unroll
    for (int s = 0; s < 14; ++s) {
      if (s < 6) WQDMA(6 + s)
      else if (s < 10) { pr[2 * s - 12] = ld1(rp, s < 8 ? pvo : pvo2, pso + (2 * s - 12) * pld); pr[2 * s - 11] = ld1(rp, s < 8 ? pvo : pvo2, pso + (2 * s - 11) * pld); }
      else if (s == 10) xres = ld4(rx, (nhB == 1 && g == 3) ? OOB : (u32)(l15 * D_ + 8 * g) * 2, (u32)((64 * g32 + 16 * mtB) * D_ + 56 * c8 + 32 * nhB) * 2);
      else if (s == 11) st4s(rws, koff, kvb, SENT);
      else if (s == 12) st4s(rws, (w >> 1) * 2048 + (w & 1) * 512 + (lane & 31) * 16 + (lane >> 5) * 1024, kvb + 8192, SENT);
      else st4s(rws, lane * 16, WS_O + ((8 * g16 + w) * 16 + head) * 1024, SENT);
#pragma unroll
      for (int j = 0; j < 4; ++j) {
        const f2 v = __builtin_elementwise_fma((f2){lo2f(xr[s][j]), hi2f(xr[s][j])}, rs2, nm2);
        xh[s][2 * j] = pk(v.x, 1.f);
        xh[s][2 * j + 1] = pk(v.y, 1.f);
        PIN(xh[s][2 * j]); PIN(xh[s][2 * j + 1]);
      }
      SB0();
    }
  }
  STAMPT(20);
  const int i1 = tid < 384 ? 512 + tid : tid;
  u64 g10 = __hip_atomic_load(gr + tid, RLX_AGENT), g11 = __hip_atomic_load(gr + i1, RLX_AGENT);
  asm volatile("s_waitcnt vmcnt(14)" ::: "memory");
  {
    LAS char* tb0 = lds + L_WQ + (8 * g + (l15 >> 2)) * 64 + (g & 1) * 32 + (l15 & 3) * 8;
    LAS char* tb1 = lds + L_WQ + (8 * g + (l15 >> 2)) * 64 + ((g & 1) ^ 1) * 32 + (l15 & 3) * 8;
    v2u r[6][4];
#pragma unroll
    for (int i = 0; i < 6; ++i) {
      const int u = (i < 5 || w < 2) ? w + 8 * i : w;
      r[i][0] = trrd(tb0 + u * 2048); r[i][1] = trrd(tb0 + u * 2048 + 256);
      r[i][2] = trrd(tb1 + u * 2048); r[i][3] = trrd(tb1 + u * 2048 + 256);
    }
    SB0();
#pragma unroll
    for (int i = 0; i < 6; ++i) {
      const int u = (i < 5 || w < 2) ? w + 8 * i : w;
      LDS4(L_WQ + u * 2048 + lane * 16) = (v4u){r[i][0].x, r[i][0].y, r[i][1].x, r[i][1].y};
      LDS4(L_WQ + u * 2048 + 1024 + lane * 16) = (v4u){r[i][2].x, r[i][2].y, r[i][3].x, r[i][3].y};
    }
  }
  v4u pe = {plo(pr[0], pr[1]), plo(pr[2], pr[3]), plo(pr[4], pr[5]), plo(pr[6], pr[7])};
  v4u po = {phi(pr[0], pr[1]), phi(pr[2], pr[3]), phi(pr[4], pr[5]), phi(pr[6], pr[7])};
  PIN(pe); PIN(po);
  STAMPT(2);
  {
    for (unsigned spins = 0;; ++spins) {
      const bool ok = (g10 >> 16) == tag && (g11 >> 16) == tag;
      if (__all(ok) || spins > SPIN_CAP) break;
      BACKOFF();
      g10 = __hip_atomic_load(gr + tid, RLX_AGENT);
      g11 = __hip_atomic_load(gr + i1, RLX_AGENT);
    }
    RUNPRIO();
    if (tid < 448) LDSH(L_AS1 + tid * 4 + 2) = (u16)g10;
    else LDSH(L_AS1 + (tid - 448) * 4) = (u16)pk(1.f + lo2f((u32)g10), 0.f);
    if (tid < 384) LDSH(L_AS1 + (64 + tid) * 4) = (u16)pk(1.f + lo2f((u32)g11), 0.f);
  }
  DRAIN();
  __syncthreads();
  if (tid < 8) __hip_atomic_store(gr0 + tid * G_REP + G_FILL + b, tag << 16, RLX_AGENT);
  STAMPT(3);
  const int jg = tid - 448;
  const int i20 = tid < 448 ? 1344 + tid : 896 + 56 * c8 + (jg < 56 ? jg : 0);
  const int i21 = tid < 448 ? 1792 + tid : 2240 + 56 * c8 + (jg < 56 ? jg : 0);
  u64 g20 = __hip_atomic_load(gr + i20, RLX_AGENT), g21 = __hip_atomic_load(gr + i21, RLX_AGENT);

  v4u hfr[14];
  {
    v4u as[3][2];
#pragma unroll
    for (int s = 0; s < 2; ++s) { as[s][0] = LDS4(L_AS1 + (32 * s + 8 * g) * 4); as[s][1] = LDS4(L_AS1 + (32 * s + 8 * g) * 4 + 16); }
#pragma unroll
    for (int s = 0; s < 14; ++s) {
      if (s < 12) { as[(s + 2) % 3][0] = LDS4(L_AS1 + (32 * (s + 2) + 8 * g) * 4); as[(s + 2) % 3][1] = LDS4(L_AS1 + (32 * (s + 2) + 8 * g) * 4 + 16); }
      SB0();
      hfr[s] = modulate(xh[s], as[s % 3][0], as[s % 3][1]);
      SB0();
    }
  }
  STAMPT(29);
  int dep = b;
  if (lane < 16) dep = xcd + 8 * (16 * (g16 & 1) + lane);
  else if (lane == 16) dep = g16 >= 1 ? ((g16 - 1) >> 1) + 8 * (16 * ((g16 - 1) & 1) + head) : b;
  else if (lane == 17) dep = g16 >= 2 ? ((g16 - 2) >> 1) + 8 * (16 * (g16 & 1) + head) : b;
  else if (lane < 32) dep = 14 * c8 + (lane - 18);
  else if (lane < 46) dep = ((896 + 98 * c8) >> 3) + (lane - 32);
  else if (lane < 50) dep = 210 + 4 * c8 + (lane - 46);
  u64 dfl = tag << 16;
  f4 aq[6];
#pragma unroll
  for (int t = 0; t < 6; ++t) aq[t] = (f4){0, 0, 0, 0};
  {
    v4u fb[3][6];
#define QLOAD(s, d) { _Pragma("unroll") for (int t = 0; t < 6; ++t) fb[d][t] = LDS4(L_WQ + ((t >> 1) * 14 + (s)) * 2048 + (t & 1) * 1024 + lane * 16); }
    QLOAD(0, 0)
    QLOAD(1, 1)
#pragma unroll
    for (int s = 0; s < 14; ++s) {
      if (s < 12) QLOAD(s + 2, (s + 2) % 3)
      if (s == 0) st4s(rws, lane * 16, pdst, pe);
      if (s == 2) st4s(rws, lane * 16, pdst + 1024, po);
      if (s == 4) st4s(rws, (w < 4 || lane < 48) ? lane * 16 : OOB, WS_X1 + (4 * g32 + (w & 3)) * 14336 + c8 * 1792 + (w >> 2) * 1024, SENT);
      if (s == 5) st4s(rws, (w < 4 && lane < 16) ? lane * 16 : OOB, WS_ST + (4 * g32 + (w & 3)) * 2048 + c8 * 256, SENT);
      if (s == 8 && w == 0) dfl = __hip_atomic_load(gr + G_FILL + (lane < 18 ? dep : b), RLX_AGENT);
      SB0();
#pragma unroll
      for (int t = 0; t < 4; ++t) aq[t] = mfma(fb[s % 3][t], hfr[s], aq[t]);
#pragma unroll
      for (int t = 4; t < 6; ++t) aq[t] = mfma(hfr[s], fb[s % 3][t], aq[t]);
      SB0();
    }
  }
  STAMPT(30);
  const v4u qf = {pk(aq[0][0], aq[0][1]), pk(aq[0][2], aq[0][3]), g < 3 ? pk(aq[1][0], aq[1][1]) : 0u, g < 3 ? pk(aq[1][2], aq[1][3]) : 0u};
  {
    const v4u kf = nosent4((v4u){pk(aq[2][0], aq[2][1]), pk(aq[2][2], aq[2][3]), pk(aq[3][0], aq[3][1]), pk(aq[3][2], aq[3][3])});
    v2u ve = {pk(aq[4][0], aq[4][1]), pk(aq[4][2], aq[4][3])}, vo_ = {pk(aq[5][0], aq[5][1]), pk(aq[5][2], aq[5][3])};
    if (l15 == 12) vo_ = (v2u){0x3F803F80u, 0x3F803F80u};
    ve.x = nosent(ve.x); ve.y = nosent(ve.y); vo_.x = nosent(vo_.x); vo_.y = nosent(vo_.y);
    {
      DRAIN();
      u32 old = 0;
      if (lane == 0) old = __hip_atomic_fetch_add((LAS u32*)(lds + L_RED + 3080), 1u, __ATOMIC_RELAXED, __HIP_MEMORY_SCOPE_WORKGROUP);
      if (__builtin_amdgcn_readfirstlane(old) == 7u && lane < 8) __hip_atomic_store(gr0 + lane * G_REP + G_PREP + b, tag << 16, RLX_AGENT);
    }
    st4s(rws, koff, kvb, kf);
    st2s(rws, voff, kvb + 8192, ve);
    st2s(rws, voff, kvb + 8192 + 1024, vo_);
    STAMPT(31);
    for (unsigned spins = 0;; ++spins) {
      bool ok = (g20 >> 16) == tag && (g21 >> 16) == tag;
      ok &= (dfl >> 16) == tag;
      if (__all(ok) || spins > SPIN_CAP) break;
      BACKOFF();
      if (w == 0) dfl = __hip_atomic_load(gr + G_FILL + (lane < 18 ? dep : b), RLX_AGENT);
      g20 = __hip_atomic_load(gr + i20, RLX_AGENT);
      g21 = __hip_atomic_load(gr + i21, RLX_AGENT);
    }
    RUNPRIO();
    if (tid < 448) {
      LDSH(L_SB + tid * 2) = (u16)g20;
      LDSH(L_A2 + tid * 2) = (u16)pk(1.f + lo2f((u32)g21), 0.f);
    } else {
      LDSF(L_GA + jg * 4) = lo2f((u32)g20);
      LDSF(L_GB + jg * 4) = lo2f((u32)g21);
    }
    STAMPT(21);
    __syncthreads();
    STAMPT(4);
    LDS4(L_K + 16384 + koff) = kf;
    LDS2(L_V + 16384 + voff) = ve;
    LDS2(L_V + 16384 + 1024 + voff) = vo_;
  }
  STAMPT(22);
  v4u w1f[14][2];
  const u32 vo1 = w < 7 ? lane * 16 : OOB;
  const v4u sws = mksrd(ws, WS_END);
  {
    const u32 b0 = g16 >= 2 ? WS_KV + ((g16 - 2) * 16 + head) * 16384 : OOB;
    const u32 b1 = g16 >= 1 ? WS_KV + ((g16 - 1) * 16 + head) * 16384 : OOB;
    v4u k0, v0, k1, v1;
    for (unsigned spins = 0;; ++spins) {
      CBAR();
      k0 = ld4s(rws, tid * 16, b0); v0 = ld4s(rws, tid * 16, b0 + 8192);
      k1 = ld4s(rws, tid * 16, b1); v1 = ld4s(rws, tid * 16, b1 + 8192);
      const bool bad = chk(chk(chk(chk(0u, k0), v0), k1), v1) == ~0u;
      if (!__any(bad) || spins > SPIN_CAP) break;
      {
        const u32 pb = (lane & 16) ? b1 : b0;
        canary(rws, lane < 32 ? pb + ((lane >> 3) & 1) * 8192 + w * 1024 + (lane & 7) * 128 : OOB, 0, spins);
      }
    }
    RUNPRIO();
    STAMPT(24);
    LDS4(L_K + tid * 16) = k0; LDS4(L_V + tid * 16) = v0;
    LDS4(L_K + 8192 + tid * 16) = k1; LDS4(L_V + 8192 + tid * 16) = v1;
  }
  if (w == 0) {
    for (unsigned spins = 0;; ++spins) {
      if (__all((__hip_atomic_load(gr + G_PREP + (lane >= 18 ? dep : b), RLX_AGENT) >> 16) == tag) || spins > SPIN_CAP) break;
      BACKOFF();
    }
    RUNPRIO();
  }
  DRAIN();
  __syncthreads();
  STAMPT(5);
  {
    const int blk0 = w >> 1;
    f4 sc[18];
    {
      v4u kf[18];
#pragma unroll
      for (int t = 0; t < 6; ++t) kf[t] = LDS4(L_K + (blk0 + (t >> 1)) * 2048 + (t & 1) * 1024 + lane * 16);
#pragma unroll
      for (int t = 0; t < 18; ++t) {
        if (t + 6 < 18) kf[t + 6] = LDS4(L_K + (blk0 + ((t + 6) >> 1)) * 2048 + (t & 1) * 1024 + lane * 16);
        if ((t & 1) && t < 16) dma16<true>(sws, lane * 16, WS_WOT + c8 * 65536 + (w * 8 + (t >> 1)) * 1024, L_WO + (w * 8 + (t >> 1)) * 1024);
        SB0();
        sc[t] = mfma(kf[t], qf, (f4){0, 0, 0, 0});
        SB0();
      }
    }
    STAMPT(25);
    v4u vf[18];
#pragma unroll
    for (int t = 0; t < 4; ++t) vf[t] = LDS4(L_V + (blk0 + (t >> 1)) * 2048 + (t & 1) * 1024 + lane * 16);
    const int ql = 256 + 16 * w + l15;
    const int kmin = (256 - 128 * g16) > (ql - 255) ? (256 - 128 * g16) : (ql - 255);
    const int kb0 = 32 * blk0 + 8 * g;
    const float NEG = -1e30f;
    if (g16 < 2) {
#pragma unroll
      for (int t = 2; t < 16; ++t)
#pragma unroll
        for (int r = 0; r < 4; ++r) sc[t][r] = (kb0 + 32 * (t >> 1) + 4 * (t & 1) + r) >= kmin ? sc[t][r] : NEG;
    }
#pragma unroll
    for (int t = 0; t < 2; ++t)
#pragma unroll
      for (int r = 0; r < 4; ++r) sc[t][r] = (kb0 + 4 * t + r) >= kmin ? sc[t][r] : NEG;
#pragma unroll
    for (int t = 16; t < 18; ++t)
#pragma unroll
      for (int r = 0; r < 4; ++r) sc[t][r] = (kb0 + 256 + 4 * (t & 1) + r) <= ql ? sc[t][r] : NEG;
    float mx = NEG;
#pragma unroll
    for (int t = 0; t < 18; ++t) mx = fmaxf(fmaxf(mx, sc[t][0]), fmaxf(sc[t][1], fmaxf(sc[t][2], sc[t][3])));
    mx = fmaxf(mx, __shfl_xor(mx, 16));
    mx = fmaxf(mx, __shfl_xor(mx, 32));
    STAMPT(26);
    STAMPT(18);
    const float cs = 0.18898223650461362f * 1.4426950408889634f;
    const float mc = mx * cs;
    f4 oe = {0, 0, 0, 0}, oo = {0, 0, 0, 0};
#pragma unroll
    for (int bb = 0; bb < 9; ++bb) {
      if (bb + 2 < 9) {
        vf[2 * bb + 4] = LDS4(L_V + (blk0 + bb + 2) * 2048 + lane * 16);
        vf[2 * bb + 5] = LDS4(L_V + (blk0 + bb + 2) * 2048 + 1024 + lane * 16);
      }
      w1f[bb >> 1][bb & 1] = ld4s(rws, vo1, WS_W1T + (7 * c8 + (w % 7)) * 28672 + bb * 1024);
      if ((bb & 1) == 0 && bb < 8) st4s(rws, vo1, WS_HID + (4 * g32 + (bb >> 1)) * 57344 + c8 * 7168 + (w % 7) * 1024, SENT);
      SB0();
      float p[8];
#pragma unroll
      for (int r = 0; r < 4; ++r) {
        p[r] = __builtin_amdgcn_exp2f(sc[2 * bb][r] * cs - mc);
        p[4 + r] = __builtin_amdgcn_exp2f(sc[2 * bb + 1][r] * cs - mc);
      }
      const v4u pf = {pk(p[0], p[1]), pk(p[2], p[3]), pk(p[4], p[5]), pk(p[6], p[7])};
      oe = mfma(vf[2 * bb], pf, oe);
      oo = mfma(vf[2 * bb + 1], pf, oo);
      SB0();
    }
    const float den = __shfl(oo[0], 48 + l15);
    const float inv = __builtin_amdgcn_rcpf(den);
    const v4u of = nosent4((v4u){pk(oe[0] * inv, oe[1] * inv), pk(oe[2] * inv, oe[3] * inv), pk(oo[0] * inv, oo[1] * inv), pk(oo[2] * inv, oo[3] * inv)});
    st4s(rws, lane * 16, WS_O + ((8 * g16 + w) * 16 + head) * 1024, of);
  }
  STAMPT(6);
  SB0();
  asm volatile("s_waitcnt vmcnt(14)" ::: "memory");
#pragma unroll
  for (int i = 9; i < 17; ++i) w1f[i >> 1][i & 1] = ld4s(rws, vo1, WS_W1T + (7 * c8 + (w % 7)) * 28672 + i * 1024);
  STAMPT(19);
  STAMPT(27);
  __syncthreads();
  STAMPT(28);

  {
    v4u of[8];
    const u32 base = WS_O + (4 * g32 + mtB) * 16384 + nhB * 8192;
    v4u wpre[4][4];
#pragma unroll
    for (int h = 0; h < 4; ++h)
#pragma unroll
      for (int t = 0; t < 4; ++t) { wpre[h][t] = LDS4(L_WO + ((t >> 1) * 16 + nhB * 8 + h) * 2048 + (t & 1) * 1024 + lane * 16); PIN(wpre[h][t]); }
    for (unsigned spins = 0;; ++spins) {
      CBAR();
      u32 m = 0;
#pragma unroll
      for (int h = 0; h < 8; ++h) of[h] = ld4s(rws, lane * 16, base + h * 1024);
#pragma unroll
      for (int h = 0; h < 8; ++h) m = chk(m, of[h]);
      if (!__any(m == ~0u) || spins > SPIN_CAP) break;
      canary(rws, lane < 8 ? lane * 1024 : OOB, base, spins);
    }
    RUNPRIO();
    DRAIN();
    STAMPT(7);
    f4 acc[4] = {{0, 0, 0, 0}, {0, 0, 0, 0}, {0, 0, 0, 0}, {0, 0, 0, 0}};
#pragma unroll
    for (int h = 0; h < 8; ++h) {
#pragma unroll
      for (int t = 0; t < 4; ++t) acc[t] = mfma(wpre[h & 3][t], of[h], acc[t]);
      SB0();
      if (h < 4) {
#pragma unroll
        for (int t = 0; t < 4; ++t) wpre[h][t] = LDS4(L_WO + ((t >> 1) * 16 + nhB * 8 + h + 4) * 2048 + (t & 1) * 1024 + lane * 16);
      }
      if (h >= 2) {
#pragma unroll
        for (int i = 17 + 2 * (h - 2); i < 19 + 2 * (h - 2) && i < 28; ++i) w1f[i >> 1][i & 1] = ld4s(rws, vo1, WS_W1T + (7 * c8 + (w % 7)) * 28672 + i * 1024);
      }
      SB0();
    }
    LDSF4(L_PR + w * 2048 + lane * 16) = nhB ? acc[0] : acc[2];
    LDSF4(L_PR + w * 2048 + 1024 + lane * 16) = nhB ? acc[1] : acc[3];
    __syncthreads();
    const f4 e = (nhB ? acc[2] : acc[0]) + LDSF4(L_PR + (w ^ 4) * 2048 + lane * 16);
    const f4 o = (nhB ? acc[3] : acc[1]) + LDSF4(L_PR + (w ^ 4) * 2048 + 1024 + lane * 16);
    const f4 g0 = LDSF4(L_GA + (32 * nhB + 8 * g) * 4), g1 = LDSF4(L_GA + (32 * nhB + 8 * g) * 4 + 16);
    const v4u a2 = LDS4(L_A2 + (56 * c8 + 32 * nhB + 8 * g) * 2);
    v4u x1;
    x1.x = pk(lo2f(xres.x) + g0.x * e[0], hi2f(xres.x) + g0.y * o[0]);
    x1.y = pk(lo2f(xres.y) + g0.z * e[1], hi2f(xres.y) + g0.w * o[1]);
    x1.z = pk(lo2f(xres.z) + g1.x * e[2], hi2f(xres.z) + g1.y * o[2]);
    x1.w = pk(lo2f(xres.w) + g1.z * e[3], hi2f(xres.w) + g1.w * o[3]);
    v4u y;
#pragma unroll
    for (int j = 0; j < 4; ++j) y[j] = nosent(pk(lo2f(x1[j]) * lo2f(a2[j]), hi2f(x1[j]) * hi2f(a2[j])));
    const bool vld = !(nhB == 1 && g == 3);
    st4s(rws, vld ? lane * 16 : OOB, WS_X1 + (4 * g32 + mtB) * 14336 + (7 * c8 + 4 * nhB) * 256, y);
    float s1 = 0.f, s2 = 0.f;
#pragma unroll
    for (int j = 0; j < 4; ++j) { s1 = dot2(x1[j], 0x3F803F80u, s1); s2 = dot2(x1[j], x1[j], s2); }
    s1 += __shfl_xor(s1, 16); s2 += __shfl_xor(s2, 16);
    s1 += __shfl_xor(s1, 32); s2 += __shfl_xor(s2, 32);
    st2s(rws, g == 0 ? l15 * 8 : OOB, WS_ST + (4 * g32 + mtB) * 2048 + (2 * c8 + nhB) * 128,
         (v2u){nosent(__builtin_bit_cast(u32, s1)), nosent(__builtin_bit_cast(u32, s2))});
    if (vld) LDS4(L_X1S + (mtB * 7 + 4 * nhB) * 256 + lane * 16) = x1;
  }
  STAMPT(8);
  f4 au[2] = {{0, 0, 0, 0}, {0, 0, 0, 0}}, av[2] = {{0, 0, 0, 0}, {0, 0, 0, 0}};
#pragma unroll
  for (int s = 0; s < 14; ++s) {
    const v4u a2 = LDS4(L_A2 + (32 * s + 8 * g) * 2), sb = LDS4(L_SB + (32 * s + 8 * g) * 2);
    au[0] = mfma(w1f[s][0], a2, au[0]); au[1] = mfma(w1f[s][1], a2, au[1]);
    av[0] = mfma(w1f[s][0], sb, av[0]); av[1] = mfma(w1f[s][1], sb, av[1]);
  }
  PIN(au[0]); PIN(au[1]); PIN(av[0]); PIN(av[1]);
  STAMPT(23);
  {
    v4u t[7], st[2];
    const u32 base = WS_X1 + 4 * g32 * 14336 + w * 7168;
    const u32 sto = w < 4 ? lane * 16 : OOB;
    for (unsigned spins = 0;; ++spins) {
      CBAR();
      u32 m = 0;
#pragma unroll
      for (int i = 0; i < 7; ++i) t[i] = ld4s(rws, lane * 16, base + i * 1024);
      st[0] = ld4s(rws, sto, WS_ST + (4 * g32 + (w & 3)) * 2048);
      st[1] = ld4s(rws, sto, WS_ST + (4 * g32 + (w & 3)) * 2048 + 1024);
#pragma unroll
      for (int i = 0; i < 7; ++i) m = chk(m, t[i]);
      m = chk(chk(m, st[0]), st[1]);
      if (!__any(m == ~0u) || spins > SPIN_CAP) break;
      canary(rws, lane < 28 ? base + lane * 256 : ((lane >= 32 && lane < 48 && w < 4) ? WS_ST + (4 * g32 + w) * 2048 + (lane - 32) * 128 : OOB), 0, spins);
    }
    RUNPRIO();
#pragma unroll
    for (int i = 0; i < 7; ++i) LDS4(w * 7168 + i * 1024 + lane * 16) = t[i];
    if (w < 4) {
      f4 a = __builtin_bit_cast(f4, st[0]) + __builtin_bit_cast(f4, st[1]);
#pragma unroll
      for (int j = 0; j < 4; ++j) { a[j] += dppf<0x128>(a[j]); a[j] += __shfl_xor(a[j], 16); a[j] += __shfl_xor(a[j], 32); }
      const float m0 = a.x * (1.f / D_), m1 = a.z * (1.f / D_);
      const float r0 = __builtin_amdgcn_rsqf(fmaxf(a.y * (1.f / D_) - m0 * m0, 0.f) + 1e-5f);
      const float r1 = __builtin_amdgcn_rsqf(fmaxf(a.w * (1.f / D_) - m1 * m1, 0.f) + 1e-5f);
      if (lane < 8) LDSF4(L_MR + w * 128 + lane * 16) = (f4){m0, r0, m1, r1};
    }
    __syncthreads();
  }
  STAMPT(9);
  v4u wf[7][4];
#pragma unroll
  for (int mp = 0; mp < 2; ++mp) {
    f4 acc[2][2] = {{{0, 0, 0, 0}, {0, 0, 0, 0}}, {{0, 0, 0, 0}, {0, 0, 0, 0}}};
    v4u xb[3][2];
#pragma unroll
    for (int m = 0; m < 2; ++m) { xb[0][m] = LDS4((2 * mp + m) * 14336 + lane * 16); xb[1][m] = LDS4((2 * mp + m) * 14336 + 1024 + lane * 16); }
#pragma unroll
    for (int s = 0; s < 14; ++s) {
      if (s < 12) {
#pragma unroll
        for (int m = 0; m < 2; ++m) xb[(s + 2) % 3][m] = LDS4((2 * mp + m) * 14336 + (s + 2) * 1024 + lane * 16);
      }
      SB0();
#pragma unroll
      for (int m = 0; m < 2; ++m) {
        acc[m][0] = mfma(w1f[s][0], xb[s % 3][m], acc[m][0]);
        acc[m][1] = mfma(w1f[s][1], xb[s % 3][m], acc[m][1]);
      }
      SB0();
      wf[(14 * mp + s) >> 2][(14 * mp + s) & 3] = ld4s(rws, lane * 16, WS_W2T + ((c8 * 56 + 7 * w) * 4 + 14 * mp + s) * 1024);
      SB0();
    }
#pragma unroll
    for (int m = 0; m < 2; ++m) {
      const f2 mr2 = *(LAS f2*)(lds + L_MR + ((2 * mp + m) * 16 + l15) * 8);
      const f2 nm = {-mr2.x, -mr2.x}, rs = {mr2.y, mr2.y};
      v4u hv;
#pragma unroll
      for (int r = 0; r < 4; ++r) {
        f2 h = __builtin_elementwise_fma(nm, (f2){au[0][r], au[1][r]}, (f2){acc[m][0][r], acc[m][1][r]});
        h = __builtin_elementwise_fma(rs, h, (f2){av[0][r], av[1][r]});
        hv[r] = pk(fmaxf(h.x, 0.f), fmaxf(h.y, 0.f));
      }
      st4s(rws, w < 7 ? lane * 16 : OOB, WS_HID + (4 * g32 + 2 * mp + m) * 57344 + (28 * c8 + 4 * (w % 7)) * 256, hv);
    }
    SB0();
  }
  STAMPT(10);
  __syncthreads();
  {
    v4u hf[2][2][7];
#define HLOAD(mp) { _Pragma("unroll") for (int i = 0; i < 2; ++i) _Pragma("unroll") for (int s = 0; s < 7; ++s) \
      hf[mp][i][s] = ld4s(rws, lane * 16, WS_HID + (4 * g32 + 2 * (mp) + i) * 57344 + w * 7168 + s * 1024); }
    HLOAD(0)
#pragma unroll
    for (int mp = 0; mp < 2; ++mp) {
      for (unsigned spins = 0;; ++spins) {
        u32 m = 0;
#pragma unroll
        for (int i = 0; i < 2; ++i)
#pragma unroll
          for (int s = 0; s < 7; ++s) m = chk(m, hf[mp][i][s]);
        if (!__any(m == ~0u) || spins > SPIN_CAP) break;
        canary(rws, (lane & 7) < 7 && lane < 16 ? (lane >> 3) * 57344 + (lane & 7) * 1024 : OOB, WS_HID + (4 * g32 + 2 * mp) * 57344 + w * 7168, spins);
        CBAR();
        HLOAD(mp)
      }
      RUNPRIO();
      if (mp == 0) STAMPT(11);
#pragma unroll
      for (int i = 0; i < 2; ++i) {
        f4 acc[4] = {{0, 0, 0, 0}, {0, 0, 0, 0}, {0, 0, 0, 0}, {0, 0, 0, 0}};
#pragma unroll
        for (int s = 0; s < 7; ++s)
#pragma unroll
          for (int t = 0; t < 4; ++t) acc[t] = mfma(wf[s][t], hf[mp][i][s], acc[t]);
#pragma unroll
        for (int r = 0; r < 4; ++r) LDSF4(((w * 4 + 2 * mp + i) * 4 + r) * 1024 + lane * 16) = (f4){acc[0][r], acc[1][r], acc[2][r], acc[3][r]};
        if (mp == 0 && i == 0) { SB0(); HLOAD(1) SB0(); }
      }
    }
  }
  __syncthreads();
  STAMPT(12);
  {
    const int mt = tid >> 7, rp = (tid >> 6) & 1;
    if (!(g == 3 && rp == 1)) {
      f4 s0 = {0, 0, 0, 0}, s1 = {0, 0, 0, 0};
#pragma unroll
      for (int ww = 0; ww < 8; ++ww) {
        s0 += LDSF4(((ww * 4 + mt) * 4 + 2 * rp) * 1024 + lane * 16);
        s1 += LDSF4(((ww * 4 + mt) * 4 + 2 * rp + 1) * 1024 + lane * 16);
      }
      const v4u x1 = LDS4(L_X1S + (mt * 7 + 2 * g + rp) * 256 + l15 * 16);
      const f4 g0 = LDSF4(L_GB + (16 * g + 8 * rp) * 4), g1 = LDSF4(L_GB + (16 * g + 8 * rp) * 4 + 16);
      v4u o;
      o.x = pk(lo2f(x1.x) + g0.x * s0.x, hi2f(x1.x) + g0.y * s0.y);
      o.y = pk(lo2f(x1.y) + g0.z * s0.z, hi2f(x1.y) + g0.w * s0.w);
      o.z = pk(lo2f(x1.z) + g1.x * s1.x, hi2f(x1.z) + g1.y * s1.y);
      o.w = pk(lo2f(x1.w) + g1.z * s1.z, hi2f(x1.w) + g1.w * s1.w);
      *(v4u*)(out + (size_t)(64 * g32 + 16 * mt + l15) * D_ + 56 * c8 + 16 * g + 8 * rp) = o;
    }
  }
  if (b == 0 && w == 1) {
    for (unsigned spins = 0;; ++spins) {
      bool ok = true;
#pragma unroll
      for (int i = 0; i < 4; ++i) ok &= (__hip_atomic_load(gr + G_PREP + lane + 64 * i, RLX_AGENT) >> 16) == tag;
      if (__all(ok) || spins > SPIN_CAP) break;
      BACKOFF();
    }
    if (lane == 0) __hip_atomic_store((gu64*)ws, epoch + 1, RLX_AGENT);
  }
  STAMPT(13);
  asm volatile(".Lmega_end:");
}

extern "C" void kernel_launch(void* const* d_in, const int* in_sizes, int n_in, void* d_out, int out_size, void* d_ws,
                              size_t ws_size, hipStream_t stream) {
  (void)hipFuncSetAttribute((const void*)mega, hipFuncAttributeMaxDynamicSharedMemorySize, LDS_BYTES);
  mega<<<256, 512, LDS_BYTES, stream>>>((const u16*)d_in[0], (const u16*)d_in[1], (const u16*)d_in[2], (const u16*)d_in[4],
                                        (const u16*)d_in[5], (const u16*)d_in[6], (const u16*)d_in[7], (u16*)d_out, (char*)d_ws);
}
```

```cpp
#include <hip/hip_runtime.h>
#include <stdint.h>

typedef unsigned int u32;
typedef unsigned long long u64;
typedef unsigned short u16;
typedef u32 v4u __attribute__((ext_vector_type(4)));
typedef u32 v2u __attribute__((ext_vector_type(2)));
typedef float f4 __attribute__((ext_vector_type(4)));
typedef float f2 __attribute__((ext_vector_type(2)));
typedef __bf16 bf2v __attribute__((ext_vector_type(2)));
typedef __bf16 bf8v __attribute__((ext_vector_type(8)));
#define GAS __attribute__((address_space(1)))
#define LAS __attribute__((address_space(3)))
typedef GAS u64 gu64;
#define RLX_AGENT __ATOMIC_RELAXED, __HIP_MEMORY_SCOPE_AGENT
typedef __amdgpu_buffer_rsrc_t rsrc_t;

constexpr int S_ = 2048, D_ = 448, DFF_ = 1792;
constexpr int WS_GRAN = 4096;
constexpr int G_PREP = 2688, G_FILL = 2944;
constexpr int G_REP = 4096;
constexpr int WS_KV = 524288;
constexpr int WS_O = WS_KV + 4194304;
constexpr int WS_X1 = WS_O + 2097152;
constexpr int WS_ST = WS_X1 + 1835008;
constexpr int WS_HID = WS_ST + 262144;
constexpr int WS_W2T = WS_HID + 7340032;
constexpr int WS_W1T = WS_W2T + 1835008;
constexpr int WS_WOT = WS_W1T + 1605632;
constexpr int WS_END = WS_WOT + 524288;
constexpr int WS_DBG = WS_END;
constexpr int L_WQ = 0;
constexpr int L_K = 0, L_V = 24576;
constexpr int L_PR = 57344;
constexpr int L_WO = 86016;
constexpr int L_X = 36864;
constexpr int L_UV = L_WO;
constexpr int L_SM = 151552;
constexpr int L_AS1 = L_SM;
constexpr int L_A2 = L_AS1 + 1792;
constexpr int L_SB = L_A2 + 896;
constexpr int L_GA = L_SB + 896;
constexpr int L_GB = L_GA + 256;
constexpr int L_MR = L_GB + 256;
constexpr int L_RED = L_MR + 512;
constexpr int L_X1S = L_RED;
constexpr int L_END = L_X1S + 7168;
static_assert(L_END <= 163840, "lds");
constexpr int LDS_BYTES = 163840;
constexpr u32 OOB = 0x80000000u;
constexpr unsigned SPIN_CAP = 1u << 15;

__device__ __forceinline__ u32 pk(float lo, float hi) { f2 v = {lo, hi}; return __builtin_bit_cast(u32, __builtin_convertvector(v, bf2v)); }
__device__ __forceinline__ float dot2(u32 a, u32 b, float c) { return __builtin_amdgcn_fdot2_f32_bf16(__builtin_bit_cast(bf2v, a), __builtin_bit_cast(bf2v, b), c, false); }
__device__ __forceinline__ f4 mfma(v4u a, v4u b, f4 c) { return __builtin_amdgcn_mfma_f32_16x16x32_bf16(__builtin_bit_cast(bf8v, a), __builtin_bit_cast(bf8v, b), c, 0, 0, 0); }
__device__ __forceinline__ float lo2f(u32 v) { return __builtin_bit_cast(float, v << 16); }
__device__ __forceinline__ float hi2f(u32 v) { return __builtin_bit_cast(float, v & 0xffff0000u); }
__device__ __forceinline__ u32 plo(u32 r0, u32 r1) { return __builtin_amdgcn_perm(r1, r0, 0x05040100u); }
__device__ __forceinline__ u32 phi(u32 r0, u32 r1) { return __builtin_amdgcn_perm(r1, r0, 0x07060302u); }
__device__ __forceinline__ u32 nosent(u32 v) { return v < 0xFFFFFFFEu ? v : 0xFFFFFFFEu; }
__device__ __forceinline__ v4u nosent4(v4u v) { v.x = nosent(v.x); v.y = nosent(v.y); v.z = nosent(v.z); v.w = nosent(v.w); return v; }
__device__ __forceinline__ u32 umax3(u32 a, u32 b, u32 c) { u32 m = a > b ? a : b; return m > c ? m : c; }
__device__ __forceinline__ u32 chk(u32 m, v4u v) { return umax3(umax3(m, v.x, v.y), v.z, v.w); }
template <int C> __device__ __forceinline__ float dppf(float v) { return __builtin_bit_cast(float, __builtin_amdgcn_update_dpp(0, __builtin_bit_cast(int, v), C, 0xf, 0xf, false)); }
__device__ __forceinline__ float rowsum16(float v) { v += dppf<0xB1>(v); v += dppf<0x4E>(v); v += dppf<0x141>(v); v += dppf<0x140>(v); return v; }
__device__ __forceinline__ v4u ld4(rsrc_t r, u32 vo, u32 so) { return __builtin_amdgcn_raw_buffer_load_b128(r, vo, so, 0); }
__device__ __forceinline__ v4u ld4s(rsrc_t r, u32 vo, u32 so) { return __builtin_amdgcn_raw_buffer_load_b128(r, vo, so, 16); }
__device__ __forceinline__ v2u ld2(rsrc_t r, u32 vo, u32 so) { return __builtin_amdgcn_raw_buffer_load_b64(r, vo, so, 0); }
__device__ __forceinline__ u32 ld1(rsrc_t r, u32 vo, u32 so) { return __builtin_amdgcn_raw_buffer_load_b32(r, vo, so, 0); }
__device__ __forceinline__ void st4s(rsrc_t r, u32 vo, u32 so, v4u v) { __builtin_amdgcn_raw_buffer_store_b128(v, r, vo + so, 0, 16); }
__device__ __forceinline__ void st2s(rsrc_t r, u32 vo, u32 so, v2u v) { __builtin_amdgcn_raw_buffer_store_b64(v, r, vo, so, 16); }
__device__ __forceinline__ v4u modulate(const u32 (&xh)[8], v4u a0, v4u a1) {
  v4u h;
  float d0, d1, d2, d3, d4, d5, d6, d7;
  asm volatile(
      "v_dot2_f32_bf16 %4, %12, %20, 0\n\tv_dot2_f32_bf16 %5, %13, %21, 0\n\tv_dot2_f32_bf16 %6, %14, %22, 0\n\tv_dot2_f32_bf16 %7, %15, %23, 0\n\t"
      "v_dot2_f32_bf16 %8, %16, %24, 0\n\tv_dot2_f32_bf16 %9, %17, %25, 0\n\tv_dot2_f32_bf16 %10, %18, %26, 0\n\tv_dot2_f32_bf16 %11, %19, %27, 0\n\t"
      "v_cvt_pk_bf16_f32 %0, %4, %5\n\tv_cvt_pk_bf16_f32 %1, %6, %7\n\tv_cvt_pk_bf16_f32 %2, %8, %9\n\tv_cvt_pk_bf16_f32 %3, %10, %11\n\ts_nop 1"
      : "=&v"(h.x), "=&v"(h.y), "=&v"(h.z), "=&v"(h.w), "=&v"(d0), "=&v"(d1), "=&v"(d2), "=&v"(d3), "=&v"(d4), "=&v"(d5), "=&v"(d6), "=&v"(d7)
      : "v"(xh[0]), "v"(xh[1]), "v"(xh[2]), "v"(xh[3]), "v"(xh[4]), "v"(xh[5]), "v"(xh[6]), "v"(xh[7]),
        "v"(a0.x), "v"(a0.y), "v"(a0.z), "v"(a0.w), "v"(a1.x), "v"(a1.y), "v"(a1.z), "v"(a1.w));
  return h;
}
typedef short s4v __attribute__((ext_vector_type(4)));
__device__ __forceinline__ v2u trrd(LAS char* p) { return __builtin_bit_cast(v2u, __builtin_amdgcn_ds_read_tr16_b64_v4i16((LAS s4v*)p)); }
__device__ __forceinline__ v4u mksrd(const void* p, u32 bytes) {
  const u64 a = (u64)p;
  return (v4u){(u32)__builtin_amdgcn_readfirstlane((u32)a), (u32)__builtin_amdgcn_readfirstlane((u32)(a >> 32)) & 0xffffu, bytes, 0x00020000u};
}
template <bool SC1> __device__ __forceinline__ void dma16(v4u srd, u32 vo, u32 so, u32 ldsaddr) {
  u32 keep;
  so = __builtin_amdgcn_readfirstlane(so);
  ldsaddr = __builtin_amdgcn_readfirstlane(ldsaddr);
  if (SC1)
    asm volatile("s_mov_b32 %0, m0\n\ts_mov_b32 m0, %4\n\ts_nop 3\n\tbuffer_load_dwordx4 %1, %2, %3 offen sc1 lds\n\ts_mov_b32 m0, %0"
                 : "=&s"(keep) : "v"(vo), "s"(srd), "s"(so), "s"(ldsaddr) : "memory");
  else
    asm volatile("s_mov_b32 %0, m0\n\ts_mov_b32 m0, %4\n\ts_nop 3\n\tbuffer_load_dwordx4 %1, %2, %3 offen lds\n\ts_mov_b32 m0, %0"
                 : "=&s"(keep) : "v"(vo), "s"(srd), "s"(so), "s"(ldsaddr) : "memory");
}
__device__ __forceinline__ void canary(rsrc_t r, u32 vo, u32 so, unsigned& spins) {
  __builtin_amdgcn_s_setprio(0);
  for (; spins <= SPIN_CAP; ++spins) {
    __builtin_amdgcn_s_sleep(2);
    asm volatile("" ::: "memory");
    if (!__any(__builtin_amdgcn_raw_buffer_load_b32(r, vo, so, 16) == ~0u)) break;
  }
}
#define LDS4(off) (*(LAS v4u*)(lds + (off)))
#define LDS2(off) (*(LAS v2u*)(lds + (off)))
#define LDSF4(off) (*(LAS f4*)(lds + (off)))
#define LDSF(off) (*(LAS float*)(lds + (off)))
#define LDSH(off) (*(LAS u16*)(lds + (off)))
#define CBAR() asm volatile("" ::: "memory")
#define PIN(v) asm volatile("" : "+v"(v))
#define SB0() __builtin_amdgcn_sched_barrier(0)
#define BACKOFF() do { __builtin_amdgcn_s_setprio(0); __builtin_amdgcn_s_sleep(6); } while (0)
#define RUNPRIO() __builtin_amdgcn_s_setprio(1)
#define DRAIN() asm volatile("s_waitcnt vmcnt(0)" ::: "memory")
#define STAMPT(i)

__global__ __launch_bounds__(512, 2) void mega(const u16* __restrict__ x, const u16* __restrict__ wqkv, const u16* __restrict__ wo,
                                               const u16* __restrict__ w1, const u16* __restrict__ w2, const u16* __restrict__ cvec,
                                               const u16* __restrict__ wmod, u16* __restrict__ out, char* ws) {
  extern __shared__ __attribute__((aligned(16))) char lds_g[];
  LAS char* lds = (LAS char*)lds_g;
  const int tid = threadIdx.x, lane = tid & 63, w = __builtin_amdgcn_readfirstlane(tid >> 6) & 7;
  const int l15 = lane & 15, g = lane >> 4;
  const int b = blockIdx.x, xcd = b & 7, slot = b >> 3;
  const int g16 = xcd * 2 + (slot >> 4), head = slot & 15;
  const int g32 = xcd * 4 + (slot >> 3), c8 = slot & 7;
  const rsrc_t rws = __builtin_amdgcn_make_buffer_rsrc(ws, 0, WS_END, 0x00020000);
  const rsrc_t rx = __builtin_amdgcn_make_buffer_rsrc((void*)x, 0, S_ * D_ * 2, 0x00020000);
  const rsrc_t rwq = __builtin_amdgcn_make_buffer_rsrc((void*)wqkv, 0, D_ * 3 * D_ * 2, 0x00020000);
  const rsrc_t rwm = __builtin_amdgcn_make_buffer_rsrc((void*)wmod, 0, D_ * 6 * D_ * 2, 0x00020000);
  const rsrc_t rc = __builtin_amdgcn_make_buffer_rsrc((void*)cvec, 0, D_ * 2, 0x00020000);
  const v4u SENT = {~0u, ~0u, ~0u, ~0u};
  STAMPT(0);
  RUNPRIO();
  u32 cpf, mpf;
  u64 epoch;
  {
    u64 pc;
    u32 len;
    asm volatile("s_getpc_b64 %0\n.Lmega_start:\n\ts_mov_b32 %1, .Lmega_end-.Lmega_start" : "=s"(pc), "=s"(len));
    const v4u scode = mksrd((const void*)pc, len), swm0 = mksrd(wmod, D_ * 6 * D_ * 2), sws0 = mksrd(ws, 64);
    const int idx = slot * 84 + tid - 320, n = idx / 448, row = idx - 448 * n, j = xcd + 8 * n;
    asm volatile("s_nop 4\n\tbuffer_load_dword %0, %3, %5, 0 offen\n\tbuffer_load_dword %1, %4, %6, 0 offen\n\tbuffer_load_dwordx2 %2, off, %7, 0 sc1"
                 : "=&v"(cpf), "=&v"(mpf), "=&v"(epoch)
                 : "v"(tid >= 448 ? (lane * 8 + (slot & 7)) * 128 : OOB), "v"((tid >= 320 && tid < 404 && j < 42) ? (u32)(row * 5376 + j * 128) : OOB),
                   "s"(scode), "s"(swm0), "s"(sws0)
                 : "memory");
  }
  gu64* gr0 = (gu64*)(ws + WS_GRAN);
  gu64* gr = gr0 + xcd * G_REP;
  const int mj = xcd + 8 * (slot / 3);
  const bool modw = (slot % 3 == 0) && mj < 42 && w < 4;
  if (tid < 4) *(volatile LAS u32*)(lds + L_RED + 3072 + tid * 4) = 0u;
  v2u mr[4][8];
  u32 craw;
  if (modw) {
    const v4u swm = mksrd(wmod, D_ * 6 * D_ * 2), sc_ = mksrd(cvec, D_ * 2);
    asm volatile("s_nop 4\n\tbuffer_load_dword %0, %1, %2, 0 offen" : "=&v"(craw) : "v"(w + 4 * g < 14 ? (32 * (w + 4 * g) + 2 * l15) * 2 : OOB), "s"(sc_) : "memory");
#pragma unroll
    for (int i = 0; i < 4; ++i) {
      const u32 vo = (i < 3 || w < 2) ? (u32)(8 * g * 2688 + 4 * l15) * 2 : OOB;
      const u32 so = (u32)((32 * ((w + 4 * i) % 14)) * 2688 + 64 * mj) * 2;
      asm volatile(
          "s_nop 4\n\t"
          "buffer_load_dwordx2 %0, %8, %9, %10 offen\n\tbuffer_load_dwordx2 %1, %8, %9, %11 offen\n\t"
          "buffer_load_dwordx2 %2, %8, %9, %12 offen\n\tbuffer_load_dwordx2 %3, %8, %9, %13 offen\n\t"
          "buffer_load_dwordx2 %4, %8, %9, %14 offen\n\tbuffer_load_dwordx2 %5, %8, %9, %15 offen\n\t"
          "buffer_load_dwordx2 %6, %8, %9, %16 offen\n\tbuffer_load_dwordx2 %7, %8, %9, %17 offen"
          : "=&v"(mr[i][0]), "=&v"(mr[i][1]), "=&v"(mr[i][2]), "=&v"(mr[i][3]), "=&v"(mr[i][4]), "=&v"(mr[i][5]), "=&v"(mr[i][6]), "=&v"(mr[i][7])
          : "v"(vo), "s"(swm), "s"(so), "s"(so + 5376), "s"(so + 2 * 5376), "s"(so + 3 * 5376), "s"(so + 4 * 5376), "s"(so + 5 * 5376),
            "s"(so + 6 * 5376), "s"(so + 7 * 5376)
          : "memory");
    }
  }
  asm volatile("s_waitcnt lgkmcnt(0)\n\ts_barrier" ::: "memory");
  STAMPT(14);
  const v4u sq_srd = mksrd(wqkv, D_ * 3 * D_ * 2);
  const u32 vq = (u32)((lane >> 2) * 2688 + (((lane & 3) ^ (2 * (lane >> 5))) * 16));
#define WQDMA(i)                                                                                                        \
  {                                                                                                                     \
    const int u = w + 8 * ((i) >> 1), d = 2 * u + ((i) & 1), mat = u / 14, kb = d - 28 * mat;                           \
    dma16<false>(sq_srd, u < 42 ? vq : OOB, (u32)(16 * kb * 2688 + (mat % 3) * 896 + 56 * head), u < 42 ? L_WQ + d * 1024 : L_RED + 4096); \
  }
  {
    const v4u sx = mksrd(x, S_ * D_ * 2);
    const u32 vo = (u32)((lane >> 3) * D_ * 2 + (((lane & 7) ^ (lane >> 3)) * 16));
#pragma unroll
    for (int q = 0; q < 14; ++q) dma16<false>(sx, vo, (u32)((128 * g16 + 16 * w + 8 * (q & 1)) * D_ * 2 + (q >> 1) * 128), L_X + w * 14336 + q * 1024);
  }
  WQDMA(0) WQDMA(1) WQDMA(2) WQDMA(3)
  u32 pr[8];
  u32 pdst;
  {
    const int u = b * 8 + w;
    const u16* base; u32 bytes, ld, so, vo, vo2;
    if (u < 896) {
      const int cc = u / 112, v = u - 112 * cc, kb = v >> 1, hf = v & 1;
      base = w2; bytes = DFF_ * D_ * 2; ld = D_ * 2; so = (u32)(32 * kb * D_ + 56 * cc + 2 * hf) * 2;
      vo = l15 < 14 ? (u32)(8 * g * D_ + 4 * l15) * 2 : OOB; vo2 = vo;
      pdst = WS_W2T + ((cc * 56 + kb) * 4 + 2 * hf) * 1024;
    } else if (u < 1680) {
      const int v = u - 896, cb = v / 14, s = v - 14 * cb;
      base = w1; bytes = DFF_ * D_ * 2; ld = DFF_ * 2; so = (u32)(32 * s * DFF_ + 32 * cb) * 2;
      vo = (u32)(8 * g * DFF_ + 2 * l15) * 2; vo2 = vo;
      pdst = WS_W1T + (cb * 14 + s) * 2048;
    } else {
      const int v = u - 1680, hh = v & 15, nh = (v >> 4) & 1, cc = (v >> 5) & 7;
      base = wo; bytes = D_ * D_ * 2; ld = D_ * 2; so = (u32)(28 * hh * D_ + 56 * cc + 32 * nh) * 2;
      vo = (u < 1936 && (nh == 0 || l15 < 12)) ? (u32)(4 * g * D_ + 2 * l15) * 2 : OOB; vo2 = g < 3 ? vo + 12 * D_ * 2 : OOB;
      pdst = u < 1936 ? WS_WOT + (cc * 32 + nh * 16 + hh) * 2048 : OOB;
    }
    const v4u sp = mksrd(base, __builtin_amdgcn_readfirstlane(bytes));
    so = __builtin_amdgcn_readfirstlane(so); ld = __builtin_amdgcn_readfirstlane(ld);
    asm volatile(
        "s_nop 4\n\t"
        "buffer_load_dword %0, %8, %10, %11 offen\n\tbuffer_load_dword %1, %8, %10, %12 offen\n\t"
        "buffer_load_dword %2, %8, %10, %13 offen\n\tbuffer_load_dword %3, %8, %10, %14 offen\n\t"
        "buffer_load_dword %4, %9, %10, %15 offen\n\tbuffer_load_dword %5, %9, %10, %16 offen\n\t"
        "buffer_load_dword %6, %9, %10, %17 offen\n\tbuffer_load_dword %7, %9, %10, %18 offen"
        : "=&v"(pr[0]), "=&v"(pr[1]), "=&v"(pr[2]), "=&v"(pr[3]), "=&v"(pr[4]), "=&v"(pr[5]), "=&v"(pr[6]), "=&v"(pr[7])
        : "v"(vo), "v"(vo2), "s"(sp), "s"(so), "s"(so + ld), "s"(so + 2 * ld), "s"(so + 3 * ld), "s"(so + 4 * ld), "s"(so + 5 * ld), "s"(so + 6 * ld),
          "s"(so + 7 * ld)
        : "memory");
  }
  SB0();
  asm volatile("s_waitcnt vmcnt(26)" : "+v"(epoch), "+v"(cpf), "+v"(mpf)::"memory");
  const u64 tag = (epoch + 1) & 0xFFFFFFFFFFFFull;
  if (modw) {
    __builtin_amdgcn_s_setprio(3);
    asm volatile(""
                 : "+v"(craw), "+v"(mr[0][0]), "+v"(mr[0][1]), "+v"(mr[0][2]), "+v"(mr[0][3]), "+v"(mr[0][4]), "+v"(mr[0][5]), "+v"(mr[0][6]), "+v"(mr[0][7]),
                   "+v"(mr[1][0]), "+v"(mr[1][1]), "+v"(mr[1][2]), "+v"(mr[1][3]), "+v"(mr[1][4]), "+v"(mr[1][5]), "+v"(mr[1][6]), "+v"(mr[1][7])::"memory");
    asm volatile(""
                 : "+v"(mr[2][0]), "+v"(mr[2][1]), "+v"(mr[2][2]), "+v"(mr[2][3]), "+v"(mr[2][4]), "+v"(mr[2][5]), "+v"(mr[2][6]), "+v"(mr[2][7]),
                   "+v"(mr[3][0]), "+v"(mr[3][1]), "+v"(mr[3][2]), "+v"(mr[3][3]), "+v"(mr[3][4]), "+v"(mr[3][5]), "+v"(mr[3][6]), "+v"(mr[3][7])::"memory");
    float c0 = lo2f(craw), c1 = hi2f(craw);
    c0 = c0 * __builtin_amdgcn_rcpf(1.f + __expf(-c0));
    c1 = c1 * __builtin_amdgcn_rcpf(1.f + __expf(-c1));
    *(LAS u32*)(lds + L_RED + w * 256 + lane * 4) = pk(c0, c1);
    f4 acc[4] = {{0, 0, 0, 0}, {0, 0, 0, 0}, {0, 0, 0, 0}, {0, 0, 0, 0}};
#pragma unroll
    for (int i = 0; i < 4; ++i) {
      const v4u sf = LDS4(L_RED + w * 256 + i * 64 + g * 16);
      acc[0] = mfma((v4u){plo(mr[i][0].x, mr[i][1].x), plo(mr[i][2].x, mr[i][3].x), plo(mr[i][4].x, mr[i][5].x), plo(mr[i][6].x, mr[i][7].x)}, sf, acc[0]);
      acc[1] = mfma((v4u){phi(mr[i][0].x, mr[i][1].x), phi(mr[i][2].x, mr[i][3].x), phi(mr[i][4].x, mr[i][5].x), phi(mr[i][6].x, mr[i][7].x)}, sf, acc[1]);
      acc[2] = mfma((v4u){plo(mr[i][0].y, mr[i][1].y), plo(mr[i][2].y, mr[i][3].y), plo(mr[i][4].y, mr[i][5].y), plo(mr[i][6].y, mr[i][7].y)}, sf, acc[2]);
      acc[3] = mfma((v4u){phi(mr[i][0].y, mr[i][1].y), phi(mr[i][2].y, mr[i][3].y), phi(mr[i][4].y, mr[i][5].y), phi(mr[i][6].y, mr[i][7].y)}, sf, acc[3]);
    }
    if (l15 == 0) {
#pragma unroll
      for (int r = 0; r < 4; ++r) LDSF4(L_RED + 1024 + w * 256 + (16 * g + 4 * r) * 4) = (f4){acc[0][r], acc[1][r], acc[2][r], acc[3][r]};
    }
    asm volatile("s_waitcnt lgkmcnt(0)" ::: "memory");
    u32 old = 0;
    if (lane == 0) old = __hip_atomic_fetch_add((LAS u32*)(lds + L_RED + 3072), 1u, __ATOMIC_RELAXED, __HIP_MEMORY_SCOPE_WORKGROUP);
    if (__builtin_amdgcn_readfirstlane(old) == 3u) {
      CBAR();
      const float sum = (LDSF(L_RED + 1024 + lane * 4) + LDSF(L_RED + 1280 + lane * 4)) + (LDSF(L_RED + 1536 + lane * 4) + LDSF(L_RED + 1792 + lane * 4));
      const u64 gval = (tag << 16) | (u64)(pk(sum, 0.f) & 0xffffu);
#pragma unroll
      for (int r = 0; r < 8; ++r) __hip_atomic_store(gr0 + r * G_REP + 64 * mj + lane, gval, RLX_AGENT);
    }
    RUNPRIO();
  }
  STAMPT(15);
  if (modw) asm volatile("s_waitcnt vmcnt(8)" ::: "memory"); else asm volatile("s_waitcnt vmcnt(12)" ::: "memory");
  STAMPT(16);
  v4u xr[14];
  {
    const u32 ra0 = L_X + w * 14336 + (l15 >> 3) * 1024 + (l15 & 7) * 128 + ((g ^ (l15 & 7)) * 16);
    const u32 ra1 = L_X + w * 14336 + (l15 >> 3) * 1024 + (l15 & 7) * 128 + (((4 + g) ^ (l15 & 7)) * 16);
#pragma unroll
    for (int s = 0; s < 14; ++s) xr[s] = LDS4(((s & 1) ? ra1 : ra0) + (s >> 1) * 2048);
#pragma unroll
    for (int s = 0; s < 14; ++s) PIN(xr[s]);
  }
  asm volatile("s_waitcnt lgkmcnt(0)\n\ts_barrier" ::: "memory");
  STAMPT(17);
  const int mtB = w & 3, nhB = w >> 2;
  v4u xres;
  const int tau = 16 * (w & 1) + l15;
  const u32 koff = (w >> 1) * 2048 + ((tau >> 2) & 1) * 1024 + (g * 16 + 4 * (tau >> 3) + (tau & 3)) * 16;
  const u32 voff = (w >> 1) * 2048 + ((2 * (w & 1) + (g >> 1)) * 16 + l15) * 16 + (g & 1) * 8;
  const u32 kvb = g16 < 15 ? WS_KV + (g16 * 16 + head) * 16384 : OOB;
  u32 xh[14][8];
  {
    const v4u ONES = {0x3F803F80u, 0x3F803F80u, 0x3F803F80u, 0x3F803F80u};
    f4 sm = {0, 0, 0, 0}, gm = {0, 0, 0, 0};
#pragma unroll
    for (int s = 0; s < 14; ++s) {
      if (s == 0 || s == 7) WQDMA(4 + s / 7)
      sm = mfma(ONES, xr[s], sm);
      gm = mfma(xr[s], xr[s], gm);
    }
    float sq = (l15 & 2) ? ((l15 & 1) ? gm[3] : gm[2]) : ((l15 & 1) ? gm[1] : gm[0]);
    sq = __shfl(sq, 16 * (l15 >> 2) + l15);
    const float mean = sm[0] * (1.f / D_);
    const float rstd = __builtin_amdgcn_rsqf(fmaxf(sq * (1.f / D_) - mean * mean, 0.f) + 1e-5f);
    const f2 rs2 = {rstd, rstd}, nm2 = {-mean * rstd, -mean * rstd};
#pragma unroll
    for (int s = 0; s < 14; ++s) {
      if (s < 6) WQDMA(6 + s)
      else if (s == 7) xres = ld4(rx, (nhB == 1 && g == 3) ? OOB : (u32)(l15 * D_ + 8 * g) * 2, (u32)((64 * g32 + 16 * mtB) * D_ + 56 * c8 + 32 * nhB) * 2);
      else if (s == 9) st4s(rws, koff, kvb, SENT);
      else if (s == 11) st4s(rws, (w >> 1) * 2048 + (w & 1) * 512 + (lane & 31) * 16 + (lane >> 5) * 1024, kvb + 8192, SENT);
      else if (s == 13) st4s(rws, lane * 16, WS_O + ((8 * g16 + w) * 16 + head) * 1024, SENT);
#pragma unroll
      for (int j = 0; j < 4; ++j) {
        const f2 v = __builtin_elementwise_fma((f2){lo2f(xr[s][j]), hi2f(xr[s][j])}, rs2, nm2);
        xh[s][2 * j] = pk(v.x, 1.f);
        xh[s][2 * j + 1] = pk(v.y, 1.f);
        PIN(xh[s][2 * j]); PIN(xh[s][2 * j + 1]);
      }
      SB0();
    }
  }
  STAMPT(20);
  const int i1 = tid < 384 ? 512 + tid : tid;
  u64 g10 = __hip_atomic_load(gr + tid, RLX_AGENT), g11 = __hip_atomic_load(gr + i1, RLX_AGENT);
  asm volatile("s_waitcnt vmcnt(6)"
               : "+v"(pr[0]), "+v"(pr[1]), "+v"(pr[2]), "+v"(pr[3]), "+v"(pr[4]), "+v"(pr[5]), "+v"(pr[6]), "+v"(pr[7])::"memory");
  {
    LAS char* tb0 = lds + L_WQ + (8 * g + (l15 >> 2)) * 64 + (g & 1) * 32 + (l15 & 3) * 8;
    LAS char* tb1 = lds + L_WQ + (8 * g + (l15 >> 2)) * 64 + ((g & 1) ^ 1) * 32 + (l15 & 3) * 8;
    v2u r[6][4];
#pragma unroll
    for (int i = 0; i < 6; ++i) {
      const int u = (i < 5 || w < 2) ? w + 8 * i : w;
      r[i][0] = trrd(tb0 + u * 2048); r[i][1] = trrd(tb0 + u * 2048 + 256);
      r[i][2] = trrd(tb1 + u * 2048); r[i][3] = trrd(tb1 + u * 2048 + 256);
    }
    SB0();
#pragma unroll
    for (int i = 0; i < 6; ++i) {
      const int u = (i < 5 || w < 2) ? w + 8 * i : w;
      LDS4(L_WQ + u * 2048 + lane * 16) = (v4u){r[i][0].x, r[i][0].y, r[i][1].x, r[i][1].y};
      LDS4(L_WQ + u * 2048 + 1024 + lane * 16) = (v4u){r[i][2].x, r[i][2].y, r[i][3].x, r[i][3].y};
    }
  }
  v4u pe = {plo(pr[0], pr[1]), plo(pr[2], pr[3]), plo(pr[4], pr[5]), plo(pr[6], pr[7])};
  v4u po = {phi(pr[0], pr[1]), phi(pr[2], pr[3]), phi(pr[4], pr[5]), phi(pr[6], pr[7])};
  PIN(pe); PIN(po);
  STAMPT(2);
  {
    for (unsigned spins = 0;; ++spins) {
      const bool ok = (g10 >> 16) == tag && (g11 >> 16) == tag;
      if (__all(ok) || spins > SPIN_CAP) break;
      BACKOFF();
      g10 = __hip_atomic_load(gr + tid, RLX_AGENT);
      g11 = __hip_atomic_load(gr + i1, RLX_AGENT);
    }
    RUNPRIO();
    if (tid < 448) LDSH(L_AS1 + tid * 4 + 2) = (u16)g10;
    else LDSH(L_AS1 + (tid - 448) * 4) = (u16)pk(1.f + lo2f((u32)g10), 0.f);
    if (tid < 384) LDSH(L_AS1 + (64 + tid) * 4) = (u16)pk(1.f + lo2f((u32)g11), 0.f);
  }
  DRAIN();
  __syncthreads();
  if (tid < 8) __hip_atomic_store(gr0 + tid * G_REP + G_FILL + b, tag << 16, RLX_AGENT);
  STAMPT(3);
  const int jg = tid - 448;
  const int i20 = tid < 448 ? 1344 + tid : 896 + 56 * c8 + (jg < 56 ? jg : 0);
  const int i21 = tid < 448 ? 1792 + tid : 2240 + 56 * c8 + (jg < 56 ? jg : 0);
  u64 g20 = __hip_atomic_load(gr + i20, RLX_AGENT), g21 = __hip_atomic_load(gr + i21, RLX_AGENT);

  v4u hfr[14];
  {
    v4u as[3][2];
#pragma unroll
    for (int s = 0; s < 2; ++s) { as[s][0] = LDS4(L_AS1 + (32 * s + 8 * g) * 4); as[s][1] = LDS4(L_AS1 + (32 * s + 8 * g) * 4 + 16); }
#pragma unroll
    for (int s = 0; s < 14; ++s) {
      if (s < 12) { as[(s + 2) % 3][0] = LDS4(L_AS1 + (32 * (s + 2) + 8 * g) * 4); as[(s + 2) % 3][1] = LDS4(L_AS1 + (32 * (s + 2) + 8 * g) * 4 + 16); }
      SB0();
      hfr[s] = modulate(xh[s], as[s % 3][0], as[s % 3][1]);
      SB0();
    }
  }
  STAMPT(29);
  int dep = b;
  if (lane < 16) dep = xcd + 8 * (16 * (g16 & 1) + lane);
  else if (lane == 16) dep = g16 >= 1 ? ((g16 - 1) >> 1) + 8 * (16 * ((g16 - 1) & 1) + head) : b;
  else if (lane == 17) dep = g16 >= 2 ? ((g16 - 2) >> 1) + 8 * (16 * (g16 & 1) + head) : b;
  else if (lane < 32) dep = 14 * c8 + (lane - 18);
  else if (lane < 46) dep = ((896 + 98 * c8) >> 3) + (lane - 32);
  else if (lane < 50) dep = 210 + 4 * c8 + (lane - 46);
  u64 dfl = tag << 16;
  f4 aq[6];
#pragma unroll
  for (int t = 0; t < 6; ++t) aq[t] = (f4){0, 0, 0, 0};
  {
    v4u fb[3][6];
#define QLOAD(s, d) { _Pragma("unroll") for (int t = 0; t < 6; ++t) fb[d][t] = LDS4(L_WQ + ((t >> 1) * 14 + (s)) * 2048 + (t & 1) * 1024 + lane * 16); }
    QLOAD(0, 0)
    QLOAD(1, 1)
#pragma unroll
    for (int s = 0; s < 14; ++s) {
      if (s < 12) QLOAD(s + 2, (s + 2) % 3)
      if (s == 0) st4s(rws, lane * 16, pdst, pe);
      if (s == 2) st4s(rws, lane * 16, pdst + 1024, po);
      if (s == 4) st4s(rws, (w < 4 || lane < 48) ? lane * 16 : OOB, WS_X1 + (4 * g32 + (w & 3)) * 14336 + c8 * 1792 + (w >> 2) * 1024, SENT);
      if (s == 5) st4s(rws, (w < 4 && lane < 16) ? lane * 16 : OOB, WS_ST + (4 * g32 + (w & 3)) * 2048 + c8 * 256, SENT);
      if (s == 8 && w == 0) dfl = __hip_atomic_load(gr + G_FILL + (lane < 18 ? dep : b), RLX_AGENT);
      SB0();
#pragma unroll
      for (int t = 0; t < 4; ++t) aq[t] = mfma(fb[s % 3][t], hfr[s], aq[t]);
#pragma unroll
      for (int t = 4; t < 6; ++t) aq[t] = mfma(hfr[s], fb[s % 3][t], aq[t]);
      SB0();
    }
  }
  STAMPT(30);
  const v4u qf = {pk(aq[0][0], aq[0][1]), pk(aq[0][2], aq[0][3]), g < 3 ? pk(aq[1][0], aq[1][1]) : 0u, g < 3 ? pk(aq[1][2], aq[1][3]) : 0u};
  {
    const v4u kf = nosent4((v4u){pk(aq[2][0], aq[2][1]), pk(aq[2][2], aq[2][3]), pk(aq[3][0], aq[3][1]), pk(aq[3][2], aq[3][3])});
    v2u ve = {pk(aq[4][0], aq[4][1]), pk(aq[4][2], aq[4][3])}, vo_ = {pk(aq[5][0], aq[5][1]), pk(aq[5][2], aq[5][3])};
    if (l15 == 12) vo_ = (v2u){0x3F803F80u, 0x3F803F80u};
    ve.x = nosent(ve.x); ve.y = nosent(ve.y); vo_.x = nosent(vo_.x); vo_.y = nosent(vo_.y);
    {
      DRAIN();
      u32 old = 0;
      if (lane == 0) old = __hip_atomic_fetch_add((LAS u32*)(lds + L_RED + 3080), 1u, __ATOMIC_RELAXED, __HIP_MEMORY_SCOPE_WORKGROUP);
      if (__builtin_amdgcn_readfirstlane(old) == 7u && lane < 8) __hip_atomic_store(gr0 + lane * G_REP + G_PREP + b, tag << 16, RLX_AGENT);
    }
    st4s(rws, koff, kvb, kf);
    st2s(rws, voff, kvb + 8192, ve);
    st2s(rws, voff, kvb + 8192 + 1024, vo_);
    STAMPT(31);
    for (unsigned spins = 0;; ++spins) {
      bool ok = (g20 >> 16) == tag && (g21 >> 16) == tag;
      ok &= (dfl >> 16) == tag;
      if (__all(ok) || spins > SPIN_CAP) break;
      BACKOFF();
      if (w == 0) dfl = __hip_atomic_load(gr + G_FILL + (lane < 18 ? dep : b), RLX_AGENT);
      g20 = __hip_atomic_load(gr + i20, RLX_AGENT);
      g21 = __hip_atomic_load(gr + i21, RLX_AGENT);
    }
    RUNPRIO();
    if (tid < 448) {
      LDSH(L_SB + tid * 2) = (u16)g20;
      LDSH(L_A2 + tid * 2) = (u16)pk(1.f + lo2f((u32)g21), 0.f);
    } else {
      LDSF(L_GA + jg * 4) = lo2f((u32)g20);
      LDSF(L_GB + jg * 4) = lo2f((u32)g21);
    }
    STAMPT(21);
    __syncthreads();
    STAMPT(4);
    LDS4(L_K + 16384 + koff) = kf;
    LDS2(L_V + 16384 + voff) = ve;
    LDS2(L_V + 16384 + 1024 + voff) = vo_;
  }
  STAMPT(22);
  v4u w1f[14][2];
  const u32 vo1 = w < 7 ? lane * 16 : OOB;
  const v4u sws = mksrd(ws, WS_END);
  {
    const u32 b0 = g16 >= 2 ? WS_KV + ((g16 - 2) * 16 + head) * 16384 : OOB;
    const u32 b1 = g16 >= 1 ? WS_KV + ((g16 - 1) * 16 + head) * 16384 : OOB;
    v4u k0, v0, k1, v1;
    for (unsigned spins = 0;; ++spins) {
      CBAR();
      k0 = ld4s(rws, tid * 16, b0); v0 = ld4s(rws, tid * 16, b0 + 8192);
      k1 = ld4s(rws, tid * 16, b1); v1 = ld4s(rws, tid * 16, b1 + 8192);
      const bool bad = chk(chk(chk(chk(0u, k0), v0), k1), v1) == ~0u;
      if (!__any(bad) || spins > SPIN_CAP) break;
      {
        const u32 pb = (lane & 16) ? b1 : b0;
        canary(rws, lane < 32 ? pb + ((lane >> 3) & 1) * 8192 + w * 1024 + (lane & 7) * 128 : OOB, 0, spins);
      }
    }
    RUNPRIO();
    STAMPT(24);
    LDS4(L_K + tid * 16) = k0; LDS4(L_V + tid * 16) = v0;
    LDS4(L_K + 8192 + tid * 16) = k1; LDS4(L_V + 8192 + tid * 16) = v1;
  }
  if (w == 0) {
    for (unsigned spins = 0;; ++spins) {
      if (__all((__hip_atomic_load(gr + G_PREP + (lane >= 18 ? dep : b), RLX_AGENT) >> 16) == tag) || spins > SPIN_CAP) break;
      BACKOFF();
    }
    RUNPRIO();
  }
  DRAIN();
  __syncthreads();
  STAMPT(5);
  {
    const int blk0 = w >> 1;
    f4 sc[18];
    {
      v4u kf[18];
#pragma unroll
      for (int t = 0; t < 6; ++t) kf[t] = LDS4(L_K + (blk0 + (t >> 1)) * 2048 + (t & 1) * 1024 + lane * 16);
#pragma unroll
      for (int t = 0; t < 18; ++t) {
        if (t + 6 < 18) kf[t + 6] = LDS4(L_K + (blk0 + ((t + 6) >> 1)) * 2048 + (t & 1) * 1024 + lane * 16);
        if ((t & 1) && t < 16) dma16<true>(sws, lane * 16, WS_WOT + c8 * 65536 + (w * 8 + (t >> 1)) * 1024, L_WO + (w * 8 + (t >> 1)) * 1024);
        SB0();
        sc[t] = mfma(kf[t], qf, (f4){0, 0, 0, 0});
        SB0();
      }
    }
    STAMPT(25);
    v4u vf[18];
#pragma unroll
    for (int t = 0; t < 4; ++t) vf[t] = LDS4(L_V + (blk0 + (t >> 1)) * 2048 + (t & 1) * 1024 + lane * 16);
    const int ql = 256 + 16 * w + l15;
    const int kmin = (256 - 128 * g16) > (ql - 255) ? (256 - 128 * g16) : (ql - 255);
    const int kb0 = 32 * blk0 + 8 * g;
    const float NEG = -1e30f;
    if (g16 < 2) {
#pragma unroll
      for (int t = 2; t < 16; ++t)
#pragma unroll
        for (int r = 0; r < 4; ++r) sc[t][r] = (kb0 + 32 * (t >> 1) + 4 * (t & 1) + r) >= kmin ? sc[t][r] : NEG;
    }
#pragma unroll
    for (int t = 0; t < 2; ++t)
#pragma unroll
      for (int r = 0; r < 4; ++r) sc[t][r] = (kb0 + 4 * t + r) >= kmin ? sc[t][r] : NEG;
#pragma unroll
    for (int t = 16; t < 18; ++t)
#pragma unroll
      for (int r = 0; r < 4; ++r) sc[t][r] = (kb0 + 256 + 4 * (t & 1) + r) <= ql ? sc[t][r] : NEG;
    float mx = NEG;
#pragma unroll
    for (int t = 0; t < 18; ++t) mx = fmaxf(fmaxf(mx, sc[t][0]), fmaxf(sc[t][1], fmaxf(sc[t][2], sc[t][3])));
    mx = fmaxf(mx, __shfl_xor(mx, 16));
    mx = fmaxf(mx, __shfl_xor(mx, 32));
    STAMPT(26);
    STAMPT(18);
    const float cs = 0.18898223650461362f * 1.4426950408889634f;
    const float mc = mx * cs;
    f4 oe = {0, 0, 0, 0}, oo = {0, 0, 0, 0};
#pragma unroll
    for (int bb = 0; bb < 9; ++bb) {
      if (bb + 2 < 9) {
        vf[2 * bb + 4] = LDS4(L_V + (blk0 + bb + 2) * 2048 + lane * 16);
        vf[2 * bb + 5] = LDS4(L_V + (blk0 + bb + 2) * 2048 + 1024 + lane * 16);
      }
      w1f[bb >> 1][bb & 1] = ld4s(rws, vo1, WS_W1T + (7 * c8 + (w % 7)) * 28672 + bb * 1024);
      if ((bb & 1) == 0 && bb < 8) st4s(rws, vo1, WS_HID + (4 * g32 + (bb >> 1)) * 57344 + c8 * 7168 + (w % 7) * 1024, SENT);
      SB0();
      float p[8];
#pragma unroll
      for (int r = 0; r < 4; ++r) {
        p[r] = __builtin_amdgcn_exp2f(sc[2 * bb][r] * cs - mc);
        p[4 + r] = __builtin_amdgcn_exp2f(sc[2 * bb + 1][r] * cs - mc);
      }
      const v4u pf = {pk(p[0], p[1]), pk(p[2], p[3]), pk(p[4], p[5]), pk(p[6], p[7])};
      oe = mfma(vf[2 * bb], pf, oe);
      oo = mfma(vf[2 * bb + 1], pf, oo);
      SB0();
    }
    const float den = __shfl(oo[0], 48 + l15);
    const float inv = __builtin_amdgcn_rcpf(den);
    const v4u of = nosent4((v4u){pk(oe[0] * inv, oe[1] * inv), pk(oe[2] * inv, oe[3] * inv), pk(oo[0] * inv, oo[1] * inv), pk(oo[2] * inv, oo[3] * inv)});
    st4s(rws, lane * 16, WS_O + ((8 * g16 + w) * 16 + head) * 1024, of);
  }
  STAMPT(6);
  SB0();
  asm volatile("s_waitcnt vmcnt(14)" ::: "memory");
#pragma unroll
  for (int i = 9; i < 17; ++i) w1f[i >> 1][i & 1] = ld4s(rws, vo1, WS_W1T + (7 * c8 + (w % 7)) * 28672 + i * 1024);
  STAMPT(19);
  STAMPT(27);
  __syncthreads();
  STAMPT(28);

  {
    v4u of[8];
    const u32 base = WS_O + (4 * g32 + mtB) * 16384 + nhB * 8192;
    v4u wpre[4][4];
#pragma unroll
    for (int h = 0; h < 4; ++h)
#pragma unroll
      for (int t = 0; t < 4; ++t) { wpre[h][t] = LDS4(L_WO + ((t >> 1) * 16 + nhB * 8 + h) * 2048 + (t & 1) * 1024 + lane * 16); PIN(wpre[h][t]); }
    for (unsigned spins = 0;; ++spins) {
      CBAR();
      u32 m = 0;
#pragma unroll
      for (int h = 0; h < 8; ++h) of[h] = ld4s(rws, lane * 16, base + h * 1024);
#pragma unroll
      for (int h = 0; h < 8; ++h) m = chk(m, of[h]);
      if (!__any(m == ~0u) || spins > SPIN_CAP) break;
      canary(rws, lane < 8 ? lane * 1024 : OOB, base, spins);
    }
    RUNPRIO();
    DRAIN();
    STAMPT(7);
    f4 acc[4] = {{0, 0, 0, 0}, {0, 0, 0, 0}, {0, 0, 0, 0}, {0, 0, 0, 0}};
#pragma unroll
    for (int h = 0; h < 8; ++h) {
#pragma unroll
      for (int t = 0; t < 4; ++t) acc[t] = mfma(wpre[h & 3][t], of[h], acc[t]);
      SB0();
      if (h < 4) {
#pragma unroll
        for (int t = 0; t < 4; ++t) wpre[h][t] = LDS4(L_WO + ((t >> 1) * 16 + nhB * 8 + h + 4) * 2048 + (t & 1) * 1024 + lane * 16);
      }
      if (h >= 2) {
#pragma unroll
        for (int i = 17 + 2 * (h - 2); i < 19 + 2 * (h - 2) && i < 28; ++i) w1f[i >> 1][i & 1] = ld4s(rws, vo1, WS_W1T + (7 * c8 + (w % 7)) * 28672 + i * 1024);
      }
      SB0();
    }
    LDSF4(L_PR + w * 2048 + lane * 16) = nhB ? acc[0] : acc[2];
    LDSF4(L_PR + w * 2048 + 1024 + lane * 16) = nhB ? acc[1] : acc[3];
    __syncthreads();
    const f4 e = (nhB ? acc[2] : acc[0]) + LDSF4(L_PR + (w ^ 4) * 2048 + lane * 16);
    const f4 o = (nhB ? acc[3] : acc[1]) + LDSF4(L_PR + (w ^ 4) * 2048 + 1024 + lane * 16);
    const f4 g0 = LDSF4(L_GA + (32 * nhB + 8 * g) * 4), g1 = LDSF4(L_GA + (32 * nhB + 8 * g) * 4 + 16);
    const v4u a2 = LDS4(L_A2 + (56 * c8 + 32 * nhB + 8 * g) * 2);
    v4u x1;
    x1.x = pk(lo2f(xres.x) + g0.x * e[0], hi2f(xres.x) + g0.y * o[0]);
    x1.y = pk(lo2f(xres.y) + g0.z * e[1], hi2f(xres.y) + g0.w * o[1]);
    x1.z = pk(lo2f(xres.z) + g1.x * e[2], hi2f(xres.z) + g1.y * o[2]);
    x1.w = pk(lo2f(xres.w) + g1.z * e[3], hi2f(xres.w) + g1.w * o[3]);
    v4u y;
#pragma unroll
    for (int j = 0; j < 4; ++j) y[j] = nosent(pk(lo2f(x1[j]) * lo2f(a2[j]), hi2f(x1[j]) * hi2f(a2[j])));
    const bool vld = !(nhB == 1 && g == 3);
    st4s(rws, vld ? lane * 16 : OOB, WS_X1 + (4 * g32 + mtB) * 14336 + (7 * c8 + 4 * nhB) * 256, y);
    float s1 = 0.f, s2 = 0.f;
#pragma unroll
    for (int j = 0; j < 4; ++j) { s1 = dot2(x1[j], 0x3F803F80u, s1); s2 = dot2(x1[j], x1[j], s2); }
    s1 += __shfl_xor(s1, 16); s2 += __shfl_xor(s2, 16);
    s1 += __shfl_xor(s1, 32); s2 += __shfl_xor(s2, 32);
    st2s(rws, g == 0 ? l15 * 8 : OOB, WS_ST + (4 * g32 + mtB) * 2048 + (2 * c8 + nhB) * 128,
         (v2u){nosent(__builtin_bit_cast(u32, s1)), nosent(__builtin_bit_cast(u32, s2))});
    if (vld) LDS4(L_X1S + (mtB * 7 + 4 * nhB) * 256 + lane * 16) = x1;
  }
  STAMPT(8);
  f4 au[2] = {{0, 0, 0, 0}, {0, 0, 0, 0}}, av[2] = {{0, 0, 0, 0}, {0, 0, 0, 0}};
#pragma unroll
  for (int s = 0; s < 14; ++s) {
    const v4u a2 = LDS4(L_A2 + (32 * s + 8 * g) * 2), sb = LDS4(L_SB + (32 * s + 8 * g) * 2);
    au[0] = mfma(w1f[s][0], a2, au[0]); au[1] = mfma(w1f[s][1], a2, au[1]);
    av[0] = mfma(w1f[s][0], sb, av[0]); av[1] = mfma(w1f[s][1], sb, av[1]);
  }
  PIN(au[0]); PIN(au[1]); PIN(av[0]); PIN(av[1]);
  STAMPT(23);
  {
    v4u t[7], st[2];
    const u32 base = WS_X1 + 4 * g32 * 14336 + w * 7168;
    const u32 sto = w < 4 ? lane * 16 : OOB;
    for (unsigned spins = 0;; ++spins) {
      CBAR();
      u32 m = 0;
#pragma unroll
      for (int i = 0; i < 7; ++i) t[i] = ld4s(rws, lane * 16, base + i * 1024);
      st[0] = ld4s(rws, sto, WS_ST + (4 * g32 + (w & 3)) * 2048);
      st[1] = ld4s(rws, sto, WS_ST + (4 * g32 + (w & 3)) * 2048 + 1024);
#pragma unroll
      for (int i = 0; i < 7; ++i) m = chk(m, t[i]);
      m = chk(chk(m, st[0]), st[1]);
      if (!__any(m == ~0u) || spins > SPIN_CAP) break;
      canary(rws, lane < 28 ? base + lane * 256 : ((lane >= 32 && lane < 48 && w < 4) ? WS_ST + (4 * g32 + w) * 2048 + (lane - 32) * 128 : OOB), 0, spins);
    }
    RUNPRIO();
#pragma unroll
    for (int i = 0; i < 7; ++i) LDS4(w * 7168 + i * 1024 + lane * 16) = t[i];
    if (w < 4) {
      f4 a = __builtin_bit_cast(f4, st[0]) + __builtin_bit_cast(f4, st[1]);
#pragma unroll
      for (int j = 0; j < 4; ++j) { a[j] += dppf<0x128>(a[j]); a[j] += __shfl_xor(a[j], 16); a[j] += __shfl_xor(a[j], 32); }
      const float m0 = a.x * (1.f / D_), m1 = a.z * (1.f / D_);
      const float r0 = __builtin_amdgcn_rsqf(fmaxf(a.y * (1.f / D_) - m0 * m0, 0.f) + 1e-5f);
      const float r1 = __builtin_amdgcn_rsqf(fmaxf(a.w * (1.f / D_) - m1 * m1, 0.f) + 1e-5f);
      if (lane < 8) LDSF4(L_MR + w * 128 + lane * 16) = (f4){m0, r0, m1, r1};
    }
    __syncthreads();
  }
  STAMPT(9);
  v4u wf[7][4];
#pragma unroll
  for (int mp = 0; mp < 2; ++mp) {
    f4 acc[2][2] = {{{0, 0, 0, 0}, {0, 0, 0, 0}}, {{0, 0, 0, 0}, {0, 0, 0, 0}}};
    v4u xb[3][2];
#pragma unroll
    for (int m = 0; m < 2; ++m) { xb[0][m] = LDS4((2 * mp + m) * 14336 + lane * 16); xb[1][m] = LDS4((2 * mp + m) * 14336 + 1024 + lane * 16); }
#pragma unroll
    for (int s = 0; s < 14; ++s) {
      if (s < 12) {
#pragma unroll
        for (int m = 0; m < 2; ++m) xb[(s + 2) % 3][m] = LDS4((2 * mp + m) * 14336 + (s + 2) * 1024 + lane * 16);
      }
      SB0();
#pragma unroll
      for (int m = 0; m < 2; ++m) {
        acc[m][0] = mfma(w1f[s][0], xb[s % 3][m], acc[m][0]);
        acc[m][1] = mfma(w1f[s][1], xb[s % 3][m], acc[m][1]);
      }
      SB0();
      wf[(14 * mp + s) >> 2][(14 * mp + s) & 3] = ld4s(rws, lane * 16, WS_W2T + ((c8 * 56 + 7 * w) * 4 + 14 * mp + s) * 1024);
      SB0();
    }
#pragma unroll
    for (int m = 0; m < 2; ++m) {
      const f2 mr2 = *(LAS f2*)(lds + L_MR + ((2 * mp + m) * 16 + l15) * 8);
      const f2 nm = {-mr2.x, -mr2.x}, rs = {mr2.y, mr2.y};
      v4u hv;
#pragma unroll
      for (int r = 0; r < 4; ++r) {
        f2 h = __builtin_elementwise_fma(nm, (f2){au[0][r], au[1][r]}, (f2){acc[m][0][r], acc[m][1][r]});
        h = __builtin_elementwise_fma(rs, h, (f2){av[0][r], av[1][r]});
        hv[r] = pk(fmaxf(h.x, 0.f), fmaxf(h.y, 0.f));
      }
      st4s(rws, w < 7 ? lane * 16 : OOB, WS_HID + (4 * g32 + 2 * mp + m) * 57344 + (28 * c8 + 4 * (w % 7)) * 256, hv);
    }
    SB0();
  }
  STAMPT(10);
  __syncthreads();
  {
    v4u hf[2][2][7];
#define HLOAD(mp) { _Pragma("unroll") for (int i = 0; i < 2; ++i) _Pragma("unroll") for (int s = 0; s < 7; ++s) \
      hf[mp][i][s] = ld4s(rws, lane * 16, WS_HID + (4 * g32 + 2 * (mp) + i) * 57344 + w * 7168 + s * 1024); }
    HLOAD(0)
#pragma unroll
    for (int mp = 0; mp < 2; ++mp) {
      for (unsigned spins = 0;; ++spins) {
        u32 m = 0;
#pragma unroll
        for (int i = 0; i < 2; ++i)
#pragma unroll
          for (int s = 0; s < 7; ++s) m = chk(m, hf[mp][i][s]);
        if (!__any(m == ~0u) || spins > SPIN_CAP) break;
        canary(rws, (lane & 7) < 7 && lane < 16 ? (lane >> 3) * 57344 + (lane & 7) * 1024 : OOB, WS_HID + (4 * g32 + 2 * mp) * 57344 + w * 7168, spins);
        CBAR();
        HLOAD(mp)
      }
      RUNPRIO();
      if (mp == 0) STAMPT(11);
#pragma unroll
      for (int i = 0; i < 2; ++i) {
        f4 acc[4] = {{0, 0, 0, 0}, {0, 0, 0, 0}, {0, 0, 0, 0}, {0, 0, 0, 0}};
#pragma unroll
        for (int s = 0; s < 7; ++s)
#pragma unroll
          for (int t = 0; t < 4; ++t) acc[t] = mfma(wf[s][t], hf[mp][i][s], acc[t]);
#pragma unroll
        for (int r = 0; r < 4; ++r) LDSF4(((w * 4 + 2 * mp + i) * 4 + r) * 1024 + lane * 16) = (f4){acc[0][r], acc[1][r], acc[2][r], acc[3][r]};
        if (mp == 0 && i == 0) { SB0(); HLOAD(1) SB0(); }
      }
    }
  }
  __syncthreads();
  STAMPT(12);
  {
    const int mt = tid >> 7, rp = (tid >> 6) & 1;
    if (!(g == 3 && rp == 1)) {
      f4 s0 = {0, 0, 0, 0}, s1 = {0, 0, 0, 0};
#pragma unroll
      for (int ww = 0; ww < 8; ++ww) {
        s0 += LDSF4(((ww * 4 + mt) * 4 + 2 * rp) * 1024 + lane * 16);
        s1 += LDSF4(((ww * 4 + mt) * 4 + 2 * rp + 1) * 1024 + lane * 16);
      }
      const v4u x1 = LDS4(L_X1S + (mt * 7 + 2 * g + rp) * 256 + l15 * 16);
      const f4 g0 = LDSF4(L_GB + (16 * g + 8 * rp) * 4), g1 = LDSF4(L_GB + (16 * g + 8 * rp) * 4 + 16);
      v4u o;
      o.x = pk(lo2f(x1.x) + g0.x * s0.x, hi2f(x1.x) + g0.y * s0.y);
      o.y = pk(lo2f(x1.y) + g0.z * s0.z, hi2f(x1.y) + g0.w * s0.w);
      o.z = pk(lo2f(x1.z) + g1.x * s1.x, hi2f(x1.z) + g1.y * s1.y);
      o.w = pk(lo2f(x1.w) + g1.z * s1.z, hi2f(x1.w) + g1.w * s1.w);
      __builtin_nontemporal_store(o, (v4u*)(out + (size_t)(64 * g32 + 16 * mt + l15) * D_ + 56 * c8 + 16 * g + 8 * rp));
    }
  }
  if (b == 0 && w == 1) {
    for (unsigned spins = 0;; ++spins) {
      bool ok = true;
#pragma unroll
      for (int i = 0; i < 4; ++i) ok &= (__hip_atomic_load(gr + G_PREP + lane + 64 * i, RLX_AGENT) >> 16) == tag;
      if (__all(ok) || spins > SPIN_CAP) break;
      BACKOFF();
    }
    if (lane == 0) __hip_atomic_store((gu64*)ws, epoch + 1, RLX_AGENT);
  }
  STAMPT(13);
  asm volatile(".Lmega_end:");
}

extern "C" void kernel_launch(void* const* d_in, const int* in_sizes, int n_in, void* d_out, int out_size, void* d_ws,
                              size_t ws_size, hipStream_t stream) {
  (void)hipFuncSetAttribute((const void*)mega, hipFuncAttributeMaxDynamicSharedMemorySize, LDS_BYTES);
  mega<<<256, 512, LDS_BYTES, stream>>>((const u16*)d_in[0], (const u16*)d_in[1], (const u16*)d_in[2], (const u16*)d_in[4],
                                        (const u16*)d_in[5], (const u16*)d_in[6], (const u16*)d_in[7], (u16*)d_out, (char*)d_ws);
}
```

```cpp
#include <hip/hip_runtime.h>
#include <stdint.h>

typedef unsigned int u32;
typedef unsigned long long u64;
typedef unsigned short u16;
typedef u32 v4u __attribute__((ext_vector_type(4)));
typedef u32 v2u __attribute__((ext_vector_type(2)));
typedef float f4 __attribute__((ext_vector_type(4)));
typedef float f2 __attribute__((ext_vector_type(2)));
typedef __bf16 bf2v __attribute__((ext_vector_type(2)));
typedef __bf16 bf8v __attribute__((ext_vector_type(8)));
#define GAS __attribute__((address_space(1)))
#define LAS __attribute__((address_space(3)))
typedef GAS u64 gu64;
#define RLX_AGENT __ATOMIC_RELAXED, __HIP_MEMORY_SCOPE_AGENT
typedef __amdgpu_buffer_rsrc_t rsrc_t;

constexpr int S_ = 2048, D_ = 448, DFF_ = 1792;
constexpr int WS_GRAN = 4096;
constexpr int G_PREP = 2688, G_FILL = 2944;
constexpr int G_REP = 4096;
constexpr int WS_KV = 524288;
constexpr int WS_O = WS_KV + 4194304;
constexpr int WS_X1 = WS_O + 2097152;
constexpr int WS_ST = WS_X1 + 1835008;
constexpr int WS_HID = WS_ST + 262144;
constexpr int WS_W2T = WS_HID + 7340032;
constexpr int WS_W1T = WS_W2T + 1835008;
constexpr int WS_WOT = WS_W1T + 1605632;
constexpr int WS_END = WS_WOT + 524288;
constexpr int WS_DBG = WS_END;
constexpr int L_WQ = 0;
constexpr int L_K = 0, L_V = 24576;
constexpr int L_PR = 57344;
constexpr int L_WO = 86016;
constexpr int L_X = 36864;
constexpr int L_UV = L_WO;
constexpr int L_SM = 151552;
constexpr int L_AS1 = L_SM;
constexpr int L_A2 = L_AS1 + 1792;
constexpr int L_SB = L_A2 + 896;
constexpr int L_GA = L_SB + 896;
constexpr int L_GB = L_GA + 256;
constexpr int L_MR = L_GB + 256;
constexpr int L_RED = L_MR + 512;
constexpr int L_X1S = L_RED;
constexpr int L_END = L_X1S + 7168;
static_assert(L_END <= 163840, "lds");
constexpr int LDS_BYTES = 163840;
constexpr u32 OOB = 0x80000000u;
constexpr unsigned SPIN_CAP = 1u << 15;

__device__ __forceinline__ u32 pk(float lo, float hi) { f2 v = {lo, hi}; return __builtin_bit_cast(u32, __builtin_convertvector(v, bf2v)); }
__device__ __forceinline__ float dot2(u32 a, u32 b, float c) { return __builtin_amdgcn_fdot2_f32_bf16(__builtin_bit_cast(bf2v, a), __builtin_bit_cast(bf2v, b), c, false); }
__device__ __forceinline__ f4 mfma(v4u a, v4u b, f4 c) { return __builtin_amdgcn_mfma_f32_16x16x32_bf16(__builtin_bit_cast(bf8v, a), __builtin_bit_cast(bf8v, b), c, 0, 0, 0); }
__device__ __forceinline__ float lo2f(u32 v) { return __builtin_bit_cast(float, v << 16); }
__device__ __forceinline__ float hi2f(u32 v) { return __builtin_bit_cast(float, v & 0xffff0000u); }
__device__ __forceinline__ u32 plo(u32 r0, u32 r1) { return __builtin_amdgcn_perm(r1, r0, 0x05040100u); }
__device__ __forceinline__ u32 phi(u32 r0, u32 r1) { return __builtin_amdgcn_perm(r1, r0, 0x07060302u); }
__device__ __forceinline__ u32 nosent(u32 v) { return v < 0xFFFFFFFEu ? v : 0xFFFFFFFEu; }
__device__ __forceinline__ v4u nosent4(v4u v) { v.x = nosent(v.x); v.y = nosent(v.y); v.z = nosent(v.z); v.w = nosent(v.w); return v; }
__device__ __forceinline__ u32 umax3(u32 a, u32 b, u32 c) { u32 m = a > b ? a : b; return m > c ? m : c; }
__device__ __forceinline__ u32 chk(u32 m, v4u v) { return umax3(umax3(m, v.x, v.y), v.z, v.w); }
template <int C> __device__ __forceinline__ float dppf(float v) { return __builtin_bit_cast(float, __builtin_amdgcn_update_dpp(0, __builtin_bit_cast(int, v), C, 0xf, 0xf, false)); }
__device__ __forceinline__ float rowsum16(float v) { v += dppf<0xB1>(v); v += dppf<0x4E>(v); v += dppf<0x141>(v); v += dppf<0x140>(v); return v; }
__device__ __forceinline__ v4u ld4(rsrc_t r, u32 vo, u32 so) { return __builtin_amdgcn_raw_buffer_load_b128(r, vo, so, 0); }
__device__ __forceinline__ v4u ld4s(rsrc_t r, u32 vo, u32 so) { return __builtin_amdgcn_raw_buffer_load_b128(r, vo, so, 16); }
__device__ __forceinline__ v2u ld2(rsrc_t r, u32 vo, u32 so) { return __builtin_amdgcn_raw_buffer_load_b64(r, vo, so, 0); }
__device__ __forceinline__ u32 ld1(rsrc_t r, u32 vo, u32 so) { return __builtin_amdgcn_raw_buffer_load_b32(r, vo, so, 0); }
__device__ __forceinline__ void st4s(rsrc_t r, u32 vo, u32 so, v4u v) { __builtin_amdgcn_raw_buffer_store_b128(v, r, vo + so, 0, 16); }
__device__ __forceinline__ void st2s(rsrc_t r, u32 vo, u32 so, v2u v) { __builtin_amdgcn_raw_buffer_store_b64(v, r, vo, so, 16); }
__device__ __forceinline__ v4u modulate(const u32 (&xh)[8], v4u a0, v4u a1) {
  v4u h;
  float d0, d1, d2, d3, d4, d5, d6, d7;
  asm volatile(
      "v_dot2_f32_bf16 %4, %12, %20, 0\n\tv_dot2_f32_bf16 %5, %13, %21, 0\n\tv_dot2_f32_bf16 %6, %14, %22, 0\n\tv_dot2_f32_bf16 %7, %15, %23, 0\n\t"
      "v_dot2_f32_bf16 %8, %16, %24, 0\n\tv_dot2_f32_bf16 %9, %17, %25, 0\n\tv_dot2_f32_bf16 %10, %18, %26, 0\n\tv_dot2_f32_bf16 %11, %19, %27, 0\n\t"
      "v_cvt_pk_bf16_f32 %0, %4, %5\n\tv_cvt_pk_bf16_f32 %1, %6, %7\n\tv_cvt_pk_bf16_f32 %2, %8, %9\n\tv_cvt_pk_bf16_f32 %3, %10, %11\n\ts_nop 1"
      : "=&v"(h.x), "=&v"(h.y), "=&v"(h.z), "=&v"(h.w), "=&v"(d0), "=&v"(d1), "=&v"(d2), "=&v"(d3), "=&v"(d4), "=&v"(d5), "=&v"(d6), "=&v"(d7)
      : "v"(xh[0]), "v"(xh[1]), "v"(xh[2]), "v"(xh[3]), "v"(xh[4]), "v"(xh[5]), "v"(xh[6]), "v"(xh[7]),
        "v"(a0.x), "v"(a0.y), "v"(a0.z), "v"(a0.w), "v"(a1.x), "v"(a1.y), "v"(a1.z), "v"(a1.w));
  return h;
}
typedef short s4v __attribute__((ext_vector_type(4)));
__device__ __forceinline__ v2u trrd(LAS char* p) { return __builtin_bit_cast(v2u, __builtin_amdgcn_ds_read_tr16_b64_v4i16((LAS s4v*)p)); }
__device__ __forceinline__ v4u mksrd(const void* p, u32 bytes) {
  const u64 a = (u64)p;
  return (v4u){(u32)__builtin_amdgcn_readfirstlane((u32)a), (u32)__builtin_amdgcn_readfirstlane((u32)(a >> 32)) & 0xffffu, bytes, 0x00020000u};
}
template <bool SC1> __device__ __forceinline__ void dma16(v4u srd, u32 vo, u32 so, u32 ldsaddr) {
  u32 keep;
  so = __builtin_amdgcn_readfirstlane(so);
  ldsaddr = __builtin_amdgcn_readfirstlane(ldsaddr);
  if (SC1)
    asm volatile("s_mov_b32 %0, m0\n\ts_mov_b32 m0, %4\n\ts_nop 3\n\tbuffer_load_dwordx4 %1, %2, %3 offen sc1 lds\n\ts_mov_b32 m0, %0"
                 : "=&s"(keep) : "v"(vo), "s"(srd), "s"(so), "s"(ldsaddr) : "memory");
  else
    asm volatile("s_mov_b32 %0, m0\n\ts_mov_b32 m0, %4\n\ts_nop 3\n\tbuffer_load_dwordx4 %1, %2, %3 offen lds\n\ts_mov_b32 m0, %0"
                 : "=&s"(keep) : "v"(vo), "s"(srd), "s"(so), "s"(ldsaddr) : "memory");
}
__device__ __forceinline__ void canary(rsrc_t r, u32 vo, u32 so, unsigned& spins) {
  __builtin_amdgcn_s_setprio(0);
  for (; spins <= SPIN_CAP; ++spins) {
    __builtin_amdgcn_s_sleep(2);
    asm volatile("" ::: "memory");
    if (!__any(__builtin_amdgcn_raw_buffer_load_b32(r, vo, so, 16) == ~0u)) break;
  }
}
#define LDS4(off) (*(LAS v4u*)(lds + (off)))
#define LDS2(off) (*(LAS v2u*)(lds + (off)))
#define LDSF4(off) (*(LAS f4*)(lds + (off)))
#define LDSF(off) (*(LAS float*)(lds + (off)))
#define LDSH(off) (*(LAS u16*)(lds + (off)))
#define CBAR() asm volatile("" ::: "memory")
#define PIN(v) asm volatile("" : "+v"(v))
#define SB0() __builtin_amdgcn_sched_barrier(0)
#define BACKOFF() do { __builtin_amdgcn_s_setprio(0); __builtin_amdgcn_s_sleep(6); } while (0)
#define RUNPRIO() __builtin_amdgcn_s_setprio(1)
#define DRAIN() asm volatile("s_waitcnt vmcnt(0)" ::: "memory")
#define STAMPT(i)

__global__ __launch_bounds__(512, 2) void mega(const u16* __restrict__ x, const u16* __restrict__ wqkv, const u16* __restrict__ wo,
                                               const u16* __restrict__ w1, const u16* __restrict__ w2, const u16* __restrict__ cvec,
                                               const u16* __restrict__ wmod, u16* __restrict__ out, char* ws) {
  extern __shared__ __attribute__((aligned(16))) char lds_g[];
  LAS char* lds = (LAS char*)lds_g;
  const int tid = threadIdx.x, lane = tid & 63, w = __builtin_amdgcn_readfirstlane(tid >> 6) & 7;
  const int l15 = lane & 15, g = lane >> 4;
  const int b = blockIdx.x, xcd = b & 7, slot = b >> 3;
  const int g16 = xcd * 2 + (slot >> 4), head = slot & 15;
  const int g32 = xcd * 4 + (slot >> 3), c8 = slot & 7;
  const rsrc_t rws = __builtin_amdgcn_make_buffer_rsrc(ws, 0, WS_END, 0x00020000);
  const rsrc_t rx = __builtin_amdgcn_make_buffer_rsrc((void*)x, 0, S_ * D_ * 2, 0x00020000);
  const rsrc_t rwq = __builtin_amdgcn_make_buffer_rsrc((void*)wqkv, 0, D_ * 3 * D_ * 2, 0x00020000);
  const rsrc_t rwm = __builtin_amdgcn_make_buffer_rsrc((void*)wmod, 0, D_ * 6 * D_ * 2, 0x00020000);
  const rsrc_t rc = __builtin_amdgcn_make_buffer_rsrc((void*)cvec, 0, D_ * 2, 0x00020000);
  const v4u SENT = {~0u, ~0u, ~0u, ~0u};
  STAMPT(0);
  RUNPRIO();
  u32 cpf, mpf;
  u64 epoch;
  {
    u64 pc;
    u32 len;
    asm volatile("s_getpc_b64 %0\n.Lmega_start:\n\ts_mov_b32 %1, .Lmega_end-.Lmega_start" : "=s"(pc), "=s"(len));
    const v4u scode = mksrd((const void*)pc, len), swm0 = mksrd(wmod, D_ * 6 * D_ * 2), sws0 = mksrd(ws, 64);
    const int idx = slot * 84 + tid - 320, n = idx / 448, row = idx - 448 * n, j = xcd + 8 * n;
    asm volatile("s_nop 4\n\tbuffer_load_dword %0, %3, %5, 0 offen\n\tbuffer_load_dword %1, %4, %6, 0 offen\n\tbuffer_load_dwordx2 %2, off, %7, 0 sc1"
                 : "=&v"(cpf), "=&v"(mpf), "=&v"(epoch)
                 : "v"(tid >= 448 ? (lane * 8 + (slot & 7)) * 128 : OOB), "v"((tid >= 320 && tid < 404 && j < 42) ? (u32)(row * 5376 + j * 128) : OOB),
                   "s"(scode), "s"(swm0), "s"(sws0)
                 : "memory");
  }
  gu64* gr0 = (gu64*)(ws + WS_GRAN);
  gu64* gr = gr0 + xcd * G_REP;
  const int mj = xcd + 8 * (slot / 3);
  const bool modw = (slot % 3 == 0) && mj < 42 && w < 4;
  if (tid < 4) *(volatile LAS u32*)(lds + L_RED + 3072 + tid * 4) = 0u;
  v2u mr[4][8];
  u32 craw;
  if (modw) {
    const v4u swm = mksrd(wmod, D_ * 6 * D_ * 2), sc_ = mksrd(cvec, D_ * 2);
    asm volatile("s_nop 4\n\tbuffer_load_dword %0, %1, %2, 0 offen" : "=&v"(craw) : "v"(w + 4 * g < 14 ? (32 * (w + 4 * g) + 2 * l15) * 2 : OOB), "s"(sc_) : "memory");
#pragma unroll
    for (int i = 0; i < 4; ++i) {
      const u32 vo = (i < 3 || w < 2) ? (u32)(8 * g * 2688 + 4 * l15) * 2 : OOB;
      const u32 so = (u32)((32 * ((w + 4 * i) % 14)) * 2688 + 64 * mj) * 2;
      asm volatile(
          "s_nop 4\n\t"
          "buffer_load_dwordx2 %0, %8, %9, %10 offen\n\tbuffer_load_dwordx2 %1, %8, %9, %11 offen\n\t"
          "buffer_load_dwordx2 %2, %8, %9, %12 offen\n\tbuffer_load_dwordx2 %3, %8, %9, %13 offen\n\t"
          "buffer_load_dwordx2 %4, %8, %9, %14 offen\n\tbuffer_load_dwordx2 %5, %8, %9, %15 offen\n\t"
          "buffer_load_dwordx2 %6, %8, %9, %16 offen\n\tbuffer_load_dwordx2 %7, %8, %9, %17 offen"
          : "=&v"(mr[i][0]), "=&v"(mr[i][1]), "=&v"(mr[i][2]), "=&v"(mr[i][3]), "=&v"(mr[i][4]), "=&v"(mr[i][5]), "=&v"(mr[i][6]), "=&v"(mr[i][7])
          : "v"(vo), "s"(swm), "s"(so), "s"(so + 5376), "s"(so + 2 * 5376), "s"(so + 3 * 5376), "s"(so + 4 * 5376), "s"(so + 5 * 5376),
            "s"(so + 6 * 5376), "s"(so + 7 * 5376)
          : "memory");
    }
  }
  asm volatile("s_waitcnt lgkmcnt(0)\n\ts_barrier" ::: "memory");
  STAMPT(14);
  const v4u sq_srd = mksrd(wqkv, D_ * 3 * D_ * 2);
  const u32 vq = (u32)((lane >> 2) * 2688 + (((lane & 3) ^ (2 * (lane >> 5))) * 16));
#define WQDMA(i)                                                                                                        \
  {                                                                                                                     \
    const int u = w + 8 * ((i) >> 1), d = 2 * u + ((i) & 1), mat = u / 14, kb = d - 28 * mat;                           \
    dma16<false>(sq_srd, u < 42 ? vq : OOB, (u32)(16 * kb * 2688 + (mat % 3) * 896 + 56 * head), u < 42 ? L_WQ + d * 1024 : L_RED + 4096); \
  }
  {
    const v4u sx = mksrd(x, S_ * D_ * 2);
    const u32 vo = (u32)((lane >> 3) * D_ * 2 + (((lane & 7) ^ (lane >> 3)) * 16));
#pragma unroll
    for (int q = 0; q < 14; ++q) dma16<false>(sx, vo, (u32)((128 * g16 + 16 * w + 8 * (q & 1)) * D_ * 2 + (q >> 1) * 128), L_X + w * 14336 + q * 1024);
  }
  asm volatile("s_barrier" ::: "memory");
  WQDMA(0) WQDMA(1) WQDMA(2) WQDMA(3)
  u32 pr[8];
  u32 pdst;
  {
    const int u = b * 8 + w;
    const u16* base; u32 bytes, ld, so, vo, vo2;
    if (u < 896) {
      const int cc = u / 112, v = u - 112 * cc, kb = v >> 1, hf = v & 1;
      base = w2; bytes = DFF_ * D_ * 2; ld = D_ * 2; so = (u32)(32 * kb * D_ + 56 * cc + 2 * hf) * 2;
      vo = l15 < 14 ? (u32)(8 * g * D_ + 4 * l15) * 2 : OOB; vo2 = vo;
      pdst = WS_W2T + ((cc * 56 + kb) * 4 + 2 * hf) * 1024;
    } else if (u < 1680) {
      const int v = u - 896, cb = v / 14, s = v - 14 * cb;
      base = w1; bytes = DFF_ * D_ * 2; ld = DFF_ * 2; so = (u32)(32 * s * DFF_ + 32 * cb) * 2;
      vo = (u32)(8 * g * DFF_ + 2 * l15) * 2; vo2 = vo;
      pdst = WS_W1T + (cb * 14 + s) * 2048;
    } else {
      const int v = u - 1680, hh = v & 15, nh = (v >> 4) & 1, cc = (v >> 5) & 7;
      base = wo; bytes = D_ * D_ * 2; ld = D_ * 2; so = (u32)(28 * hh * D_ + 56 * cc + 32 * nh) * 2;
      vo = (u < 1936 && (nh == 0 || l15 < 12)) ? (u32)(4 * g * D_ + 2 * l15) * 2 : OOB; vo2 = g < 3 ? vo + 12 * D_ * 2 : OOB;
      pdst = u < 1936 ? WS_WOT + (cc * 32 + nh * 16 + hh) * 2048 : OOB;
    }
    const v4u sp = mksrd(base, __builtin_amdgcn_readfirstlane(bytes));
    so = __builtin_amdgcn_readfirstlane(so); ld = __builtin_amdgcn_readfirstlane(ld);
    asm volatile(
        "s_nop 4\n\t"
        "buffer_load_dword %0, %8, %10, %11 offen\n\tbuffer_load_dword %1, %8, %10, %12 offen\n\t"
        "buffer_load_dword %2, %8, %10, %13 offen\n\tbuffer_load_dword %3, %8, %10, %14 offen\n\t"
        "buffer_load_dword %4, %9, %10, %15 offen\n\tbuffer_load_dword %5, %9, %10, %16 offen\n\t"
        "buffer_load_dword %6, %9, %10, %17 offen\n\tbuffer_load_dword %7, %9, %10, %18 offen"
        : "=&v"(pr[0]), "=&v"(pr[1]), "=&v"(pr[2]), "=&v"(pr[3]), "=&v"(pr[4]), "=&v"(pr[5]), "=&v"(pr[6]), "=&v"(pr[7])
        : "v"(vo), "v"(vo2), "s"(sp), "s"(so), "s"(so + ld), "s"(so + 2 * ld), "s"(so + 3 * ld), "s"(so + 4 * ld), "s"(so + 5 * ld), "s"(so + 6 * ld),
          "s"(so + 7 * ld)
        : "memory");
  }
  SB0();
  asm volatile("s_waitcnt vmcnt(26)" : "+v"(epoch), "+v"(cpf), "+v"(mpf)::"memory");
  const u64 tag = (epoch + 1) & 0xFFFFFFFFFFFFull;
  if (modw) {
    __builtin_amdgcn_s_setprio(3);
    asm volatile(""
                 : "+v"(craw), "+v"(mr[0][0]), "+v"(mr[0][1]), "+v"(mr[0][2]), "+v"(mr[0][3]), "+v"(mr[0][4]), "+v"(mr[0][5]), "+v"(mr[0][6]), "+v"(mr[0][7]),
                   "+v"(mr[1][0]), "+v"(mr[1][1]), "+v"(mr[1][2]), "+v"(mr[1][3]), "+v"(mr[1][4]), "+v"(mr[1][5]), "+v"(mr[1][6]), "+v"(mr[1][7])::"memory");
    asm volatile(""
                 : "+v"(mr[2][0]), "+v"(mr[2][1]), "+v"(mr[2][2]), "+v"(mr[2][3]), "+v"(mr[2][4]), "+v"(mr[2][5]), "+v"(mr[2][6]), "+v"(mr[2][7]),
                   "+v"(mr[3][0]), "+v"(mr[3][1]), "+v"(mr[3][2]), "+v"(mr[3][3]), "+v"(mr[3][4]), "+v"(mr[3][5]), "+v"(mr[3][6]), "+v"(mr[3][7])::"memory");
    float c0 = lo2f(craw), c1 = hi2f(craw);
    c0 = c0 * __builtin_amdgcn_rcpf(1.f + __expf(-c0));
    c1 = c1 * __builtin_amdgcn_rcpf(1.f + __expf(-c1));
    *(LAS u32*)(lds + L_RED + w * 256 + lane * 4) = pk(c0, c1);
    f4 acc[4] = {{0, 0, 0, 0}, {0, 0, 0, 0}, {0, 0, 0, 0}, {0, 0, 0, 0}};
#pragma unroll
    for (int i = 0; i < 4; ++i) {
      const v4u sf = LDS4(L_RED + w * 256 + i * 64 + g * 16);
      acc[0] = mfma((v4u){plo(mr[i][0].x, mr[i][1].x), plo(mr[i][2].x, mr[i][3].x), plo(mr[i][4].x, mr[i][5].x), plo(mr[i][6].x, mr[i][7].x)}, sf, acc[0]);
      acc[1] = mfma((v4u){phi(mr[i][0].x, mr[i][1].x), phi(mr[i][2].x, mr[i][3].x), phi(mr[i][4].x, mr[i][5].x), phi(mr[i][6].x, mr[i][7].x)}, sf, acc[1]);
      acc[2] = mfma((v4u){plo(mr[i][0].y, mr[i][1].y), plo(mr[i][2].y, mr[i][3].y), plo(mr[i][4].y, mr[i][5].y), plo(mr[i][6].y, mr[i][7].y)}, sf, acc[2]);
      acc[3] = mfma((v4u){phi(mr[i][0].y, mr[i][1].y), phi(mr[i][2].y, mr[i][3].y), phi(mr[i][4].y, mr[i][5].y), phi(mr[i][6].y, mr[i][7].y)}, sf, acc[3]);
    }
    if (l15 == 0) {
#pragma unroll
      for (int r = 0; r < 4; ++r) LDSF4(L_RED + 1024 + w * 256 + (16 * g + 4 * r) * 4) = (f4){acc[0][r], acc[1][r], acc[2][r], acc[3][r]};
    }
    asm volatile("s_waitcnt lgkmcnt(0)" ::: "memory");
    u32 old = 0;
    if (lane == 0) old = __hip_atomic_fetch_add((LAS u32*)(lds + L_RED + 3072), 1u, __ATOMIC_RELAXED, __HIP_MEMORY_SCOPE_WORKGROUP);
    if (__builtin_amdgcn_readfirstlane(old) == 3u) {
      CBAR();
      const float sum = (LDSF(L_RED + 1024 + lane * 4) + LDSF(L_RED + 1280 + lane * 4)) + (LDSF(L_RED + 1536 + lane * 4) + LDSF(L_RED + 1792 + lane * 4));
      const u64 gval = (tag << 16) | (u64)(pk(sum, 0.f) & 0xffffu);
#pragma unroll
      for (int r = 0; r < 8; ++r) __hip_atomic_store(gr0 + r * G_REP + 64 * mj + lane, gval, RLX_AGENT);
    }
    RUNPRIO();
  }
  STAMPT(15);
  if (modw) asm volatile("s_waitcnt vmcnt(8)" ::: "memory"); else asm volatile("s_waitcnt vmcnt(12)" ::: "memory");
  STAMPT(16);
  v4u xr[14];
  {
    const u32 ra0 = L_X + w * 14336 + (l15 >> 3) * 1024 + (l15 & 7) * 128 + ((g ^ (l15 & 7)) * 16);
    const u32 ra1 = L_X + w * 14336 + (l15 >> 3) * 1024 + (l15 & 7) * 128 + (((4 + g) ^ (l15 & 7)) * 16);
#pragma unroll
    for (int s = 0; s < 14; ++s) xr[s] = LDS4(((s & 1) ? ra1 : ra0) + (s >> 1) * 2048);
#pragma unroll
    for (int s = 0; s < 14; ++s) PIN(xr[s]);
  }
  asm volatile("s_waitcnt lgkmcnt(0)\n\ts_barrier" ::: "memory");
  STAMPT(17);
  const int mtB = w & 3, nhB = w >> 2;
  v4u xres;
  const int tau = 16 * (w & 1) + l15;
  const u32 koff = (w >> 1) * 2048 + ((tau >> 2) & 1) * 1024 + (g * 16 + 4 * (tau >> 3) + (tau & 3)) * 16;
  const u32 voff = (w >> 1) * 2048 + ((2 * (w & 1) + (g >> 1)) * 16 + l15) * 16 + (g & 1) * 8;
  const u32 kvb = g16 < 15 ? WS_KV + (g16 * 16 + head) * 16384 : OOB;
  u32 xh[14][8];
  {
    const v4u ONES = {0x3F803F80u, 0x3F803F80u, 0x3F803F80u, 0x3F803F80u};
    f4 sm = {0, 0, 0, 0}, gm = {0, 0, 0, 0};
#pragma unroll
    for (int s = 0; s < 14; ++s) {
      if (s == 0 || s == 7) WQDMA(4 + s / 7)
      sm = mfma(ONES, xr[s], sm);
      gm = mfma(xr[s], xr[s], gm);
    }
    float sq = (l15 & 2) ? ((l15 & 1) ? gm[3] : gm[2]) : ((l15 & 1) ? gm[1] : gm[0]);
    sq = __shfl(sq, 16 * (l15 >> 2) + l15);
    const float mean = sm[0] * (1.f / D_);
    const float rstd = __builtin_amdgcn_rsqf(fmaxf(sq * (1.f / D_) - mean * mean, 0.f) + 1e-5f);
    const f2 rs2 = {rstd, rstd}, nm2 = {-mean * rstd, -mean * rstd};
#pragma unroll
    for (int s = 0; s < 14; ++s) {
      if (s < 6) WQDMA(6 + s)
      else if (s == 7) xres = ld4(rx, (nhB == 1 && g == 3) ? OOB : (u32)(l15 * D_ + 8 * g) * 2, (u32)((64 * g32 + 16 * mtB) * D_ + 56 * c8 + 32 * nhB) * 2);
      else if (s == 9) st4s(rws, koff, kvb, SENT);
      else if (s == 11) st4s(rws, (w >> 1) * 2048 + (w & 1) * 512 + (lane & 31) * 16 + (lane >> 5) * 1024, kvb + 8192, SENT);
      else if (s == 13) st4s(rws, lane * 16, WS_O + ((8 * g16 + w) * 16 + head) * 1024, SENT);
#pragma unroll
      for (int j = 0; j < 4; ++j) {
        const f2 v = __builtin_elementwise_fma((f2){lo2f(xr[s][j]), hi2f(xr[s][j])}, rs2, nm2);
        xh[s][2 * j] = pk(v.x, 1.f);
        xh[s][2 * j + 1] = pk(v.y, 1.f);
        PIN(xh[s][2 * j]); PIN(xh[s][2 * j + 1]);
      }
      SB0();
    }
  }
  STAMPT(20);
  const int i1 = tid < 384 ? 512 + tid : tid;
  u64 g10 = __hip_atomic_load(gr + tid, RLX_AGENT), g11 = __hip_atomic_load(gr + i1, RLX_AGENT);
  asm volatile("s_waitcnt vmcnt(6)"
               : "+v"(pr[0]), "+v"(pr[1]), "+v"(pr[2]), "+v"(pr[3]), "+v"(pr[4]), "+v"(pr[5]), "+v"(pr[6]), "+v"(pr[7])::"memory");
  {
    LAS char* tb0 = lds + L_WQ + (8 * g + (l15 >> 2)) * 64 + (g & 1) * 32 + (l15 & 3) * 8;
    LAS char* tb1 = lds + L_WQ + (8 * g + (l15 >> 2)) * 64 + ((g & 1) ^ 1) * 32 + (l15 & 3) * 8;
    v2u r[6][4];
#pragma unroll
    for (int i = 0; i < 6; ++i) {
      const int u = (i < 5 || w < 2) ? w + 8 * i : w;
      r[i][0] = trrd(tb0 + u * 2048); r[i][1] = trrd(tb0 + u * 2048 + 256);
      r[i][2] = trrd(tb1 + u * 2048); r[i][3] = trrd(tb1 + u * 2048 + 256);
    }
    SB0();
#pragma unroll
    for (int i = 0; i < 6; ++i) {
      const int u = (i < 5 || w < 2) ? w + 8 * i : w;
      LDS4(L_WQ + u * 2048 + lane * 16) = (v4u){r[i][0].x, r[i][0].y, r[i][1].x, r[i][1].y};
      LDS4(L_WQ + u * 2048 + 1024 + lane * 16) = (v4u){r[i][2].x, r[i][2].y, r[i][3].x, r[i][3].y};
    }
  }
  v4u pe = {plo(pr[0], pr[1]), plo(pr[2], pr[3]), plo(pr[4], pr[5]), plo(pr[6], pr[7])};
  v4u po = {phi(pr[0], pr[1]), phi(pr[2], pr[3]), phi(pr[4], pr[5]), phi(pr[6], pr[7])};
  PIN(pe); PIN(po);
  STAMPT(2);
  {
    for (unsigned spins = 0;; ++spins) {
      const bool ok = (g10 >> 16) == tag && (g11 >> 16) == tag;
      if (__all(ok) || spins > SPIN_CAP) break;
      BACKOFF();
      g10 = __hip_atomic_load(gr + tid, RLX_AGENT);
      g11 = __hip_atomic_load(gr + i1, RLX_AGENT);
    }
    RUNPRIO();
    if (tid < 448) LDSH(L_AS1 + tid * 4 + 2) = (u16)g10;
    else LDSH(L_AS1 + (tid - 448) * 4) = (u16)pk(1.f + lo2f((u32)g10), 0.f);
    if (tid < 384) LDSH(L_AS1 + (64 + tid) * 4) = (u16)pk(1.f + lo2f((u32)g11), 0.f);
  }
  DRAIN();
  __syncthreads();
  if (tid < 8) __hip_atomic_store(gr0 + tid * G_REP + G_FILL + b, tag << 16, RLX_AGENT);
  STAMPT(3);
  const int jg = tid - 448;
  const int i20 = tid < 448 ? 1344 + tid : 896 + 56 * c8 + (jg < 56 ? jg : 0);
  const int i21 = tid < 448 ? 1792 + tid : 2240 + 56 * c8 + (jg < 56 ? jg : 0);
  u64 g20 = __hip_atomic_load(gr + i20, RLX_AGENT), g21 = __hip_atomic_load(gr + i21, RLX_AGENT);

  v4u hfr[14];
  {
    v4u as[3][2];
#pragma unroll
    for (int s = 0; s < 2; ++s) { as[s][0] = LDS4(L_AS1 + (32 * s + 8 * g) * 4); as[s][1] = LDS4(L_AS1 + (32 * s + 8 * g) * 4 + 16); }
#pragma unroll
    for (int s = 0; s < 14; ++s) {
      if (s < 12) { as[(s + 2) % 3][0] = LDS4(L_AS1 + (32 * (s + 2) + 8 * g) * 4); as[(s + 2) % 3][1] = LDS4(L_AS1 + (32 * (s + 2) + 8 * g) * 4 + 16); }
      SB0();
      hfr[s] = modulate(xh[s], as[s % 3][0], as[s % 3][1]);
      SB0();
    }
  }
  STAMPT(29);
  int dep = b;
  if (lane < 16) dep = xcd + 8 * (16 * (g16 & 1) + lane);
  else if (lane == 16) dep = g16 >= 1 ? ((g16 - 1) >> 1) + 8 * (16 * ((g16 - 1) & 1) + head) : b;
  else if (lane == 17) dep = g16 >= 2 ? ((g16 - 2) >> 1) + 8 * (16 * (g16 & 1) + head) : b;
  else if (lane < 32) dep = 14 * c8 + (lane - 18);
  else if (lane < 46) dep = ((896 + 98 * c8) >> 3) + (lane - 32);
  else if (lane < 50) dep = 210 + 4 * c8 + (lane - 46);
  u64 dfl = tag << 16;
  f4 aq[6];
#pragma unroll
  for (int t = 0; t < 6; ++t) aq[t] = (f4){0, 0, 0, 0};
  {
    v4u fb[3][6];
#define QLOAD(s, d) { _Pragma("unroll") for (int t = 0; t < 6; ++t) fb[d][t] = LDS4(L_WQ + ((t >> 1) * 14 + (s)) * 2048 + (t & 1) * 1024 + lane * 16); }
    QLOAD(0, 0)
    QLOAD(1, 1)
#pragma unroll
    for (int s = 0; s < 14; ++s) {
      if (s < 12) QLOAD(s + 2, (s + 2) % 3)
      if (s == 0) st4s(rws, lane * 16, pdst, pe);
      if (s == 2) st4s(rws, lane * 16, pdst + 1024, po);
      if (s == 4) st4s(rws, (w < 4 || lane < 48) ? lane * 16 : OOB, WS_X1 + (4 * g32 + (w & 3)) * 14336 + c8 * 1792 + (w >> 2) * 1024, SENT);
      if (s == 5) st4s(rws, (w < 4 && lane < 16) ? lane * 16 : OOB, WS_ST + (4 * g32 + (w & 3)) * 2048 + c8 * 256, SENT);
      if (s == 8 && w == 0) dfl = __hip_atomic_load(gr + G_FILL + (lane < 18 ? dep : b), RLX_AGENT);
      SB0();
#pragma unroll
      for (int t = 0; t < 4; ++t) aq[t] = mfma(fb[s % 3][t], hfr[s], aq[t]);
#pragma unroll
      for (int t = 4; t < 6; ++t) aq[t] = mfma(hfr[s], fb[s % 3][t], aq[t]);
      SB0();
    }
  }
  STAMPT(30);
  const v4u qf = {pk(aq[0][0], aq[0][1]), pk(aq[0][2], aq[0][3]), g < 3 ? pk(aq[1][0], aq[1][1]) : 0u, g < 3 ? pk(aq[1][2], aq[1][3]) : 0u};
  {
    const v4u kf = nosent4((v4u){pk(aq[2][0], aq[2][1]), pk(aq[2][2], aq[2][3]), pk(aq[3][0], aq[3][1]), pk(aq[3][2], aq[3][3])});
    v2u ve = {pk(aq[4][0], aq[4][1]), pk(aq[4][2], aq[4][3])}, vo_ = {pk(aq[5][0], aq[5][1]), pk(aq[5][2], aq[5][3])};
    if (l15 == 12) vo_ = (v2u){0x3F803F80u, 0x3F803F80u};
    ve.x = nosent(ve.x); ve.y = nosent(ve.y); vo_.x = nosent(vo_.x); vo_.y = nosent(vo_.y);
    {
      DRAIN();
      u32 old = 0;
      if (lane == 0) old = __hip_atomic_fetch_add((LAS u32*)(lds + L_RED + 3080), 1u, __ATOMIC_RELAXED, __HIP_MEMORY_SCOPE_WORKGROUP);
      if (__builtin_amdgcn_readfirstlane(old) == 7u && lane < 8) __hip_atomic_store(gr0 + lane * G_REP + G_PREP + b, tag << 16, RLX_AGENT);
    }
    st4s(rws, koff, kvb, kf);
    st2s(rws, voff, kvb + 8192, ve);
    st2s(rws, voff, kvb + 8192 + 1024, vo_);
    STAMPT(31);
    for (unsigned spins = 0;; ++spins) {
      bool ok = (g20 >> 16) == tag && (g21 >> 16) == tag;
      ok &= (dfl >> 16) == tag;
      if (__all(ok) || spins > SPIN_CAP) break;
      BACKOFF();
      if (w == 0) dfl = __hip_atomic_load(gr + G_FILL + (lane < 18 ? dep : b), RLX_AGENT);
      g20 = __hip_atomic_load(gr + i20, RLX_AGENT);
      g21 = __hip_atomic_load(gr + i21, RLX_AGENT);
    }
    RUNPRIO();
    if (tid < 448) {
      LDSH(L_SB + tid * 2) = (u16)g20;
      LDSH(L_A2 + tid * 2) = (u16)pk(1.f + lo2f((u32)g21), 0.f);
    } else {
      LDSF(L_GA + jg * 4) = lo2f((u32)g20);
      LDSF(L_GB + jg * 4) = lo2f((u32)g21);
    }
    STAMPT(21);
    __syncthreads();
    STAMPT(4);
    LDS4(L_K + 16384 + koff) = kf;
    LDS2(L_V + 16384 + voff) = ve;
    LDS2(L_V + 16384 + 1024 + voff) = vo_;
  }
  STAMPT(22);
  v4u w1f[14][2];
  const u32 vo1 = w < 7 ? lane * 16 : OOB;
  const v4u sws = mksrd(ws, WS_END);
  {
    const u32 b0 = g16 >= 2 ? WS_KV + ((g16 - 2) * 16 + head) * 16384 : OOB;
    const u32 b1 = g16 >= 1 ? WS_KV + ((g16 - 1) * 16 + head) * 16384 : OOB;
    v4u k0, v0, k1, v1;
    for (unsigned spins = 0;; ++spins) {
      CBAR();
      k0 = ld4s(rws, tid * 16, b0); v0 = ld4s(rws, tid * 16, b0 + 8192);
      k1 = ld4s(rws, tid * 16, b1); v1 = ld4s(rws, tid * 16, b1 + 8192);
      const bool bad = chk(chk(chk(chk(0u, k0), v0), k1), v1) == ~0u;
      if (!__any(bad) || spins > SPIN_CAP) break;
      {
        const u32 pb = (lane & 16) ? b1 : b0;
        canary(rws, lane < 32 ? pb + ((lane >> 3) & 1) * 8192 + w * 1024 + (lane & 7) * 128 : OOB, 0, spins);
      }
    }
    RUNPRIO();
    STAMPT(24);
    LDS4(L_K + tid * 16) = k0; LDS4(L_V + tid * 16) = v0;
    LDS4(L_K + 8192 + tid * 16) = k1; LDS4(L_V + 8192 + tid * 16) = v1;
  }
  if (w == 0) {
    for (unsigned spins = 0;; ++spins) {
      if (__all((__hip_atomic_load(gr + G_PREP + (lane >= 18 ? dep : b), RLX_AGENT) >> 16) == tag) || spins > SPIN_CAP) break;
      BACKOFF();
    }
    RUNPRIO();
  }
  DRAIN();
  __syncthreads();
  STAMPT(5);
  {
    const int blk0 = w >> 1;
    f4 sc[18];
    {
      v4u kf[18];
#pragma unroll
      for (int t = 0; t < 6; ++t) kf[t] = LDS4(L_K + (blk0 + (t >> 1)) * 2048 + (t & 1) * 1024 + lane * 16);
#pragma unroll
      for (int t = 0; t < 18; ++t) {
        if (t + 6 < 18) kf[t + 6] = LDS4(L_K + (blk0 + ((t + 6) >> 1)) * 2048 + (t & 1) * 1024 + lane * 16);
        if ((t & 1) && t < 16) dma16<true>(sws, lane * 16, WS_WOT + c8 * 65536 + (w * 8 + (t >> 1)) * 1024, L_WO + (w * 8 + (t >> 1)) * 1024);
        SB0();
        sc[t] = mfma(kf[t], qf, (f4){0, 0, 0, 0});
        SB0();
      }
    }
    STAMPT(25);
    v4u vf[18];
#pragma unroll
    for (int t = 0; t < 4; ++t) vf[t] = LDS4(L_V + (blk0 + (t >> 1)) * 2048 + (t & 1) * 1024 + lane * 16);
    const int ql = 256 + 16 * w + l15;
    const int kmin = (256 - 128 * g16) > (ql - 255) ? (256 - 128 * g16) : (ql - 255);
    const int kb0 = 32 * blk0 + 8 * g;
    const float NEG = -1e30f;
    if (g16 < 2) {
#pragma unroll
      for (int t = 2; t < 16; ++t)
#pragma unroll
        for (int r = 0; r < 4; ++r) sc[t][r] = (kb0 + 32 * (t >> 1) + 4 * (t & 1) + r) >= kmin ? sc[t][r] : NEG;
    }
#pragma unroll
    for (int t = 0; t < 2; ++t)
#pragma unroll
      for (int r = 0; r < 4; ++r) sc[t][r] = (kb0 + 4 * t + r) >= kmin ? sc[t][r] : NEG;
#pragma unroll
    for (int t = 16; t < 18; ++t)
#pragma unroll
      for (int r = 0; r < 4; ++r) sc[t][r] = (kb0 + 256 + 4 * (t & 1) + r) <= ql ? sc[t][r] : NEG;
    float mx = NEG;
#pragma unroll
    for (int t = 0; t < 18; ++t) mx = fmaxf(fmaxf(mx, sc[t][0]), fmaxf(sc[t][1], fmaxf(sc[t][2], sc[t][3])));
    mx = fmaxf(mx, __shfl_xor(mx, 16));
    mx = fmaxf(mx, __shfl_xor(mx, 32));
    STAMPT(26);
    STAMPT(18);
    const float cs = 0.18898223650461362f * 1.4426950408889634f;
    const float mc = mx * cs;
    f4 oe = {0, 0, 0, 0}, oo = {0, 0, 0, 0};
#pragma unroll
    for (int bb = 0; bb < 9; ++bb) {
      if (bb + 2 < 9) {
        vf[2 * bb + 4] = LDS4(L_V + (blk0 + bb + 2) * 2048 + lane * 16);
        vf[2 * bb + 5] = LDS4(L_V + (blk0 + bb + 2) * 2048 + 1024 + lane * 16);
      }
      w1f[bb >> 1][bb & 1] = ld4s(rws, vo1, WS_W1T + (7 * c8 + (w % 7)) * 28672 + bb * 1024);
      if ((bb & 1) == 0 && bb < 8) st4s(rws, vo1, WS_HID + (4 * g32 + (bb >> 1)) * 57344 + c8 * 7168 + (w % 7) * 1024, SENT);
      SB0();
      float p[8];
#pragma unroll
      for (int r = 0; r < 4; ++r) {
        p[r] = __builtin_amdgcn_exp2f(sc[2 * bb][r] * cs - mc);
        p[4 + r] = __builtin_amdgcn_exp2f(sc[2 * bb + 1][r] * cs - mc);
      }
      const v4u pf = {pk(p[0], p[1]), pk(p[2], p[3]), pk(p[4], p[5]), pk(p[6], p[7])};
      oe = mfma(vf[2 * bb], pf, oe);
      oo = mfma(vf[2 * bb + 1], pf, oo);
      SB0();
    }
    const float den = __shfl(oo[0], 48 + l15);
    const float inv = __builtin_amdgcn_rcpf(den);
    const v4u of = nosent4((v4u){pk(oe[0] * inv, oe[1] * inv), pk(oe[2] * inv, oe[3] * inv), pk(oo[0] * inv, oo[1] * inv), pk(oo[2] * inv, oo[3] * inv)});
    st4s(rws, lane * 16, WS_O + ((8 * g16 + w) * 16 + head) * 1024, of);
  }
  STAMPT(6);
  SB0();
  asm volatile("s_waitcnt vmcnt(14)" ::: "memory");
#pragma unroll
  for (int i = 9; i < 17; ++i) w1f[i >> 1][i & 1] = ld4s(rws, vo1, WS_W1T + (7 * c8 + (w % 7)) * 28672 + i * 1024);
  STAMPT(19);
  STAMPT(27);
  __syncthreads();
  STAMPT(28);

  {
    v4u of[8];
    const u32 base = WS_O + (4 * g32 + mtB) * 16384 + nhB * 8192;
    v4u wpre[4][4];
#pragma unroll
    for (int h = 0; h < 4; ++h)
#pragma unroll
      for (int t = 0; t < 4; ++t) { wpre[h][t] = LDS4(L_WO + ((t >> 1) * 16 + nhB * 8 + h) * 2048 + (t & 1) * 1024 + lane * 16); PIN(wpre[h][t]); }
    for (unsigned spins = 0;; ++spins) {
      CBAR();
      u32 m = 0;
#pragma unroll
      for (int h = 0; h < 8; ++h) of[h] = ld4s(rws, lane * 16, base + h * 1024);
#pragma unroll
      for (int h = 0; h < 8; ++h) m = chk(m, of[h]);
      if (!__any(m == ~0u) || spins > SPIN_CAP) break;
      canary(rws, lane < 8 ? lane * 1024 : OOB, base, spins);
    }
    RUNPRIO();
    DRAIN();
    STAMPT(7);
    f4 acc[4] = {{0, 0, 0, 0}, {0, 0, 0, 0}, {0, 0, 0, 0}, {0, 0, 0, 0}};
#pragma unroll
    for (int h = 0; h < 8; ++h) {
#pragma unroll
      for (int t = 0; t < 4; ++t) acc[t] = mfma(wpre[h & 3][t], of[h], acc[t]);
      SB0();
      if (h < 4) {
#pragma unroll
        for (int t = 0; t < 4; ++t) wpre[h][t] = LDS4(L_WO + ((t >> 1) * 16 + nhB * 8 + h + 4) * 2048 + (t & 1) * 1024 + lane * 16);
      }
      if (h >= 2) {
#pragma unroll
        for (int i = 17 + 2 * (h - 2); i < 19 + 2 * (h - 2) && i < 28; ++i) w1f[i >> 1][i & 1] = ld4s(rws, vo1, WS_W1T + (7 * c8 + (w % 7)) * 28672 + i * 1024);
      }
      SB0();
    }
    LDSF4(L_PR + w * 2048 + lane * 16) = nhB ? acc[0] : acc[2];
    LDSF4(L_PR + w * 2048 + 1024 + lane * 16) = nhB ? acc[1] : acc[3];
    __syncthreads();
    const f4 e = (nhB ? acc[2] : acc[0]) + LDSF4(L_PR + (w ^ 4) * 2048 + lane * 16);
    const f4 o = (nhB ? acc[3] : acc[1]) + LDSF4(L_PR + (w ^ 4) * 2048 + 1024 + lane * 16);
    const f4 g0 = LDSF4(L_GA + (32 * nhB + 8 * g) * 4), g1 = LDSF4(L_GA + (32 * nhB + 8 * g) * 4 + 16);
    const v4u a2 = LDS4(L_A2 + (56 * c8 + 32 * nhB + 8 * g) * 2);
    v4u x1;
    x1.x = pk(lo2f(xres.x) + g0.x * e[0], hi2f(xres.x) + g0.y * o[0]);
    x1.y = pk(lo2f(xres.y) + g0.z * e[1], hi2f(xres.y) + g0.w * o[1]);
    x1.z = pk(lo2f(xres.z) + g1.x * e[2], hi2f(xres.z) + g1.y * o[2]);
    x1.w = pk(lo2f(xres.w) + g1.z * e[3], hi2f(xres.w) + g1.w * o[3]);
    v4u y;
#pragma unroll
    for (int j = 0; j < 4; ++j) y[j] = nosent(pk(lo2f(x1[j]) * lo2f(a2[j]), hi2f(x1[j]) * hi2f(a2[j])));
    const bool vld = !(nhB == 1 && g == 3);
    st4s(rws, vld ? lane * 16 : OOB, WS_X1 + (4 * g32 + mtB) * 14336 + (7 * c8 + 4 * nhB) * 256, y);
    float s1 = 0.f, s2 = 0.f;
#pragma unroll
    for (int j = 0; j < 4; ++j) { s1 = dot2(x1[j], 0x3F803F80u, s1); s2 = dot2(x1[j], x1[j], s2); }
    s1 += __shfl_xor(s1, 16); s2 += __shfl_xor(s2, 16);
    s1 += __shfl_xor(s1, 32); s2 += __shfl_xor(s2, 32);
    st2s(rws, g == 0 ? l15 * 8 : OOB, WS_ST + (4 * g32 + mtB) * 2048 + (2 * c8 + nhB) * 128,
         (v2u){nosent(__builtin_bit_cast(u32, s1)), nosent(__builtin_bit_cast(u32, s2))});
    if (vld) LDS4(L_X1S + (mtB * 7 + 4 * nhB) * 256 + lane * 16) = x1;
  }
  STAMPT(8);
#pragma unroll
  for (int i = 0; i < 8; ++i) dma16<true>(sws, lane * 16, WS_W2T + ((c8 * 56 + 7 * w) * 4 + 20 + i) * 1024, L_WO + (w * 8 + i) * 1024);
  f4 au[2] = {{0, 0, 0, 0}, {0, 0, 0, 0}}, av[2] = {{0, 0, 0, 0}, {0, 0, 0, 0}};
#pragma unroll
  for (int s = 0; s < 14; ++s) {
    const v4u a2 = LDS4(L_A2 + (32 * s + 8 * g) * 2), sb = LDS4(L_SB + (32 * s + 8 * g) * 2);
    au[0] = mfma(w1f[s][0], a2, au[0]); au[1] = mfma(w1f[s][1], a2, au[1]);
    av[0] = mfma(w1f[s][0], sb, av[0]); av[1] = mfma(w1f[s][1], sb, av[1]);
  }
  PIN(au[0]); PIN(au[1]); PIN(av[0]); PIN(av[1]);
  STAMPT(23);
  {
    v4u t[7], st[2];
    const u32 base = WS_X1 + 4 * g32 * 14336 + w * 7168;
    const u32 sto = w < 4 ? lane * 16 : OOB;
    for (unsigned spins = 0;; ++spins) {
      CBAR();
      u32 m = 0;
#pragma unroll
      for (int i = 0; i < 7; ++i) t[i] = ld4s(rws, lane * 16, base + i * 1024);
      st[0] = ld4s(rws, sto, WS_ST + (4 * g32 + (w & 3)) * 2048);
      st[1] = ld4s(rws, sto, WS_ST + (4 * g32 + (w & 3)) * 2048 + 1024);
#pragma unroll
      for (int i = 0; i < 7; ++i) m = chk(m, t[i]);
      m = chk(chk(m, st[0]), st[1]);
      if (!__any(m == ~0u) || spins > SPIN_CAP) break;
      canary(rws, lane < 28 ? base + lane * 256 : ((lane >= 32 && lane < 48 && w < 4) ? WS_ST + (4 * g32 + w) * 2048 + (lane - 32) * 128 : OOB), 0, spins);
    }
    RUNPRIO();
#pragma unroll
    for (int i = 0; i < 7; ++i) LDS4(w * 7168 + i * 1024 + lane * 16) = t[i];
    if (w < 4) {
      f4 a = __builtin_bit_cast(f4, st[0]) + __builtin_bit_cast(f4, st[1]);
#pragma unroll
      for (int j = 0; j < 4; ++j) { a[j] += dppf<0x128>(a[j]); a[j] += __shfl_xor(a[j], 16); a[j] += __shfl_xor(a[j], 32); }
      const float m0 = a.x * (1.f / D_), m1 = a.z * (1.f / D_);
      const float r0 = __builtin_amdgcn_rsqf(fmaxf(a.y * (1.f / D_) - m0 * m0, 0.f) + 1e-5f);
      const float r1 = __builtin_amdgcn_rsqf(fmaxf(a.w * (1.f / D_) - m1 * m1, 0.f) + 1e-5f);
      if (lane < 8) LDSF4(L_MR + w * 128 + lane * 16) = (f4){m0, r0, m1, r1};
    }
    __syncthreads();
  }
  STAMPT(9);
  v4u wf[7][4];
  {
    f4 acc[4][2];
    f2 mrs[4];
#pragma unroll
    for (int m = 0; m < 4; ++m) { acc[m][0] = (f4){0, 0, 0, 0}; acc[m][1] = (f4){0, 0, 0, 0}; mrs[m] = *(LAS f2*)(lds + L_MR + (m * 16 + l15) * 8); }
#define F1EPI(m)                                                                                                        \
  {                                                                                                                     \
    const f2 nm = {-mrs[m].x, -mrs[m].x}, rs = {mrs[m].y, mrs[m].y};                                                    \
    v4u hv;                                                                                                             \
    _Pragma("unroll") for (int r = 0; r < 4; ++r) {                                                                     \
      f2 h = __builtin_elementwise_fma(nm, (f2){au[0][r], au[1][r]}, (f2){acc[m][0][r], acc[m][1][r]});                 \
      h = __builtin_elementwise_fma(rs, h, (f2){av[0][r], av[1][r]});                                                   \
      hv[r] = pk(fmaxf(h.x, 0.f), fmaxf(h.y, 0.f));                         \
    }                                                                                                                   \
    st4s(rws, w < 7 ? lane * 16 : OOB, WS_HID + (4 * g32 + (m)) * 57344 + (28 * c8 + 4 * (w % 7)) * 256, hv);           \
  }
    v4u xb[3][2];
#pragma unroll
    for (int m = 0; m < 2; ++m) { xb[0][m] = LDS4(m * 14336 + lane * 16); xb[1][m] = LDS4(m * 14336 + 1024 + lane * 16); }
#pragma unroll
    for (int q = 0; q < 28; ++q) {
      const int mp = q / 14, s = q - 14 * mp, q2 = q + 2, mp2 = q2 / 14, s2 = q2 - 14 * mp2;
      if (q2 < 28) {
#pragma unroll
        for (int m = 0; m < 2; ++m) xb[q2 % 3][m] = LDS4((2 * mp2 + m) * 14336 + s2 * 1024 + lane * 16);
      }
      SB0();
#pragma unroll
      for (int m = 0; m < 2; ++m) {
        acc[2 * mp + m][0] = mfma(w1f[s][0], xb[q % 3][m], acc[2 * mp + m][0]);
        acc[2 * mp + m][1] = mfma(w1f[s][1], xb[q % 3][m], acc[2 * mp + m][1]);
      }
      if (q == 15) F1EPI(0)
      if (q == 17) F1EPI(1)
      SB0();
      {
        const int f = q < 14 ? ((q & 1) ? -1 : q >> 1) : (q < 27 ? q - 7 : -1);
        if (f >= 0) wf[f >> 2][f & 3] = ld4s(rws, lane * 16, WS_W2T + ((c8 * 56 + 7 * w) * 4 + f) * 1024);
      }
      SB0();
    }
    F1EPI(2)
    F1EPI(3)
  }
#pragma unroll
  for (int i = 0; i < 8; ++i) { wf[5 + (i >> 2)][i & 3] = LDS4(L_WO + (w * 8 + i) * 1024 + lane * 16); PIN(wf[5 + (i >> 2)][i & 3]); }
  STAMPT(10);
  __syncthreads();
  {
    v4u hf[2][2][7];
#define HLOAD(mp) { _Pragma("unroll") for (int i = 0; i < 2; ++i) _Pragma("unroll") for (int s = 0; s < 7; ++s) \
      hf[mp][i][s] = ld4s(rws, lane * 16, WS_HID + (4 * g32 + 2 * (mp) + i) * 57344 + w * 7168 + s * 1024); }
    HLOAD(0)
#pragma unroll
    for (int mp = 0; mp < 2; ++mp) {
      for (unsigned spins = 0;; ++spins) {
        u32 m = 0;
#pragma unroll
        for (int i = 0; i < 2; ++i)
#pragma unroll
          for (int s = 0; s < 7; ++s) m = chk(m, hf[mp][i][s]);
        if (!__any(m == ~0u) || spins > SPIN_CAP) break;
        canary(rws, (lane & 7) < 7 && lane < 16 ? (lane >> 3) * 57344 + (lane & 7) * 1024 : OOB, WS_HID + (4 * g32 + 2 * mp) * 57344 + w * 7168, spins);
        CBAR();
        HLOAD(mp)
      }
      RUNPRIO();
      if (mp == 0) STAMPT(11);
#pragma unroll
      for (int i = 0; i < 2; ++i) {
        f4 acc[4] = {{0, 0, 0, 0}, {0, 0, 0, 0}, {0, 0, 0, 0}, {0, 0, 0, 0}};
#pragma unroll
        for (int s = 0; s < 7; ++s)
#pragma unroll
          for (int t = 0; t < 4; ++t) acc[t] = mfma(wf[s][t], hf[mp][i][s], acc[t]);
#pragma unroll
        for (int r = 0; r < 4; ++r) LDSF4(((w * 4 + 2 * mp + i) * 4 + r) * 1024 + lane * 16) = (f4){acc[0][r], acc[1][r], acc[2][r], acc[3][r]};
        if (mp == 0 && i == 0) { SB0(); HLOAD(1) SB0(); }
      }
    }
  }
  __syncthreads();
  STAMPT(12);
  {
    const int mt = tid >> 7, rp = (tid >> 6) & 1;
    if (!(g == 3 && rp == 1)) {
      f4 s0 = {0, 0, 0, 0}, s1 = {0, 0, 0, 0};
#pragma unroll
      for (int ww = 0; ww < 8; ++ww) {
        s0 += LDSF4(((ww * 4 + mt) * 4 + 2 * rp) * 1024 + lane * 16);
        s1 += LDSF4(((ww * 4 + mt) * 4 + 2 * rp + 1) * 1024 + lane * 16);
      }
      const v4u x1 = LDS4(L_X1S + (mt * 7 + 2 * g + rp) * 256 + l15 * 16);
      const f4 g0 = LDSF4(L_GB + (16 * g + 8 * rp) * 4), g1 = LDSF4(L_GB + (16 * g + 8 * rp) * 4 + 16);
      v4u o;
      o.x = pk(lo2f(x1.x) + g0.x * s0.x, hi2f(x1.x) + g0.y * s0.y);
      o.y = pk(lo2f(x1.y) + g0.z * s0.z, hi2f(x1.y) + g0.w * s0.w);
      o.z = pk(lo2f(x1.z) + g1.x * s1.x, hi2f(x1.z) + g1.y * s1.y);
      o.w = pk(lo2f(x1.w) + g1.z * s1.z, hi2f(x1.w) + g1.w * s1.w);
      __builtin_nontemporal_store(o, (v4u*)(out + (size_t)(64 * g32 + 16 * mt + l15) * D_ + 56 * c8 + 16 * g + 8 * rp));
    }
  }
  if (b == 0 && w == 1) {
    for (unsigned spins = 0;; ++spins) {
      bool ok = true;
#pragma unroll
      for (int i = 0; i < 4; ++i) ok &= (__hip_atomic_load(gr + G_PREP + lane + 64 * i, RLX_AGENT) >> 16) == tag;
      if (__all(ok) || spins > SPIN_CAP) break;
      BACKOFF();
    }
    if (lane == 0) __hip_atomic_store((gu64*)ws, epoch + 1, RLX_AGENT);
  }
  STAMPT(13);
  asm volatile(".Lmega_end:");
}

extern "C" void kernel_launch(void* const* d_in, const int* in_sizes, int n_in, void* d_out, int out_size, void* d_ws,
                              size_t ws_size, hipStream_t stream) {
  (void)hipFuncSetAttribute((const void*)mega, hipFuncAttributeMaxDynamicSharedMemorySize, LDS_BYTES);
  mega<<<256, 512, LDS_BYTES, stream>>>((const u16*)d_in[0], (const u16*)d_in[1], (const u16*)d_in[2], (const u16*)d_in[4],
                                        (const u16*)d_in[5], (const u16*)d_in[6], (const u16*)d_in[7], (u16*)d_out, (char*)d_ws);
}
```

```cpp
#include <hip/hip_runtime.h>
#include <stdint.h>

typedef unsigned int u32;
typedef unsigned long long u64;
typedef unsigned short u16;
typedef u32 v4u __attribute__((ext_vector_type(4)));
typedef u32 v2u __attribute__((ext_vector_type(2)));
typedef float f4 __attribute__((ext_vector_type(4)));
typedef float f2 __attribute__((ext_vector_type(2)));
typedef __bf16 bf2v __attribute__((ext_vector_type(2)));
typedef __bf16 bf8v __attribute__((ext_vector_type(8)));
#define GAS __attribute__((address_space(1)))
#define LAS __attribute__((address_space(3)))
typedef GAS u64 gu64;
#define RLX_AGENT __ATOMIC_RELAXED, __HIP_MEMORY_SCOPE_AGENT
typedef __amdgpu_buffer_rsrc_t rsrc_t;

constexpr int S_ = 2048, D_ = 448, DFF_ = 1792;
constexpr int WS_GRAN = 4096;
constexpr int G_PREP = 2688, G_FILL = 2944;
constexpr int G_REP = 4096;
constexpr int WS_KV = 524288;
constexpr int WS_O = WS_KV + 4194304;
constexpr int WS_X1 = WS_O + 2097152;
constexpr int WS_ST = WS_X1 + 1835008;
constexpr int WS_HID = WS_ST + 262144;
constexpr int WS_W2T = WS_HID + 7340032;
constexpr int WS_W1T = WS_W2T + 1835008;
constexpr int WS_WOT = WS_W1T + 1605632;
constexpr int WS_END = WS_WOT + 524288;
constexpr int WS_DBG = WS_END;
constexpr int L_WQ = 0;
constexpr int L_K = 0, L_V = 24576;
constexpr int L_PR = 57344;
constexpr int L_WO = 86016;
constexpr int L_X = 36864;
constexpr int L_UV = L_WO;
constexpr int L_SM = 151552;
constexpr int L_AS1 = L_SM;
constexpr int L_A2 = L_AS1 + 1792;
constexpr int L_SB = L_A2 + 896;
constexpr int L_GA = L_SB + 896;
constexpr int L_GB = L_GA + 256;
constexpr int L_MR = L_GB + 256;
constexpr int L_RED = L_MR + 512;
constexpr int L_X1S = L_RED;
constexpr int L_END = L_X1S + 7168;
static_assert(L_END <= 163840, "lds");
constexpr int LDS_BYTES = 163840;
constexpr u32 OOB = 0x80000000u;
constexpr unsigned SPIN_CAP = 1u << 15;

__device__ __forceinline__ u32 pk(float lo, float hi) { f2 v = {lo, hi}; return __builtin_bit_cast(u32, __builtin_convertvector(v, bf2v)); }
__device__ __forceinline__ float dot2(u32 a, u32 b, float c) { return __builtin_amdgcn_fdot2_f32_bf16(__builtin_bit_cast(bf2v, a), __builtin_bit_cast(bf2v, b), c, false); }
__device__ __forceinline__ f4 mfma(v4u a, v4u b, f4 c) { return __builtin_amdgcn_mfma_f32_16x16x32_bf16(__builtin_bit_cast(bf8v, a), __builtin_bit_cast(bf8v, b), c, 0, 0, 0); }
__device__ __forceinline__ float lo2f(u32 v) { return __builtin_bit_cast(float, v << 16); }
__device__ __forceinline__ float hi2f(u32 v) { return __builtin_bit_cast(float, v & 0xffff0000u); }
__device__ __forceinline__ u32 plo(u32 r0, u32 r1) { return __builtin_amdgcn_perm(r1, r0, 0x05040100u); }
__device__ __forceinline__ u32 phi(u32 r0, u32 r1) { return __builtin_amdgcn_perm(r1, r0, 0x07060302u); }
__device__ __forceinline__ u32 nosent(u32 v) { return v < 0xFFFFFFFEu ? v : 0xFFFFFFFEu; }
__device__ __forceinline__ v4u nosent4(v4u v) { v.x = nosent(v.x); v.y = nosent(v.y); v.z = nosent(v.z); v.w = nosent(v.w); return v; }
__device__ __forceinline__ u32 umax3(u32 a, u32 b, u32 c) { u32 m = a > b ? a : b; return m > c ? m : c; }
__device__ __forceinline__ u32 chk(u32 m, v4u v) { return umax3(umax3(m, v.x, v.y), v.z, v.w); }
template <int C> __device__ __forceinline__ float dppf(float v) { return __builtin_bit_cast(float, __builtin_amdgcn_update_dpp(0, __builtin_bit_cast(int, v), C, 0xf, 0xf, false)); }
__device__ __forceinline__ float rowsum16(float v) { v += dppf<0xB1>(v); v += dppf<0x4E>(v); v += dppf<0x141>(v); v += dppf<0x140>(v); return v; }
__device__ __forceinline__ v4u ld4(rsrc_t r, u32 vo, u32 so) { return __builtin_amdgcn_raw_buffer_load_b128(r, vo, so, 0); }
__device__ __forceinline__ v4u ld4s(rsrc_t r, u32 vo, u32 so) { return __builtin_amdgcn_raw_buffer_load_b128(r, vo, so, 16); }
__device__ __forceinline__ v2u ld2(rsrc_t r, u32 vo, u32 so) { return __builtin_amdgcn_raw_buffer_load_b64(r, vo, so, 0); }
__device__ __forceinline__ u32 ld1(rsrc_t r, u32 vo, u32 so) { return __builtin_amdgcn_raw_buffer_load_b32(r, vo, so, 0); }
__device__ __forceinline__ void st4s(rsrc_t r, u32 vo, u32 so, v4u v) { __builtin_amdgcn_raw_buffer_store_b128(v, r, vo + so, 0, 16); }
__device__ __forceinline__ void st2s(rsrc_t r, u32 vo, u32 so, v2u v) { __builtin_amdgcn_raw_buffer_store_b64(v, r, vo, so, 16); }
__device__ __forceinline__ v4u modulate(const u32 (&xh)[8], v4u a0, v4u a1) {
  v4u h;
  float d0, d1, d2, d3, d4, d5, d6, d7;
  asm volatile(
      "v_dot2_f32_bf16 %4, %12, %20, 0\n\tv_dot2_f32_bf16 %5, %13, %21, 0\n\tv_dot2_f32_bf16 %6, %14, %22, 0\n\tv_dot2_f32_bf16 %7, %15, %23, 0\n\t"
      "v_dot2_f32_bf16 %8, %16, %24, 0\n\tv_dot2_f32_bf16 %9, %17, %25, 0\n\tv_dot2_f32_bf16 %10, %18, %26, 0\n\tv_dot2_f32_bf16 %11, %19, %27, 0\n\t"
      "v_cvt_pk_bf16_f32 %0, %4, %5\n\tv_cvt_pk_bf16_f32 %1, %6, %7\n\tv_cvt_pk_bf16_f32 %2, %8, %9\n\tv_cvt_pk_bf16_f32 %3, %10, %11\n\ts_nop 1"
      : "=&v"(h.x), "=&v"(h.y), "=&v"(h.z), "=&v"(h.w), "=&v"(d0), "=&v"(d1), "=&v"(d2), "=&v"(d3), "=&v"(d4), "=&v"(d5), "=&v"(d6), "=&v"(d7)
      : "v"(xh[0]), "v"(xh[1]), "v"(xh[2]), "v"(xh[3]), "v"(xh[4]), "v"(xh[5]), "v"(xh[6]), "v"(xh[7]),
        "v"(a0.x), "v"(a0.y), "v"(a0.z), "v"(a0.w), "v"(a1.x), "v"(a1.y), "v"(a1.z), "v"(a1.w));
  return h;
}
typedef short s4v __attribute__((ext_vector_type(4)));
__device__ __forceinline__ v2u trrd(LAS char* p) { return __builtin_bit_cast(v2u, __builtin_amdgcn_ds_read_tr16_b64_v4i16((LAS s4v*)p)); }
__device__ __forceinline__ v4u mksrd(const void* p, u32 bytes) {
  const u64 a = (u64)p;
  return (v4u){(u32)__builtin_amdgcn_readfirstlane((u32)a), (u32)__builtin_amdgcn_readfirstlane((u32)(a >> 32)) & 0xffffu, bytes, 0x00020000u};
}
template <bool SC1> __device__ __forceinline__ void dma16(v4u srd, u32 vo, u32 so, u32 ldsaddr) {
  u32 keep;
  so = __builtin_amdgcn_readfirstlane(so);
  ldsaddr = __builtin_amdgcn_readfirstlane(ldsaddr);
  if (SC1)
    asm volatile("s_mov_b32 %0, m0\n\ts_mov_b32 m0, %4\n\ts_nop 3\n\tbuffer_load_dwordx4 %1, %2, %3 offen sc1 lds\n\ts_mov_b32 m0, %0"
                 : "=&s"(keep) : "v"(vo), "s"(srd), "s"(so), "s"(ldsaddr) : "memory");
  else
    asm volatile("s_mov_b32 %0, m0\n\ts_mov_b32 m0, %4\n\ts_nop 3\n\tbuffer_load_dwordx4 %1, %2, %3 offen lds\n\ts_mov_b32 m0, %0"
                 : "=&s"(keep) : "v"(vo), "s"(srd), "s"(so), "s"(ldsaddr) : "memory");
}
__device__ __forceinline__ void canary(rsrc_t r, u32 vo, u32 so, unsigned& spins) {
  __builtin_amdgcn_s_setprio(0);
  for (; spins <= SPIN_CAP; ++spins) {
    __builtin_amdgcn_s_sleep(2);
    asm volatile("" ::: "memory");
    if (!__any(__builtin_amdgcn_raw_buffer_load_b32(r, vo, so, 16) == ~0u)) break;
  }
}
#define LDS4(off) (*(LAS v4u*)(lds + (off)))
#define LDS2(off) (*(LAS v2u*)(lds + (off)))
#define LDSF4(off) (*(LAS f4*)(lds + (off)))
#define LDSF(off) (*(LAS float*)(lds + (off)))
#define LDSH(off) (*(LAS u16*)(lds + (off)))
#define CBAR() asm volatile("" ::: "memory")
#define PIN(v) asm volatile("" : "+v"(v))
#define SB0() __builtin_amdgcn_sched_barrier(0)
#define BACKOFF() do { __builtin_amdgcn_s_setprio(0); __builtin_amdgcn_s_sleep(6); } while (0)
#define RUNPRIO() __builtin_amdgcn_s_setprio(1)
#define DRAIN() asm volatile("s_waitcnt vmcnt(0)" ::: "memory")
#define STAMPT(i)

__global__ __launch_bounds__(512, 2) void mega(const u16* __restrict__ x, const u16* __restrict__ wqkv, const u16* __restrict__ wo,
                                               const u16* __restrict__ w1, const u16* __restrict__ w2, const u16* __restrict__ cvec,
                                               const u16* __restrict__ wmod, u16* __restrict__ out, char* ws) {
  extern __shared__ __attribute__((aligned(16))) char lds_g[];
  LAS char* lds = (LAS char*)lds_g;
  const int tid = threadIdx.x, lane = tid & 63, w = __builtin_amdgcn_readfirstlane(tid >> 6) & 7;
  const int l15 = lane & 15, g = lane >> 4;
  const int b = blockIdx.x, xcd = b & 7, slot = b >> 3;
  const int g16 = xcd * 2 + (slot >> 4), head = slot & 15;
  const int g32 = xcd * 4 + (slot >> 3), c8 = slot & 7;
  const rsrc_t rws = __builtin_amdgcn_make_buffer_rsrc(ws, 0, WS_END, 0x00020000);
  const rsrc_t rx = __builtin_amdgcn_make_buffer_rsrc((void*)x, 0, S_ * D_ * 2, 0x00020000);
  const rsrc_t rwq = __builtin_amdgcn_make_buffer_rsrc((void*)wqkv, 0, D_ * 3 * D_ * 2, 0x00020000);
  const rsrc_t rwm = __builtin_amdgcn_make_buffer_rsrc((void*)wmod, 0, D_ * 6 * D_ * 2, 0x00020000);
  const rsrc_t rc = __builtin_amdgcn_make_buffer_rsrc((void*)cvec, 0, D_ * 2, 0x00020000);
  const v4u SENT = {~0u, ~0u, ~0u, ~0u};
  STAMPT(0);
  const u64 dbg_t0 = __builtin_amdgcn_s_memrealtime();
  RUNPRIO();
  u32 cpf, mpf;
  u64 epoch;
  {
    u64 pc;
    u32 len;
    asm volatile("s_getpc_b64 %0\n.Lmega_start:\n\ts_mov_b32 %1, .Lmega_end-.Lmega_start" : "=s"(pc), "=s"(len));
    const v4u scode = mksrd((const void*)pc, len), swm0 = mksrd(wmod, D_ * 6 * D_ * 2), sws0 = mksrd(ws, 64);
    const int idx = slot * 84 + tid - 320, n = idx / 448, row = idx - 448 * n, j = xcd + 8 * n;
    asm volatile("s_nop 4\n\tbuffer_load_dword %0, %3, %5, 0 offen\n\tbuffer_load_dword %1, %4, %6, 0 offen\n\tbuffer_load_dwordx2 %2, off, %7, 0 sc1"
                 : "=&v"(cpf), "=&v"(mpf), "=&v"(epoch)
                 : "v"(tid >= 448 ? (lane * 8 + (slot & 7)) * 128 : OOB), "v"((tid >= 320 && tid < 404 && j < 42) ? (u32)(row * 5376 + j * 128) : OOB),
                   "s"(scode), "s"(swm0), "s"(sws0)
                 : "memory");
  }
  gu64* gr0 = (gu64*)(ws + WS_GRAN);
  gu64* gr = gr0 + xcd * G_REP;
  const int mj = xcd + 8 * (slot / 3);
  const bool modw = (slot % 3 == 0) && mj < 42 && w < 4;
  if (tid < 4) *(volatile LAS u32*)(lds + L_RED + 3072 + tid * 4) = 0u;
  v2u mr[4][8];
  u32 craw;
  if (modw) {
    const v4u swm = mksrd(wmod, D_ * 6 * D_ * 2), sc_ = mksrd(cvec, D_ * 2);
    asm volatile("s_nop 4\n\tbuffer_load_dword %0, %1, %2, 0 offen" : "=&v"(craw) : "v"(w + 4 * g < 14 ? (32 * (w + 4 * g) + 2 * l15) * 2 : OOB), "s"(sc_) : "memory");
#pragma unroll
    for (int i = 0; i < 4; ++i) {
      const u32 vo = (i < 3 || w < 2) ? (u32)(8 * g * 2688 + 4 * l15) * 2 : OOB;
      const u32 so = (u32)((32 * ((w + 4 * i) % 14)) * 2688 + 64 * mj) * 2;
      asm volatile(
          "s_nop 4\n\t"
          "buffer_load_dwordx2 %0, %8, %9, %10 offen\n\tbuffer_load_dwordx2 %1, %8, %9, %11 offen\n\t"
          "buffer_load_dwordx2 %2, %8, %9, %12 offen\n\tbuffer_load_dwordx2 %3, %8, %9, %13 offen\n\t"
          "buffer_load_dwordx2 %4, %8, %9, %14 offen\n\tbuffer_load_dwordx2 %5, %8, %9, %15 offen\n\t"
          "buffer_load_dwordx2 %6, %8, %9, %16 offen\n\tbuffer_load_dwordx2 %7, %8, %9, %17 offen"
          : "=&v"(mr[i][0]), "=&v"(mr[i][1]), "=&v"(mr[i][2]), "=&v"(mr[i][3]), "=&v"(mr[i][4]), "=&v"(mr[i][5]), "=&v"(mr[i][6]), "=&v"(mr[i][7])
          : "v"(vo), "s"(swm), "s"(so), "s"(so + 5376), "s"(so + 2 * 5376), "s"(so + 3 * 5376), "s"(so + 4 * 5376), "s"(so + 5 * 5376),
            "s"(so + 6 * 5376), "s"(so + 7 * 5376)
          : "memory");
    }
  }
  asm volatile("s_waitcnt lgkmcnt(0)\n\ts_barrier" ::: "memory");
  STAMPT(14);
  const v4u sq_srd = mksrd(wqkv, D_ * 3 * D_ * 2);
  const u32 vq = (u32)((lane >> 2) * 2688 + (((lane & 3) ^ (2 * (lane >> 5))) * 16));
#define WQDMA(i)                                                                                                        \
  {                                                                                                                     \
    const int u = w + 8 * ((i) >> 1), d = 2 * u + ((i) & 1), mat = u / 14, kb = d - 28 * mat;                           \
    dma16<false>(sq_srd, u < 42 ? vq : OOB, (u32)(16 * kb * 2688 + (mat % 3) * 896 + 56 * head), u < 42 ? L_WQ + d * 1024 : L_RED + 4096); \
  }
  {
    const v4u sx = mksrd(x, S_ * D_ * 2);
    const u32 vo = (u32)((lane >> 3) * D_ * 2 + (((lane & 7) ^ (lane >> 3)) * 16));
#pragma unroll
    for (int q = 0; q < 14; ++q) dma16<false>(sx, vo, (u32)((128 * g16 + 16 * w + 8 * (q & 1)) * D_ * 2 + (q >> 1) * 128), L_X + w * 14336 + q * 1024);
  }
  asm volatile("s_barrier" ::: "memory");
  WQDMA(0) WQDMA(1) WQDMA(2) WQDMA(3)
  u32 pr[8];
  u32 pdst;
  {
    const int u = b * 8 + w;
    const u16* base; u32 bytes, ld, so, vo, vo2;
    if (u < 896) {
      const int cc = u / 112, v = u - 112 * cc, kb = v >> 1, hf = v & 1;
      base = w2; bytes = DFF_ * D_ * 2; ld = D_ * 2; so = (u32)(32 * kb * D_ + 56 * cc + 2 * hf) * 2;
      vo = l15 < 14 ? (u32)(8 * g * D_ + 4 * l15) * 2 : OOB; vo2 = vo;
      pdst = WS_W2T + ((cc * 56 + kb) * 4 + 2 * hf) * 1024;
    } else if (u < 1680) {
      const int v = u - 896, cb = v / 14, s = v - 14 * cb;
      base = w1; bytes = DFF_ * D_ * 2; ld = DFF_ * 2; so = (u32)(32 * s * DFF_ + 32 * cb) * 2;
      vo = (u32)(8 * g * DFF_ + 2 * l15) * 2; vo2 = vo;
      pdst = WS_W1T + (cb * 14 + s) * 2048;
    } else {
      const int v = u - 1680, hh = v & 15, nh = (v >> 4) & 1, cc = (v >> 5) & 7;
      base = wo; bytes = D_ * D_ * 2; ld = D_ * 2; so = (u32)(28 * hh * D_ + 56 * cc + 32 * nh) * 2;
      vo = (u < 1936 && (nh == 0 || l15 < 12)) ? (u32)(4 * g * D_ + 2 * l15) * 2 : OOB; vo2 = g < 3 ? vo + 12 * D_ * 2 : OOB;
      pdst = u < 1936 ? WS_WOT + (cc * 32 + nh * 16 + hh) * 2048 : OOB;
    }
    const v4u sp = mksrd(base, __builtin_amdgcn_readfirstlane(bytes));
    so = __builtin_amdgcn_readfirstlane(so); ld = __builtin_amdgcn_readfirstlane(ld);
    asm volatile(
        "s_nop 4\n\t"
        "buffer_load_dword %0, %8, %10, %11 offen\n\tbuffer_load_dword %1, %8, %10, %12 offen\n\t"
        "buffer_load_dword %2, %8, %10, %13 offen\n\tbuffer_load_dword %3, %8, %10, %14 offen\n\t"
        "buffer_load_dword %4, %9, %10, %15 offen\n\tbuffer_load_dword %5, %9, %10, %16 offen\n\t"
        "buffer_load_dword %6, %9, %10, %17 offen\n\tbuffer_load_dword %7, %9, %10, %18 offen"
        : "=&v"(pr[0]), "=&v"(pr[1]), "=&v"(pr[2]), "=&v"(pr[3]), "=&v"(pr[4]), "=&v"(pr[5]), "=&v"(pr[6]), "=&v"(pr[7])
        : "v"(vo), "v"(vo2), "s"(sp), "s"(so), "s"(so + ld), "s"(so + 2 * ld), "s"(so + 3 * ld), "s"(so + 4 * ld), "s"(so + 5 * ld), "s"(so + 6 * ld),
          "s"(so + 7 * ld)
        : "memory");
  }
  SB0();
  asm volatile("s_waitcnt vmcnt(26)" : "+v"(epoch), "+v"(cpf), "+v"(mpf)::"memory");
  const u64 tag = (epoch + 1) & 0xFFFFFFFFFFFFull;
  if (modw) {
    __builtin_amdgcn_s_setprio(3);
    asm volatile(""
                 : "+v"(craw), "+v"(mr[0][0]), "+v"(mr[0][1]), "+v"(mr[0][2]), "+v"(mr[0][3]), "+v"(mr[0][4]), "+v"(mr[0][5]), "+v"(mr[0][6]), "+v"(mr[0][7]),
                   "+v"(mr[1][0]), "+v"(mr[1][1]), "+v"(mr[1][2]), "+v"(mr[1][3]), "+v"(mr[1][4]), "+v"(mr[1][5]), "+v"(mr[1][6]), "+v"(mr[1][7])::"memory");
    asm volatile(""
                 : "+v"(mr[2][0]), "+v"(mr[2][1]), "+v"(mr[2][2]), "+v"(mr[2][3]), "+v"(mr[2][4]), "+v"(mr[2][5]), "+v"(mr[2][6]), "+v"(mr[2][7]),
                   "+v"(mr[3][0]), "+v"(mr[3][1]), "+v"(mr[3][2]), "+v"(mr[3][3]), "+v"(mr[3][4]), "+v"(mr[3][5]), "+v"(mr[3][6]), "+v"(mr[3][7])::"memory");
    float c0 = lo2f(craw), c1 = hi2f(craw);
    c0 = c0 * __builtin_amdgcn_rcpf(1.f + __expf(-c0));
    c1 = c1 * __builtin_amdgcn_rcpf(1.f + __expf(-c1));
    *(LAS u32*)(lds + L_RED + w * 256 + lane * 4) = pk(c0, c1);
    f4 acc[4] = {{0, 0, 0, 0}, {0, 0, 0, 0}, {0, 0, 0, 0}, {0, 0, 0, 0}};
#pragma unroll
    for (int i = 0; i < 4; ++i) {
      const v4u sf = LDS4(L_RED + w * 256 + i * 64 + g * 16);
      acc[0] = mfma((v4u){plo(mr[i][0].x, mr[i][1].x), plo(mr[i][2].x, mr[i][3].x), plo(mr[i][4].x, mr[i][5].x), plo(mr[i][6].x, mr[i][7].x)}, sf, acc[0]);
      acc[1] = mfma((v4u){phi(mr[i][0].x, mr[i][1].x), phi(mr[i][2].x, mr[i][3].x), phi(mr[i][4].x, mr[i][5].x), phi(mr[i][6].x, mr[i][7].x)}, sf, acc[1]);
      acc[2] = mfma((v4u){plo(mr[i][0].y, mr[i][1].y), plo(mr[i][2].y, mr[i][3].y), plo(mr[i][4].y, mr[i][5].y), plo(mr[i][6].y, mr[i][7].y)}, sf, acc[2]);
      acc[3] = mfma((v4u){phi(mr[i][0].y, mr[i][1].y), phi(mr[i][2].y, mr[i][3].y), phi(mr[i][4].y, mr[i][5].y), phi(mr[i][6].y, mr[i][7].y)}, sf, acc[3]);
    }
    if (l15 == 0) {
#pragma unroll
      for (int r = 0; r < 4; ++r) LDSF4(L_RED + 1024 + w * 256 + (16 * g + 4 * r) * 4) = (f4){acc[0][r], acc[1][r], acc[2][r], acc[3][r]};
    }
    asm volatile("s_waitcnt lgkmcnt(0)" ::: "memory");
    u32 old = 0;
    if (lane == 0) old = __hip_atomic_fetch_add((LAS u32*)(lds + L_RED + 3072), 1u, __ATOMIC_RELAXED, __HIP_MEMORY_SCOPE_WORKGROUP);
    if (__builtin_amdgcn_readfirstlane(old) == 3u) {
      CBAR();
      const float sum = (LDSF(L_RED + 1024 + lane * 4) + LDSF(L_RED + 1280 + lane * 4)) + (LDSF(L_RED + 1536 + lane * 4) + LDSF(L_RED + 1792 + lane * 4));
      const u64 gval = (tag << 16) | (u64)(pk(sum, 0.f) & 0xffffu);
#pragma unroll
      for (int r = 0; r < 8; ++r) __hip_atomic_store(gr0 + r * G_REP + 64 * mj + lane, gval, RLX_AGENT);
    }
    RUNPRIO();
  }
  STAMPT(15);
  if (modw) asm volatile("s_waitcnt vmcnt(8)" ::: "memory"); else asm volatile("s_waitcnt vmcnt(12)" ::: "memory");
  STAMPT(16);
  v4u xr[14];
  {
    const u32 ra0 = L_X + w * 14336 + (l15 >> 3) * 1024 + (l15 & 7) * 128 + ((g ^ (l15 & 7)) * 16);
    const u32 ra1 = L_X + w * 14336 + (l15 >> 3) * 1024 + (l15 & 7) * 128 + (((4 + g) ^ (l15 & 7)) * 16);
#pragma unroll
    for (int s = 0; s < 14; ++s) xr[s] = LDS4(((s & 1) ? ra1 : ra0) + (s >> 1) * 2048);
#pragma unroll
    for (int s = 0; s < 14; ++s) PIN(xr[s]);
  }
  asm volatile("s_waitcnt lgkmcnt(0)\n\ts_barrier" ::: "memory");
  STAMPT(17);
  const int mtB = w & 3, nhB = w >> 2;
  v4u xres;
  const int tau = 16 * (w & 1) + l15;
  const u32 koff = (w >> 1) * 2048 + ((tau >> 2) & 1) * 1024 + (g * 16 + 4 * (tau >> 3) + (tau & 3)) * 16;
  const u32 voff = (w >> 1) * 2048 + ((2 * (w & 1) + (g >> 1)) * 16 + l15) * 16 + (g & 1) * 8;
  const u32 kvb = g16 < 15 ? WS_KV + (g16 * 16 + head) * 16384 : OOB;
  u32 xh[14][8];
  {
    const v4u ONES = {0x3F803F80u, 0x3F803F80u, 0x3F803F80u, 0x3F803F80u};
    f4 sm = {0, 0, 0, 0}, gm = {0, 0, 0, 0};
#pragma unroll
    for (int s = 0; s < 14; ++s) {
      if (s == 0 || s == 7) WQDMA(4 + s / 7)
      sm = mfma(ONES, xr[s], sm);
      gm = mfma(xr[s], xr[s], gm);
    }
    float sq = (l15 & 2) ? ((l15 & 1) ? gm[3] : gm[2]) : ((l15 & 1) ? gm[1] : gm[0]);
    sq = __shfl(sq, 16 * (l15 >> 2) + l15);
    const float mean = sm[0] * (1.f / D_);
    const float rstd = __builtin_amdgcn_rsqf(fmaxf(sq * (1.f / D_) - mean * mean, 0.f) + 1e-5f);
    const f2 rs2 = {rstd, rstd}, nm2 = {-mean * rstd, -mean * rstd};
#pragma unroll
    for (int s = 0; s < 14; ++s) {
      if (s < 6) WQDMA(6 + s)
      else if (s == 7) xres = ld4(rx, (nhB == 1 && g == 3) ? OOB : (u32)(l15 * D_ + 8 * g) * 2, (u32)((64 * g32 + 16 * mtB) * D_ + 56 * c8 + 32 * nhB) * 2);
      else if (s == 9) st4s(rws, koff, kvb, SENT);
      else if (s == 11) st4s(rws, (w >> 1) * 2048 + (w & 1) * 512 + (lane & 31) * 16 + (lane >> 5) * 1024, kvb + 8192, SENT);
      else if (s == 13) st4s(rws, lane * 16, WS_O + ((8 * g16 + w) * 16 + head) * 1024, SENT);
#pragma unroll
      for (int j = 0; j < 4; ++j) {
        const f2 v = __builtin_elementwise_fma((f2){lo2f(xr[s][j]), hi2f(xr[s][j])}, rs2, nm2);
        xh[s][2 * j] = pk(v.x, 1.f);
        xh[s][2 * j + 1] = pk(v.y, 1.f);
        PIN(xh[s][2 * j]); PIN(xh[s][2 * j + 1]);
      }
      SB0();
    }
  }
  STAMPT(20);
  const int i1 = tid < 384 ? 512 + tid : tid;
  u64 g10 = __hip_atomic_load(gr + tid, RLX_AGENT), g11 = __hip_atomic_load(gr + i1, RLX_AGENT);
  asm volatile("s_waitcnt vmcnt(6)"
               : "+v"(pr[0]), "+v"(pr[1]), "+v"(pr[2]), "+v"(pr[3]), "+v"(pr[4]), "+v"(pr[5]), "+v"(pr[6]), "+v"(pr[7])::"memory");
  {
    LAS char* tb0 = lds + L_WQ + (8 * g + (l15 >> 2)) * 64 + (g & 1) * 32 + (l15 & 3) * 8;
    LAS char* tb1 = lds + L_WQ + (8 * g + (l15 >> 2)) * 64 + ((g & 1) ^ 1) * 32 + (l15 & 3) * 8;
    v2u r[6][4];
#pragma unroll
    for (int i = 0; i < 6; ++i) {
      const int u = (i < 5 || w < 2) ? w + 8 * i : w;
      r[i][0] = trrd(tb0 + u * 2048); r[i][1] = trrd(tb0 + u * 2048 + 256);
      r[i][2] = trrd(tb1 + u * 2048); r[i][3] = trrd(tb1 + u * 2048 + 256);
    }
    SB0();
#pragma unroll
    for (int i = 0; i < 6; ++i) {
      const int u = (i < 5 || w < 2) ? w + 8 * i : w;
      LDS4(L_WQ + u * 2048 + lane * 16) = (v4u){r[i][0].x, r[i][0].y, r[i][1].x, r[i][1].y};
      LDS4(L_WQ + u * 2048 + 1024 + lane * 16) = (v4u){r[i][2].x, r[i][2].y, r[i][3].x, r[i][3].y};
    }
  }
  v4u pe = {plo(pr[0], pr[1]), plo(pr[2], pr[3]), plo(pr[4], pr[5]), plo(pr[6], pr[7])};
  v4u po = {phi(pr[0], pr[1]), phi(pr[2], pr[3]), phi(pr[4], pr[5]), phi(pr[6], pr[7])};
  PIN(pe); PIN(po);
  STAMPT(2);
  {
    for (unsigned spins = 0;; ++spins) {
      const bool ok = (g10 >> 16) == tag && (g11 >> 16) == tag;
      if (__all(ok) || spins > SPIN_CAP) break;
      BACKOFF();
      g10 = __hip_atomic_load(gr + tid, RLX_AGENT);
      g11 = __hip_atomic_load(gr + i1, RLX_AGENT);
    }
    RUNPRIO();
    if (tid < 448) LDSH(L_AS1 + tid * 4 + 2) = (u16)g10;
    else LDSH(L_AS1 + (tid - 448) * 4) = (u16)pk(1.f + lo2f((u32)g10), 0.f);
    if (tid < 384) LDSH(L_AS1 + (64 + tid) * 4) = (u16)pk(1.f + lo2f((u32)g11), 0.f);
  }
  DRAIN();
  __syncthreads();
  if (tid < 8) __hip_atomic_store(gr0 + tid * G_REP + G_FILL + b, tag << 16, RLX_AGENT);
  STAMPT(3);
  const int jg = tid - 448;
  const int i20 = tid < 448 ? 1344 + tid : 896 + 56 * c8 + (jg < 56 ? jg : 0);
  const int i21 = tid < 448 ? 1792 + tid : 2240 + 56 * c8 + (jg < 56 ? jg : 0);
  u64 g20 = __hip_atomic_load(gr + i20, RLX_AGENT), g21 = __hip_atomic_load(gr + i21, RLX_AGENT);

  v4u hfr[14];
  {
    v4u as[3][2];
#pragma unroll
    for (int s = 0; s < 2; ++s) { as[s][0] = LDS4(L_AS1 + (32 * s + 8 * g) * 4); as[s][1] = LDS4(L_AS1 + (32 * s + 8 * g) * 4 + 16); }
#pragma unroll
    for (int s = 0; s < 14; ++s) {
      if (s < 12) { as[(s + 2) % 3][0] = LDS4(L_AS1 + (32 * (s + 2) + 8 * g) * 4); as[(s + 2) % 3][1] = LDS4(L_AS1 + (32 * (s + 2) + 8 * g) * 4 + 16); }
      SB0();
      hfr[s] = modulate(xh[s], as[s % 3][0], as[s % 3][1]);
      SB0();
    }
  }
  STAMPT(29);
  int dep = b;
  if (lane < 16) dep = xcd + 8 * (16 * (g16 & 1) + lane);
  else if (lane == 16) dep = g16 >= 1 ? ((g16 - 1) >> 1) + 8 * (16 * ((g16 - 1) & 1) + head) : b;
  else if (lane == 17) dep = g16 >= 2 ? ((g16 - 2) >> 1) + 8 * (16 * (g16 & 1) + head) : b;
  else if (lane < 32) dep = 14 * c8 + (lane - 18);
  else if (lane < 46) dep = ((896 + 98 * c8) >> 3) + (lane - 32);
  else if (lane < 50) dep = 210 + 4 * c8 + (lane - 46);
  u64 dfl = tag << 16;
  f4 aq[6];
#pragma unroll
  for (int t = 0; t < 6; ++t) aq[t] = (f4){0, 0, 0, 0};
  {
    v4u fb[3][6];
#define QLOAD(s, d) { _Pragma("unroll") for (int t = 0; t < 6; ++t) fb[d][t] = LDS4(L_WQ + ((t >> 1) * 14 + (s)) * 2048 + (t & 1) * 1024 + lane * 16); }
    QLOAD(0, 0)
    QLOAD(1, 1)
#pragma unroll
    for (int s = 0; s < 14; ++s) {
      if (s < 12) QLOAD(s + 2, (s + 2) % 3)
      if (s == 0) st4s(rws, lane * 16, pdst, pe);
      if (s == 2) st4s(rws, lane * 16, pdst + 1024, po);
      if (s == 4) st4s(rws, (w < 4 || lane < 48) ? lane * 16 : OOB, WS_X1 + (4 * g32 + (w & 3)) * 14336 + c8 * 1792 + (w >> 2) * 1024, SENT);
      if (s == 5) st4s(rws, (w < 4 && lane < 16) ? lane * 16 : OOB, WS_ST + (4 * g32 + (w & 3)) * 2048 + c8 * 256, SENT);
      if (s == 8 && w == 0) dfl = __hip_atomic_load(gr + G_FILL + (lane < 18 ? dep : b), RLX_AGENT);
      SB0();
#pragma unroll
      for (int t = 0; t < 4; ++t) aq[t] = mfma(fb[s % 3][t], hfr[s], aq[t]);
#pragma unroll
      for (int t = 4; t < 6; ++t) aq[t] = mfma(hfr[s], fb[s % 3][t], aq[t]);
      SB0();
    }
  }
  STAMPT(30);
  const v4u qf = {pk(aq[0][0], aq[0][1]), pk(aq[0][2], aq[0][3]), g < 3 ? pk(aq[1][0], aq[1][1]) : 0u, g < 3 ? pk(aq[1][2], aq[1][3]) : 0u};
  {
    const v4u kf = nosent4((v4u){pk(aq[2][0], aq[2][1]), pk(aq[2][2], aq[2][3]), pk(aq[3][0], aq[3][1]), pk(aq[3][2], aq[3][3])});
    v2u ve = {pk(aq[4][0], aq[4][1]), pk(aq[4][2], aq[4][3])}, vo_ = {pk(aq[5][0], aq[5][1]), pk(aq[5][2], aq[5][3])};
    if (l15 == 12) vo_ = (v2u){0x3F803F80u, 0x3F803F80u};
    ve.x = nosent(ve.x); ve.y = nosent(ve.y); vo_.x = nosent(vo_.x); vo_.y = nosent(vo_.y);
    {
      DRAIN();
      u32 old = 0;
      if (lane == 0) old = __hip_atomic_fetch_add((LAS u32*)(lds + L_RED + 3080), 1u, __ATOMIC_RELAXED, __HIP_MEMORY_SCOPE_WORKGROUP);
      if (__builtin_amdgcn_readfirstlane(old) == 7u && lane < 8) __hip_atomic_store(gr0 + lane * G_REP + G_PREP + b, tag << 16, RLX_AGENT);
    }
    st4s(rws, koff, kvb, kf);
    st2s(rws, voff, kvb + 8192, ve);
    st2s(rws, voff, kvb + 8192 + 1024, vo_);
    STAMPT(31);
    for (unsigned spins = 0;; ++spins) {
      bool ok = (g20 >> 16) == tag && (g21 >> 16) == tag;
      ok &= (dfl >> 16) == tag;
      if (__all(ok) || spins > SPIN_CAP) break;
      BACKOFF();
      if (w == 0) dfl = __hip_atomic_load(gr + G_FILL + (lane < 18 ? dep : b), RLX_AGENT);
      g20 = __hip_atomic_load(gr + i20, RLX_AGENT);
      g21 = __hip_atomic_load(gr + i21, RLX_AGENT);
    }
    RUNPRIO();
    if (tid < 448) {
      LDSH(L_SB + tid * 2) = (u16)g20;
      LDSH(L_A2 + tid * 2) = (u16)pk(1.f + lo2f((u32)g21), 0.f);
    } else {
      LDSF(L_GA + jg * 4) = lo2f((u32)g20);
      LDSF(L_GB + jg * 4) = lo2f((u32)g21);
    }
    STAMPT(21);
    __syncthreads();
    STAMPT(4);
    LDS4(L_K + 16384 + koff) = kf;
    LDS2(L_V + 16384 + voff) = ve;
    LDS2(L_V + 16384 + 1024 + voff) = vo_;
  }
  STAMPT(22);
  v4u w1f[14][2];
  const u32 vo1 = w < 7 ? lane * 16 : OOB;
  const v4u sws = mksrd(ws, WS_END);
  {
    const u32 b0 = g16 >= 2 ? WS_KV + ((g16 - 2) * 16 + head) * 16384 : OOB;
    const u32 b1 = g16 >= 1 ? WS_KV + ((g16 - 1) * 16 + head) * 16384 : OOB;
    v4u k0, v0, k1, v1;
    for (unsigned spins = 0;; ++spins) {
      CBAR();
      k0 = ld4s(rws, tid * 16, b0); v0 = ld4s(rws, tid * 16, b0 + 8192);
      k1 = ld4s(rws, tid * 16, b1); v1 = ld4s(rws, tid * 16, b1 + 8192);
      const bool bad = chk(chk(chk(chk(0u, k0), v0), k1), v1) == ~0u;
      if (!__any(bad) || spins > SPIN_CAP) break;
      {
        const u32 pb = (lane & 16) ? b1 : b0;
        canary(rws, lane < 32 ? pb + ((lane >> 3) & 1) * 8192 + w * 1024 + (lane & 7) * 128 : OOB, 0, spins);
      }
    }
    RUNPRIO();
    STAMPT(24);
    LDS4(L_K + tid * 16) = k0; LDS4(L_V + tid * 16) = v0;
    LDS4(L_K + 8192 + tid * 16) = k1; LDS4(L_V + 8192 + tid * 16) = v1;
  }
  if (w == 0) {
    for (unsigned spins = 0;; ++spins) {
      if (__all((__hip_atomic_load(gr + G_PREP + (lane >= 18 ? dep : b), RLX_AGENT) >> 16) == tag) || spins > SPIN_CAP) break;
      BACKOFF();
    }
    RUNPRIO();
  }
  DRAIN();
  __syncthreads();
  STAMPT(5);
  {
    const int blk0 = w >> 1;
    f4 sc[18];
    {
      v4u kf[18];
#pragma unroll
      for (int t = 0; t < 6; ++t) kf[t] = LDS4(L_K + (blk0 + (t >> 1)) * 2048 + (t & 1) * 1024 + lane * 16);
#pragma unroll
      for (int t = 0; t < 18; ++t) {
        if (t + 6 < 18) kf[t + 6] = LDS4(L_K + (blk0 + ((t + 6) >> 1)) * 2048 + (t & 1) * 1024 + lane * 16);
        if ((t & 1) && t < 16) dma16<true>(sws, lane * 16, WS_WOT + c8 * 65536 + (w * 8 + (t >> 1)) * 1024, L_WO + (w * 8 + (t >> 1)) * 1024);
        SB0();
        sc[t] = mfma(kf[t], qf, (f4){0, 0, 0, 0});
        SB0();
      }
    }
    STAMPT(25);
    v4u vf[18];
#pragma unroll
    for (int t = 0; t < 4; ++t) vf[t] = LDS4(L_V + (blk0 + (t >> 1)) * 2048 + (t & 1) * 1024 + lane * 16);
    const int ql = 256 + 16 * w + l15;
    const int kmin = (256 - 128 * g16) > (ql - 255) ? (256 - 128 * g16) : (ql - 255);
    const int kb0 = 32 * blk0 + 8 * g;
    const float NEG = -1e30f;
    if (g16 < 2) {
#pragma unroll
      for (int t = 2; t < 16; ++t)
#pragma unroll
        for (int r = 0; r < 4; ++r) sc[t][r] = (kb0 + 32 * (t >> 1) + 4 * (t & 1) + r) >= kmin ? sc[t][r] : NEG;
    }
#pragma unroll
    for (int t = 0; t < 2; ++t)
#pragma unroll
      for (int r = 0; r < 4; ++r) sc[t][r] = (kb0 + 4 * t + r) >= kmin ? sc[t][r] : NEG;
#pragma unroll
    for (int t = 16; t < 18; ++t)
#pragma unroll
      for (int r = 0; r < 4; ++r) sc[t][r] = (kb0 + 256 + 4 * (t & 1) + r) <= ql ? sc[t][r] : NEG;
    float mx = NEG;
#pragma unroll
    for (int t = 0; t < 18; ++t) mx = fmaxf(fmaxf(mx, sc[t][0]), fmaxf(sc[t][1], fmaxf(sc[t][2], sc[t][3])));
    mx = fmaxf(mx, __shfl_xor(mx, 16));
    mx = fmaxf(mx, __shfl_xor(mx, 32));
    STAMPT(26);
    STAMPT(18);
    const float cs = 0.18898223650461362f * 1.4426950408889634f;
    const float mc = mx * cs;
    f4 oe = {0, 0, 0, 0}, oo = {0, 0, 0, 0};
#pragma unroll
    for (int bb = 0; bb < 9; ++bb) {
      if (bb + 2 < 9) {
        vf[2 * bb + 4] = LDS4(L_V + (blk0 + bb + 2) * 2048 + lane * 16);
        vf[2 * bb + 5] = LDS4(L_V + (blk0 + bb + 2) * 2048 + 1024 + lane * 16);
      }
      w1f[bb >> 1][bb & 1] = ld4s(rws, vo1, WS_W1T + (7 * c8 + (w % 7)) * 28672 + bb * 1024);
      if ((bb & 1) == 0 && bb < 8) st4s(rws, vo1, WS_HID + (4 * g32 + (bb >> 1)) * 57344 + c8 * 7168 + (w % 7) * 1024, SENT);
      SB0();
      float p[8];
#pragma unroll
      for (int r = 0; r < 4; ++r) {
        p[r] = __builtin_amdgcn_exp2f(sc[2 * bb][r] * cs - mc);
        p[4 + r] = __builtin_amdgcn_exp2f(sc[2 * bb + 1][r] * cs - mc);
      }
      const v4u pf = {pk(p[0], p[1]), pk(p[2], p[3]), pk(p[4], p[5]), pk(p[6], p[7])};
      oe = mfma(vf[2 * bb], pf, oe);
      oo = mfma(vf[2 * bb + 1], pf, oo);
      SB0();
    }
    const float den = __shfl(oo[0], 48 + l15);
    const float inv = __builtin_amdgcn_rcpf(den);
    const v4u of = nosent4((v4u){pk(oe[0] * inv, oe[1] * inv), pk(oe[2] * inv, oe[3] * inv), pk(oo[0] * inv, oo[1] * inv), pk(oo[2] * inv, oo[3] * inv)});
    st4s(rws, lane * 16, WS_O + ((8 * g16 + w) * 16 + head) * 1024, of);
  }
  STAMPT(6);
  SB0();
  asm volatile("s_waitcnt vmcnt(14)" ::: "memory");
#pragma unroll
  for (int i = 9; i < 17; ++i) w1f[i >> 1][i & 1] = ld4s(rws, vo1, WS_W1T + (7 * c8 + (w % 7)) * 28672 + i * 1024);
  STAMPT(19);
  STAMPT(27);
  __syncthreads();
  STAMPT(28);

  {
    v4u of[8];
    const u32 base = WS_O + (4 * g32 + mtB) * 16384 + nhB * 8192;
    v4u wpre[4][4];
#pragma unroll
    for (int h = 0; h < 4; ++h)
#pragma unroll
      for (int t = 0; t < 4; ++t) { wpre[h][t] = LDS4(L_WO + ((t >> 1) * 16 + nhB * 8 + h) * 2048 + (t & 1) * 1024 + lane * 16); PIN(wpre[h][t]); }
    for (unsigned spins = 0;; ++spins) {
      CBAR();
      u32 m = 0;
#pragma unroll
      for (int h = 0; h < 8; ++h) of[h] = ld4s(rws, lane * 16, base + h * 1024);
#pragma unroll
      for (int h = 0; h < 8; ++h) m = chk(m, of[h]);
      if (!__any(m == ~0u) || spins > SPIN_CAP) break;
      canary(rws, lane < 8 ? lane * 1024 : OOB, base, spins);
    }
    RUNPRIO();
    DRAIN();
    STAMPT(7);
    f4 acc[4] = {{0, 0, 0, 0}, {0, 0, 0, 0}, {0, 0, 0, 0}, {0, 0, 0, 0}};
#pragma unroll
    for (int h = 0; h < 8; ++h) {
#pragma unroll
      for (int t = 0; t < 4; ++t) acc[t] = mfma(wpre[h & 3][t], of[h], acc[t]);
      SB0();
      if (h < 4) {
#pragma unroll
        for (int t = 0; t < 4; ++t) wpre[h][t] = LDS4(L_WO + ((t >> 1) * 16 + nhB * 8 + h + 4) * 2048 + (t & 1) * 1024 + lane * 16);
      }
      if (h >= 2) {
#pragma unroll
        for (int i = 17 + 2 * (h - 2); i < 19 + 2 * (h - 2) && i < 28; ++i) w1f[i >> 1][i & 1] = ld4s(rws, vo1, WS_W1T + (7 * c8 + (w % 7)) * 28672 + i * 1024);
      }
      SB0();
    }
    LDSF4(L_PR + w * 2048 + lane * 16) = nhB ? acc[0] : acc[2];
    LDSF4(L_PR + w * 2048 + 1024 + lane * 16) = nhB ? acc[1] : acc[3];
    __syncthreads();
    const f4 e = (nhB ? acc[2] : acc[0]) + LDSF4(L_PR + (w ^ 4) * 2048 + lane * 16);
    const f4 o = (nhB ? acc[3] : acc[1]) + LDSF4(L_PR + (w ^ 4) * 2048 + 1024 + lane * 16);
    const f4 g0 = LDSF4(L_GA + (32 * nhB + 8 * g) * 4), g1 = LDSF4(L_GA + (32 * nhB + 8 * g) * 4 + 16);
    const v4u a2 = LDS4(L_A2 + (56 * c8 + 32 * nhB + 8 * g) * 2);
    v4u x1;
    x1.x = pk(lo2f(xres.x) + g0.x * e[0], hi2f(xres.x) + g0.y * o[0]);
    x1.y = pk(lo2f(xres.y) + g0.z * e[1], hi2f(xres.y) + g0.w * o[1]);
    x1.z = pk(lo2f(xres.z) + g1.x * e[2], hi2f(xres.z) + g1.y * o[2]);
    x1.w = pk(lo2f(xres.w) + g1.z * e[3], hi2f(xres.w) + g1.w * o[3]);
    v4u y;
#pragma unroll
    for (int j = 0; j < 4; ++j) y[j] = nosent(pk(lo2f(x1[j]) * lo2f(a2[j]), hi2f(x1[j]) * hi2f(a2[j])));
    const bool vld = !(nhB == 1 && g == 3);
    st4s(rws, vld ? lane * 16 : OOB, WS_X1 + (4 * g32 + mtB) * 14336 + (7 * c8 + 4 * nhB) * 256, y);
    float s1 = 0.f, s2 = 0.f;
#pragma unroll
    for (int j = 0; j < 4; ++j) { s1 = dot2(x1[j], 0x3F803F80u, s1); s2 = dot2(x1[j], x1[j], s2); }
    s1 += __shfl_xor(s1, 16); s2 += __shfl_xor(s2, 16);
    s1 += __shfl_xor(s1, 32); s2 += __shfl_xor(s2, 32);
    st2s(rws, g == 0 ? l15 * 8 : OOB, WS_ST + (4 * g32 + mtB) * 2048 + (2 * c8 + nhB) * 128,
         (v2u){nosent(__builtin_bit_cast(u32, s1)), nosent(__builtin_bit_cast(u32, s2))});
    if (vld) LDS4(L_X1S + (mtB * 7 + 4 * nhB) * 256 + lane * 16) = x1;
  }
  STAMPT(8);
  f4 au[2] = {{0, 0, 0, 0}, {0, 0, 0, 0}}, av[2] = {{0, 0, 0, 0}, {0, 0, 0, 0}};
#pragma unroll
  for (int s = 0; s < 14; ++s) {
    const v4u a2 = LDS4(L_A2 + (32 * s + 8 * g) * 2), sb = LDS4(L_SB + (32 * s + 8 * g) * 2);
    au[0] = mfma(w1f[s][0], a2, au[0]); au[1] = mfma(w1f[s][1], a2, au[1]);
    av[0] = mfma(w1f[s][0], sb, av[0]); av[1] = mfma(w1f[s][1], sb, av[1]);
  }
  PIN(au[0]); PIN(au[1]); PIN(av[0]); PIN(av[1]);
  STAMPT(23);
  {
    v4u t[7], st[2];
    const u32 base = WS_X1 + 4 * g32 * 14336 + w * 7168;
    const u32 sto = w < 4 ? lane * 16 : OOB;
    for (unsigned spins = 0;; ++spins) {
      CBAR();
      u32 m = 0;
#pragma unroll
      for (int i = 0; i < 7; ++i) t[i] = ld4s(rws, lane * 16, base + i * 1024);
      st[0] = ld4s(rws, sto, WS_ST + (4 * g32 + (w & 3)) * 2048);
      st[1] = ld4s(rws, sto, WS_ST + (4 * g32 + (w & 3)) * 2048 + 1024);
#pragma unroll
      for (int i = 0; i < 7; ++i) m = chk(m, t[i]);
      m = chk(chk(m, st[0]), st[1]);
      if (!__any(m == ~0u) || spins > SPIN_CAP) break;
      canary(rws, lane < 28 ? base + lane * 256 : ((lane >= 32 && lane < 48 && w < 4) ? WS_ST + (4 * g32 + w) * 2048 + (lane - 32) * 128 : OOB), 0, spins);
    }
    RUNPRIO();
#pragma unroll
    for (int i = 0; i < 7; ++i) LDS4(w * 7168 + i * 1024 + lane * 16) = t[i];
    if (w < 4) {
      f4 a = __builtin_bit_cast(f4, st[0]) + __builtin_bit_cast(f4, st[1]);
#pragma unroll
      for (int j = 0; j < 4; ++j) { a[j] += dppf<0x128>(a[j]); a[j] += __shfl_xor(a[j], 16); a[j] += __shfl_xor(a[j], 32); }
      const float m0 = a.x * (1.f / D_), m1 = a.z * (1.f / D_);
      const float r0 = __builtin_amdgcn_rsqf(fmaxf(a.y * (1.f / D_) - m0 * m0, 0.f) + 1e-5f);
      const float r1 = __builtin_amdgcn_rsqf(fmaxf(a.w * (1.f / D_) - m1 * m1, 0.f) + 1e-5f);
      if (lane < 8) LDSF4(L_MR + w * 128 + lane * 16) = (f4){m0, r0, m1, r1};
    }
    __syncthreads();
  }
  STAMPT(9);
  v4u wf[7][4];
#pragma unroll
  for (int mp = 0; mp < 2; ++mp) {
    f4 acc[2][2] = {{{0, 0, 0, 0}, {0, 0, 0, 0}}, {{0, 0, 0, 0}, {0, 0, 0, 0}}};
    v4u xb[3][2];
#pragma unroll
    for (int m = 0; m < 2; ++m) { xb[0][m] = LDS4((2 * mp + m) * 14336 + lane * 16); xb[1][m] = LDS4((2 * mp + m) * 14336 + 1024 + lane * 16); }
#pragma unroll
    for (int s = 0; s < 14; ++s) {
      if (s < 12) {
#pragma unroll
        for (int m = 0; m < 2; ++m) xb[(s + 2) % 3][m] = LDS4((2 * mp + m) * 14336 + (s + 2) * 1024 + lane * 16);
      }
      SB0();
#pragma unroll
      for (int m = 0; m < 2; ++m) {
        acc[m][0] = mfma(w1f[s][0], xb[s % 3][m], acc[m][0]);
        acc[m][1] = mfma(w1f[s][1], xb[s % 3][m], acc[m][1]);
      }
      SB0();
      wf[(14 * mp + s) >> 2][(14 * mp + s) & 3] = ld4s(rws, lane * 16, WS_W2T + ((c8 * 56 + 7 * w) * 4 + 14 * mp + s) * 1024);
      SB0();
    }
#pragma unroll
    for (int m = 0; m < 2; ++m) {
      const f2 mr2 = *(LAS f2*)(lds + L_MR + ((2 * mp + m) * 16 + l15) * 8);
      const f2 nm = {-mr2.x, -mr2.x}, rs = {mr2.y, mr2.y};
      v4u hv;
#pragma unroll
      for (int r = 0; r < 4; ++r) {
        f2 h = __builtin_elementwise_fma(nm, (f2){au[0][r], au[1][r]}, (f2){acc[m][0][r], acc[m][1][r]});
        h = __builtin_elementwise_fma(rs, h, (f2){av[0][r], av[1][r]});
        hv[r] = pk(fmaxf(h.x, 0.f), fmaxf(h.y, 0.f));
      }
      st4s(rws, w < 7 ? lane * 16 : OOB, WS_HID + (4 * g32 + 2 * mp + m) * 57344 + (28 * c8 + 4 * (w % 7)) * 256, hv);
    }
    SB0();
  }
  STAMPT(10);
  __syncthreads();
  {
    v4u hf[2][2][7];
#define HLOAD(mp) { _Pragma("unroll") for (int i = 0; i < 2; ++i) _Pragma("unroll") for (int s = 0; s < 7; ++s) \
      hf[mp][i][s] = ld4s(rws, lane * 16, WS_HID + (4 * g32 + 2 * (mp) + i) * 57344 + w * 7168 + s * 1024); }
    HLOAD(0)
#pragma unroll
    for (int mp = 0; mp < 2; ++mp) {
      for (unsigned spins = 0;; ++spins) {
        u32 m = 0;
#pragma unroll
        for (int i = 0; i < 2; ++i)
#pragma unroll
          for (int s = 0; s < 7; ++s) m = chk(m, hf[mp][i][s]);
        if (!__any(m == ~0u) || spins > SPIN_CAP) break;
        canary(rws, (lane & 7) < 7 && lane < 16 ? (lane >> 3) * 57344 + (lane & 7) * 1024 : OOB, WS_HID + (4 * g32 + 2 * mp) * 57344 + w * 7168, spins);
        CBAR();
        HLOAD(mp)
      }
      RUNPRIO();
      if (mp == 0) STAMPT(11);
#pragma unroll
      for (int i = 0; i < 2; ++i) {
        f4 acc[4] = {{0, 0, 0, 0}, {0, 0, 0, 0}, {0, 0, 0, 0}, {0, 0, 0, 0}};
#pragma unroll
        for (int s = 0; s < 7; ++s)
#pragma unroll
          for (int t = 0; t < 4; ++t) acc[t] = mfma(wf[s][t], hf[mp][i][s], acc[t]);
#pragma unroll
        for (int r = 0; r < 4; ++r) LDSF4(((w * 4 + 2 * mp + i) * 4 + r) * 1024 + lane * 16) = (f4){acc[0][r], acc[1][r], acc[2][r], acc[3][r]};
        if (mp == 0 && i == 0) { SB0(); HLOAD(1) SB0(); }
      }
    }
  }
  __syncthreads();
  STAMPT(12);
  {
    const int mt = tid >> 7, rp = (tid >> 6) & 1;
    if (!(g == 3 && rp == 1)) {
      f4 s0 = {0, 0, 0, 0}, s1 = {0, 0, 0, 0};
#pragma unroll
      for (int ww = 0; ww < 8; ++ww) {
        s0 += LDSF4(((ww * 4 + mt) * 4 + 2 * rp) * 1024 + lane * 16);
        s1 += LDSF4(((ww * 4 + mt) * 4 + 2 * rp + 1) * 1024 + lane * 16);
      }
      const v4u x1 = LDS4(L_X1S + (mt * 7 + 2 * g + rp) * 256 + l15 * 16);
      const f4 g0 = LDSF4(L_GB + (16 * g + 8 * rp) * 4), g1 = LDSF4(L_GB + (16 * g + 8 * rp) * 4 + 16);
      v4u o;
      o.x = pk(lo2f(x1.x) + g0.x * s0.x, hi2f(x1.x) + g0.y * s0.y);
      o.y = pk(lo2f(x1.y) + g0.z * s0.z, hi2f(x1.y) + g0.w * s0.w);
      o.z = pk(lo2f(x1.z) + g1.x * s1.x, hi2f(x1.z) + g1.y * s1.y);
      o.w = pk(lo2f(x1.w) + g1.z * s1.z, hi2f(x1.w) + g1.w * s1.w);
      __builtin_nontemporal_store(o, (v4u*)(out + (size_t)(64 * g32 + 16 * mt + l15) * D_ + 56 * c8 + 16 * g + 8 * rp));
    }
  }
  if (b == 0 && w == 1) {
    for (unsigned spins = 0;; ++spins) {
      bool ok = true;
#pragma unroll
      for (int i = 0; i < 4; ++i) ok &= (__hip_atomic_load(gr + G_PREP + lane + 64 * i, RLX_AGENT) >> 16) == tag;
      if (__all(ok) || spins > SPIN_CAP) break;
      BACKOFF();
    }
    if (lane == 0) __hip_atomic_store((gu64*)ws, epoch + 1, RLX_AGENT);
  }
  { const u64 dt = __builtin_amdgcn_s_memrealtime() - dbg_t0; __syncthreads(); if (b == 100 && tid == 0 && dt >= 3500 && dt < 3700) out[(size_t)(64 * g32) * D_ + 56 * c8] = 0x4700; }
  STAMPT(13);
  asm volatile(".Lmega_end:");
}

extern "C" void kernel_launch(void* const* d_in, const int* in_sizes, int n_in, void* d_out, int out_size, void* d_ws,
                              size_t ws_size, hipStream_t stream) {
  (void)hipFuncSetAttribute((const void*)mega, hipFuncAttributeMaxDynamicSharedMemorySize, LDS_BYTES);
  mega<<<256, 512, LDS_BYTES, stream>>>((const u16*)d_in[0], (const u16*)d_in[1], (const u16*)d_in[2], (const u16*)d_in[4],
                                        (const u16*)d_in[5], (const u16*)d_in[6], (const u16*)d_in[7], (u16*)d_out, (char*)d_ws);
}
```

```cpp
#include <hip/hip_runtime.h>
#include <stdint.h>

typedef unsigned int u32;
typedef unsigned long long u64;
typedef unsigned short u16;
typedef u32 v4u __attribute__((ext_vector_type(4)));
typedef u32 v2u __attribute__((ext_vector_type(2)));
typedef float f4 __attribute__((ext_vector_type(4)));
typedef float f2 __attribute__((ext_vector_type(2)));
typedef __bf16 bf2v __attribute__((ext_vector_type(2)));
typedef __bf16 bf8v __attribute__((ext_vector_type(8)));
#define GAS __attribute__((address_space(1)))
#define LAS __attribute__((address_space(3)))
typedef GAS u64 gu64;
#define RLX_AGENT __ATOMIC_RELAXED, __HIP_MEMORY_SCOPE_AGENT
typedef __amdgpu_buffer_rsrc_t rsrc_t;

constexpr int S_ = 2048, D_ = 448, DFF_ = 1792;
constexpr int WS_GRAN = 4096;
constexpr int G_PREP = 2688, G_FILL = 2944;
constexpr int G_REP = 4096;
constexpr int WS_KV = 524288;
constexpr int WS_O = WS_KV + 4194304;
constexpr int WS_X1 = WS_O + 2097152;
constexpr int WS_ST = WS_X1 + 1835008;
constexpr int WS_HID = WS_ST + 262144;
constexpr int WS_W2T = WS_HID + 7340032;
constexpr int WS_W1T = WS_W2T + 1835008;
constexpr int WS_WOT = WS_W1T + 1605632;
constexpr int WS_END = WS_WOT + 524288;
constexpr int L_WQ = 0;
constexpr int L_K = 0, L_V = 24576;
constexpr int L_PR = 57344;
constexpr int L_WO = 86016;
constexpr int L_X = 36864;
constexpr int L_UV = L_WO;
constexpr int L_SM = 151552;
constexpr int L_AS1 = L_SM;
constexpr int L_A2 = L_AS1 + 1792;
constexpr int L_SB = L_A2 + 896;
constexpr int L_GA = L_SB + 896;
constexpr int L_GB = L_GA + 256;
constexpr int L_MR = L_GB + 256;
constexpr int L_RED = L_MR + 512;
constexpr int L_X1S = L_RED;
constexpr int L_END = L_X1S + 7168;
static_assert(L_END <= 163840, "lds");
constexpr int LDS_BYTES = 163840;
constexpr u32 OOB = 0x80000000u;
constexpr unsigned SPIN_CAP = 1u << 15;

__device__ __forceinline__ u32 pk(float lo, float hi) { f2 v = {lo, hi}; return __builtin_bit_cast(u32, __builtin_convertvector(v, bf2v)); }
__device__ __forceinline__ float dot2(u32 a, u32 b, float c) { return __builtin_amdgcn_fdot2_f32_bf16(__builtin_bit_cast(bf2v, a), __builtin_bit_cast(bf2v, b), c, false); }
__device__ __forceinline__ f4 mfma(v4u a, v4u b, f4 c) { return __builtin_amdgcn_mfma_f32_16x16x32_bf16(__builtin_bit_cast(bf8v, a), __builtin_bit_cast(bf8v, b), c, 0, 0, 0); }
__device__ __forceinline__ float lo2f(u32 v) { return __builtin_bit_cast(float, v << 16); }
__device__ __forceinline__ float hi2f(u32 v) { return __builtin_bit_cast(float, v & 0xffff0000u); }
__device__ __forceinline__ u32 plo(u32 r0, u32 r1) { return __builtin_amdgcn_perm(r1, r0, 0x05040100u); }
__device__ __forceinline__ u32 phi(u32 r0, u32 r1) { return __builtin_amdgcn_perm(r1, r0, 0x07060302u); }
__device__ __forceinline__ u32 nosent(u32 v) { return v < 0xFFFFFFFEu ? v : 0xFFFFFFFEu; }
__device__ __forceinline__ v4u nosent4(v4u v) { v.x = nosent(v.x); v.y = nosent(v.y); v.z = nosent(v.z); v.w = nosent(v.w); return v; }
__device__ __forceinline__ u32 umax3(u32 a, u32 b, u32 c) { u32 m = a > b ? a : b; return m > c ? m : c; }
__device__ __forceinline__ u32 chk(u32 m, v4u v) { return umax3(umax3(m, v.x, v.y), v.z, v.w); }
template <int C> __device__ __forceinline__ float dppf(float v) { return __builtin_bit_cast(float, __builtin_amdgcn_update_dpp(0, __builtin_bit_cast(int, v), C, 0xf, 0xf, false)); }
__device__ __forceinline__ float rowsum16(float v) { v += dppf<0xB1>(v); v += dppf<0x4E>(v); v += dppf<0x141>(v); v += dppf<0x140>(v); return v; }
__device__ __forceinline__ v4u ld4(rsrc_t r, u32 vo, u32 so) { return __builtin_amdgcn_raw_buffer_load_b128(r, vo, so, 0); }
__device__ __forceinline__ v4u ld4s(rsrc_t r, u32 vo, u32 so) { return __builtin_amdgcn_raw_buffer_load_b128(r, vo, so, 16); }
__device__ __forceinline__ v2u ld2(rsrc_t r, u32 vo, u32 so) { return __builtin_amdgcn_raw_buffer_load_b64(r, vo, so, 0); }
__device__ __forceinline__ u32 ld1(rsrc_t r, u32 vo, u32 so) { return __builtin_amdgcn_raw_buffer_load_b32(r, vo, so, 0); }
__device__ __forceinline__ void st4s(rsrc_t r, u32 vo, u32 so, v4u v) { __builtin_amdgcn_raw_buffer_store_b128(v, r, vo + so, 0, 16); }
__device__ __forceinline__ void st2s(rsrc_t r, u32 vo, u32 so, v2u v) { __builtin_amdgcn_raw_buffer_store_b64(v, r, vo, so, 16); }
__device__ __forceinline__ v4u modulate(const u32 (&xh)[8], v4u a0, v4u a1) {
  v4u h;
  float d0, d1, d2, d3, d4, d5, d6, d7;
  asm volatile(
      "v_dot2_f32_bf16 %4, %12, %20, 0\n\tv_dot2_f32_bf16 %5, %13, %21, 0\n\tv_dot2_f32_bf16 %6, %14, %22, 0\n\tv_dot2_f32_bf16 %7, %15, %23, 0\n\t"
      "v_dot2_f32_bf16 %8, %16, %24, 0\n\tv_dot2_f32_bf16 %9, %17, %25, 0\n\tv_dot2_f32_bf16 %10, %18, %26, 0\n\tv_dot2_f32_bf16 %11, %19, %27, 0\n\t"
      "v_cvt_pk_bf16_f32 %0, %4, %5\n\tv_cvt_pk_bf16_f32 %1, %6, %7\n\tv_cvt_pk_bf16_f32 %2, %8, %9\n\tv_cvt_pk_bf16_f32 %3, %10, %11\n\ts_nop 1"
      : "=&v"(h.x), "=&v"(h.y), "=&v"(h.z), "=&v"(h.w), "=&v"(d0), "=&v"(d1), "=&v"(d2), "=&v"(d3), "=&v"(d4), "=&v"(d5), "=&v"(d6), "=&v"(d7)
      : "v"(xh[0]), "v"(xh[1]), "v"(xh[2]), "v"(xh[3]), "v"(xh[4]), "v"(xh[5]), "v"(xh[6]), "v"(xh[7]),
        "v"(a0.x), "v"(a0.y), "v"(a0.z), "v"(a0.w), "v"(a1.x), "v"(a1.y), "v"(a1.z), "v"(a1.w));
  return h;
}
__device__ __forceinline__ v2u f1epi(float a0, float a1, float a2, float a3, float u0, float u1, float u2, float u3,
                                     float v0, float v1, float v2, float v3, float nm, float rs) {
  v2u o;
  float t0, t1, t2, t3;
  asm volatile(
      "s_nop 7\n\t"
      "v_fma_f32 %2, %18, %10, %6\n\tv_fma_f32 %3, %18, %11, %7\n\tv_fma_f32 %4, %18, %12, %8\n\tv_fma_f32 %5, %18, %13, %9\n\t"
      "v_fma_f32 %2, %19, %2, %14\n\tv_fma_f32 %3, %19, %3, %15\n\tv_fma_f32 %4, %19, %4, %16\n\tv_fma_f32 %5, %19, %5, %17\n\t"
      "v_max_f32 %2, 0, %2\n\tv_max_f32 %3, 0, %3\n\tv_max_f32 %4, 0, %4\n\tv_max_f32 %5, 0, %5\n\t"
      "v_cvt_pk_bf16_f32 %0, %2, %3\n\tv_cvt_pk_bf16_f32 %1, %4, %5"
      : "=&v"(o.x), "=&v"(o.y), "=&v"(t0), "=&v"(t1), "=&v"(t2), "=&v"(t3)
      : "v"(a0), "v"(a1), "v"(a2), "v"(a3), "v"(u0), "v"(u1), "v"(u2), "v"(u3), "v"(v0), "v"(v1), "v"(v2), "v"(v3), "v"(nm), "v"(rs));
  return o;
}
typedef short s4v __attribute__((ext_vector_type(4)));
__device__ __forceinline__ v2u trrd(LAS char* p) { return __builtin_bit_cast(v2u, __builtin_amdgcn_ds_read_tr16_b64_v4i16((LAS s4v*)p)); }
__device__ __forceinline__ v4u mksrd(const void* p, u32 bytes) {
  const u64 a = (u64)p;
  return (v4u){(u32)__builtin_amdgcn_readfirstlane((u32)a), (u32)__builtin_amdgcn_readfirstlane((u32)(a >> 32)) & 0xffffu, bytes, 0x00020000u};
}
template <bool SC1> __device__ __forceinline__ void dma16(v4u srd, u32 vo, u32 so, u32 ldsaddr) {
  u32 keep;
  so = __builtin_amdgcn_readfirstlane(so);
  ldsaddr = __builtin_amdgcn_readfirstlane(ldsaddr);
  if (SC1)
    asm volatile("s_mov_b32 %0, m0\n\ts_mov_b32 m0, %4\n\ts_nop 3\n\tbuffer_load_dwordx4 %1, %2, %3 offen sc1 lds\n\ts_mov_b32 m0, %0"
                 : "=&s"(keep) : "v"(vo), "s"(srd), "s"(so), "s"(ldsaddr) : "memory");
  else
    asm volatile("s_mov_b32 %0, m0\n\ts_mov_b32 m0, %4\n\ts_nop 3\n\tbuffer_load_dwordx4 %1, %2, %3 offen lds\n\ts_mov_b32 m0, %0"
                 : "=&s"(keep) : "v"(vo), "s"(srd), "s"(so), "s"(ldsaddr) : "memory");
}
__device__ __forceinline__ void canary(rsrc_t r, u32 vo, u32 so, unsigned& spins) {
  __builtin_amdgcn_s_setprio(0);
  for (; spins <= SPIN_CAP; ++spins) {
    __builtin_amdgcn_s_sleep(2);
    asm volatile("" ::: "memory");
    if (!__any(__builtin_amdgcn_raw_buffer_load_b32(r, vo, so, 16) == ~0u)) break;
  }
}
#define LDS4(off) (*(LAS v4u*)(lds + (off)))
#define LDS2(off) (*(LAS v2u*)(lds + (off)))
#define LDSF4(off) (*(LAS f4*)(lds + (off)))
#define LDSF(off) (*(LAS float*)(lds + (off)))
#define LDSH(off) (*(LAS u16*)(lds + (off)))
#define CBAR() asm volatile("" ::: "memory")
#define PIN(v) asm volatile("" : "+v"(v))
#define SB0() __builtin_amdgcn_sched_barrier(0)
#define BACKOFF() do { __builtin_amdgcn_s_setprio(0); __builtin_amdgcn_s_sleep(6); } while (0)
#define RUNPRIO() __builtin_amdgcn_s_setprio(1)
#define DRAIN() asm volatile("s_waitcnt vmcnt(0)" ::: "memory")

__global__ __launch_bounds__(512, 2) void mega(const u16* __restrict__ x, const u16* __restrict__ wqkv, const u16* __restrict__ wo,
                                               const u16* __restrict__ w1, const u16* __restrict__ w2, const u16* __restrict__ cvec,
                                               const u16* __restrict__ wmod, u16* __restrict__ out, char* ws) {
  extern __shared__ __attribute__((aligned(16))) char lds_g[];
  LAS char* lds = (LAS char*)lds_g;
  const int tid = threadIdx.x, lane = tid & 63, w = __builtin_amdgcn_readfirstlane(tid >> 6) & 7;
  const int l15 = lane & 15, g = lane >> 4;
  const int b = blockIdx.x, xcd = b & 7, slot = b >> 3;
  const int g16 = xcd * 2 + (slot >> 4), head = slot & 15;
  const int g32 = xcd * 4 + (slot >> 3), c8 = slot & 7;
  const rsrc_t rws = __builtin_amdgcn_make_buffer_rsrc(ws, 0, WS_END, 0x00020000);
  const rsrc_t rx = __builtin_amdgcn_make_buffer_rsrc((void*)x, 0, S_ * D_ * 2, 0x00020000);
  const rsrc_t rwq = __builtin_amdgcn_make_buffer_rsrc((void*)wqkv, 0, D_ * 3 * D_ * 2, 0x00020000);
  const rsrc_t rwm = __builtin_amdgcn_make_buffer_rsrc((void*)wmod, 0, D_ * 6 * D_ * 2, 0x00020000);
  const rsrc_t rc = __builtin_amdgcn_make_buffer_rsrc((void*)cvec, 0, D_ * 2, 0x00020000);
  const v4u SENT = {~0u, ~0u, ~0u, ~0u};
  RUNPRIO();
  u32 cpf, mpf;
  u64 epoch;
  {
    u64 pc;
    u32 len;
    asm volatile("s_getpc_b64 %0\n.Lmega_start:\n\ts_mov_b32 %1, .Lmega_end-.Lmega_start" : "=s"(pc), "=s"(len));
    const v4u scode = mksrd((const void*)pc, len), swm0 = mksrd(wmod, D_ * 6 * D_ * 2), sws0 = mksrd(ws, 64);
    const int idx = slot * 84 + tid - 320, n = idx / 448, row = idx - 448 * n, j = xcd + 8 * n;
    asm volatile("s_nop 4\n\tbuffer_load_dword %0, %3, %5, 0 offen\n\tbuffer_load_dword %1, %4, %6, 0 offen\n\tbuffer_load_dwordx2 %2, off, %7, 0 sc1"
                 : "=&v"(cpf), "=&v"(mpf), "=&v"(epoch)
                 : "v"(tid >= 448 ? (lane * 8 + (slot & 7)) * 128 : OOB), "v"((tid >= 320 && tid < 404 && j < 42) ? (u32)(row * 5376 + j * 128) : OOB),
                   "s"(scode), "s"(swm0), "s"(sws0)
                 : "memory");
  }
  gu64* gr0 = (gu64*)(ws + WS_GRAN);
  gu64* gr = gr0 + xcd * G_REP;
  const int mj = xcd + 8 * (slot / 3);
  const bool modw = (slot % 3 == 0) && mj < 42 && w < 4;
  if (tid < 4) *(volatile LAS u32*)(lds + L_RED + 3072 + tid * 4) = 0u;
  v2u mr[4][8];
  u32 craw;
  if (modw) {
    const v4u swm = mksrd(wmod, D_ * 6 * D_ * 2), sc_ = mksrd(cvec, D_ * 2);
    asm volatile("s_nop 4\n\tbuffer_load_dword %0, %1, %2, 0 offen" : "=&v"(craw) : "v"(w + 4 * g < 14 ? (32 * (w + 4 * g) + 2 * l15) * 2 : OOB), "s"(sc_) : "memory");
#pragma unroll
    for (int i = 0; i < 4; ++i) {
      const u32 vo = (i < 3 || w < 2) ? (u32)(8 * g * 2688 + 4 * l15) * 2 : OOB;
      const u32 so = (u32)((32 * ((w + 4 * i) % 14)) * 2688 + 64 * mj) * 2;
      asm volatile(
          "s_nop 4\n\t"
          "buffer_load_dwordx2 %0, %8, %9, %10 offen\n\tbuffer_load_dwordx2 %1, %8, %9, %11 offen\n\t"
          "buffer_load_dwordx2 %2, %8, %9, %12 offen\n\tbuffer_load_dwordx2 %3, %8, %9, %13 offen\n\t"
          "buffer_load_dwordx2 %4, %8, %9, %14 offen\n\tbuffer_load_dwordx2 %5, %8, %9, %15 offen\n\t"
          "buffer_load_dwordx2 %6, %8, %9, %16 offen\n\tbuffer_load_dwordx2 %7, %8, %9, %17 offen"
          : "=&v"(mr[i][0]), "=&v"(mr[i][1]), "=&v"(mr[i][2]), "=&v"(mr[i][3]), "=&v"(mr[i][4]), "=&v"(mr[i][5]), "=&v"(mr[i][6]), "=&v"(mr[i][7])
          : "v"(vo), "s"(swm), "s"(so), "s"(so + 5376), "s"(so + 2 * 5376), "s"(so + 3 * 5376), "s"(so + 4 * 5376), "s"(so + 5 * 5376),
            "s"(so + 6 * 5376), "s"(so + 7 * 5376)
          : "memory");
    }
  }
  asm volatile("s_waitcnt lgkmcnt(0)\n\ts_barrier" ::: "memory");
  const v4u sq_srd = mksrd(wqkv, D_ * 3 * D_ * 2);
  const u32 vq = (u32)((lane >> 2) * 2688 + (((lane & 3) ^ (2 * (lane >> 5))) * 16));
#define WQDMA(i)                                                                                                        \
  {                                                                                                                     \
    const int u = w + 8 * ((i) >> 1), d = 2 * u + ((i) & 1), mat = u / 14, kb = d - 28 * mat;                           \
    dma16<false>(sq_srd, u < 42 ? vq : OOB, (u32)(16 * kb * 2688 + (mat % 3) * 896 + 56 * head), u < 42 ? L_WQ + d * 1024 : L_RED + 4096); \
  }
  {
    const v4u sx = mksrd(x, S_ * D_ * 2);
    const u32 vo = (u32)((lane >> 3) * D_ * 2 + (((lane & 7) ^ (lane >> 3)) * 16));
#pragma unroll
    for (int q = 0; q < 14; ++q) dma16<false>(sx, vo, (u32)((128 * g16 + 16 * w + 8 * (q & 1)) * D_ * 2 + (q >> 1) * 128), L_X + w * 14336 + q * 1024);
  }
  asm volatile("s_barrier" ::: "memory");
  WQDMA(0) WQDMA(1) WQDMA(2) WQDMA(3)
  u32 pr[8];
  u32 pdst;
  {
    const int u = b * 8 + w;
    const u16* base; u32 bytes, ld, so, vo, vo2;
    if (u < 896) {
      const int cc = u / 112, v = u - 112 * cc, kb = v >> 1, hf = v & 1;
      base = w2; bytes = DFF_ * D_ * 2; ld = D_ * 2; so = (u32)(32 * kb * D_ + 56 * cc + 2 * hf) * 2;
      vo = l15 < 14 ? (u32)(8 * g * D_ + 4 * l15) * 2 : OOB; vo2 = vo;
      pdst = WS_W2T + ((cc * 56 + kb) * 4 + 2 * hf) * 1024;
    } else if (u < 1680) {
      const int v = u - 896, cb = v / 14, s = v - 14 * cb;
      base = w1; bytes = DFF_ * D_ * 2; ld = DFF_ * 2; so = (u32)(32 * s * DFF_ + 32 * cb) * 2;
      vo = (u32)(8 * g * DFF_ + 2 * l15) * 2; vo2 = vo;
      pdst = WS_W1T + (cb * 14 + s) * 2048;
    } else {
      const int v = u - 1680, hh = v & 15, nh = (v >> 4) & 1, cc = (v >> 5) & 7;
      base = wo; bytes = D_ * D_ * 2; ld = D_ * 2; so = (u32)(28 * hh * D_ + 56 * cc + 32 * nh) * 2;
      vo = (u < 1936 && (nh == 0 || l15 < 12)) ? (u32)(4 * g * D_ + 2 * l15) * 2 : OOB; vo2 = g < 3 ? vo + 12 * D_ * 2 : OOB;
      pdst = u < 1936 ? WS_WOT + (cc * 32 + nh * 16 + hh) * 2048 : OOB;
    }
    const v4u sp = mksrd(base, __builtin_amdgcn_readfirstlane(bytes));
    so = __builtin_amdgcn_readfirstlane(so); ld = __builtin_amdgcn_readfirstlane(ld);
    asm volatile(
        "s_nop 4\n\t"
        "buffer_load_dword %0, %8, %10, %11 offen\n\tbuffer_load_dword %1, %8, %10, %12 offen\n\t"
        "buffer_load_dword %2, %8, %10, %13 offen\n\tbuffer_load_dword %3, %8, %10, %14 offen\n\t"
        "buffer_load_dword %4, %9, %10, %15 offen\n\tbuffer_load_dword %5, %9, %10, %16 offen\n\t"
        "buffer_load_dword %6, %9, %10, %17 offen\n\tbuffer_load_dword %7, %9, %10, %18 offen"
        : "=&v"(pr[0]), "=&v"(pr[1]), "=&v"(pr[2]), "=&v"(pr[3]), "=&v"(pr[4]), "=&v"(pr[5]), "=&v"(pr[6]), "=&v"(pr[7])
        : "v"(vo), "v"(vo2), "s"(sp), "s"(so), "s"(so + ld), "s"(so + 2 * ld), "s"(so + 3 * ld), "s"(so + 4 * ld), "s"(so + 5 * ld), "s"(so + 6 * ld),
          "s"(so + 7 * ld)
        : "memory");
  }
  SB0();
  asm volatile("s_waitcnt vmcnt(26)" : "+v"(epoch), "+v"(cpf), "+v"(mpf)::"memory");
  const u64 tag = (epoch + 1) & 0xFFFFFFFFFFFFull;
  if (modw) {
    __builtin_amdgcn_s_setprio(3);
    asm volatile(""
                 : "+v"(craw), "+v"(mr[0][0]), "+v"(mr[0][1]), "+v"(mr[0][2]), "+v"(mr[0][3]), "+v"(mr[0][4]), "+v"(mr[0][5]), "+v"(mr[0][6]), "+v"(mr[0][7]),
                   "+v"(mr[1][0]), "+v"(mr[1][1]), "+v"(mr[1][2]), "+v"(mr[1][3]), "+v"(mr[1][4]), "+v"(mr[1][5]), "+v"(mr[1][6]), "+v"(mr[1][7])::"memory");
    asm volatile(""
                 : "+v"(mr[2][0]), "+v"(mr[2][1]), "+v"(mr[2][2]), "+v"(mr[2][3]), "+v"(mr[2][4]), "+v"(mr[2][5]), "+v"(mr[2][6]), "+v"(mr[2][7]),
                   "+v"(mr[3][0]), "+v"(mr[3][1]), "+v"(mr[3][2]), "+v"(mr[3][3]), "+v"(mr[3][4]), "+v"(mr[3][5]), "+v"(mr[3][6]), "+v"(mr[3][7])::"memory");
    float c0 = lo2f(craw), c1 = hi2f(craw);
    c0 = c0 * __builtin_amdgcn_rcpf(1.f + __expf(-c0));
    c1 = c1 * __builtin_amdgcn_rcpf(1.f + __expf(-c1));
    *(LAS u32*)(lds + L_RED + w * 256 + lane * 4) = pk(c0, c1);
    f4 acc[4] = {{0, 0, 0, 0}, {0, 0, 0, 0}, {0, 0, 0, 0}, {0, 0, 0, 0}};
#pragma unroll
    for (int i = 0; i < 4; ++i) {
      const v4u sf = LDS4(L_RED + w * 256 + i * 64 + g * 16);
      acc[0] = mfma((v4u){plo(mr[i][0].x, mr[i][1].x), plo(mr[i][2].x, mr[i][3].x), plo(mr[i][4].x, mr[i][5].x), plo(mr[i][6].x, mr[i][7].x)}, sf, acc[0]);
      acc[1] = mfma((v4u){phi(mr[i][0].x, mr[i][1].x), phi(mr[i][2].x, mr[i][3].x), phi(mr[i][4].x, mr[i][5].x), phi(mr[i][6].x, mr[i][7].x)}, sf, acc[1]);
      acc[2] = mfma((v4u){plo(mr[i][0].y, mr[i][1].y), plo(mr[i][2].y, mr[i][3].y), plo(mr[i][4].y, mr[i][5].y), plo(mr[i][6].y, mr[i][7].y)}, sf, acc[2]);
      acc[3] = mfma((v4u){phi(mr[i][0].y, mr[i][1].y), phi(mr[i][2].y, mr[i][3].y), phi(mr[i][4].y, mr[i][5].y), phi(mr[i][6].y, mr[i][7].y)}, sf, acc[3]);
    }
    if (l15 == 0) {
#pragma unroll
      for (int r = 0; r < 4; ++r) LDSF4(L_RED + 1024 + w * 256 + (16 * g + 4 * r) * 4) = (f4){acc[0][r], acc[1][r], acc[2][r], acc[3][r]};
    }
    asm volatile("s_waitcnt lgkmcnt(0)" ::: "memory");
    u32 old = 0;
    if (lane == 0) old = __hip_atomic_fetch_add((LAS u32*)(lds + L_RED + 3072), 1u, __ATOMIC_RELAXED, __HIP_MEMORY_SCOPE_WORKGROUP);
    if (__builtin_amdgcn_readfirstlane(old) == 3u) {
      CBAR();
      const float sum = (LDSF(L_RED + 1024 + lane * 4) + LDSF(L_RED + 1280 + lane * 4)) + (LDSF(L_RED + 1536 + lane * 4) + LDSF(L_RED + 1792 + lane * 4));
      const u64 gval = (tag << 16) | (u64)(pk(sum, 0.f) & 0xffffu);
#pragma unroll
      for (int r = 0; r < 8; ++r) __hip_atomic_store(gr0 + r * G_REP + 64 * mj + lane, gval, RLX_AGENT);
    }
    RUNPRIO();
  }
  if (modw) asm volatile("s_waitcnt vmcnt(8)" ::: "memory"); else asm volatile("s_waitcnt vmcnt(12)" ::: "memory");
  v4u xr[14];
  {
    const u32 ra0 = L_X + w * 14336 + (l15 >> 3) * 1024 + (l15 & 7) * 128 + ((g ^ (l15 & 7)) * 16);
    const u32 ra1 = L_X + w * 14336 + (l15 >> 3) * 1024 + (l15 & 7) * 128 + (((4 + g) ^ (l15 & 7)) * 16);
#pragma unroll
    for (int s = 0; s < 14; ++s) xr[s] = LDS4(((s & 1) ? ra1 : ra0) + (s >> 1) * 2048);
#pragma unroll
    for (int s = 0; s < 14; ++s) PIN(xr[s]);
  }
  asm volatile("s_waitcnt lgkmcnt(0)\n\ts_barrier" ::: "memory");
  const int mtB = w & 3, nhB = w >> 2;
  v4u xres;
  const int tau = 16 * (w & 1) + l15;
  const u32 koff = (w >> 1) * 2048 + ((tau >> 2) & 1) * 1024 + (g * 16 + 4 * (tau >> 3) + (tau & 3)) * 16;
  const u32 voff = (w >> 1) * 2048 + ((2 * (w & 1) + (g >> 1)) * 16 + l15) * 16 + (g & 1) * 8;
  const u32 kvb = g16 < 15 ? WS_KV + (g16 * 16 + head) * 16384 : OOB;
  u32 xh[14][8];
  {
    const v4u ONES = {0x3F803F80u, 0x3F803F80u, 0x3F803F80u, 0x3F803F80u};
    f4 sm = {0, 0, 0, 0}, gm = {0, 0, 0, 0};
#pragma unroll
    for (int s = 0; s < 14; ++s) {
      if (s == 0 || s == 7) WQDMA(4 + s / 7)
      sm = mfma(ONES, xr[s], sm);
      gm = mfma(xr[s], xr[s], gm);
    }
    float sq = (l15 & 2) ? ((l15 & 1) ? gm[3] : gm[2]) : ((l15 & 1) ? gm[1] : gm[0]);
    sq = __shfl(sq, 16 * (l15 >> 2) + l15);
    const float mean = sm[0] * (1.f / D_);
    const float rstd = __builtin_amdgcn_rsqf(fmaxf(sq * (1.f / D_) - mean * mean, 0.f) + 1e-5f);
    const f2 rs2 = {rstd, rstd}, nm2 = {-mean * rstd, -mean * rstd};
#pragma unroll
    for (int s = 0; s < 14; ++s) {
      if (s < 6) WQDMA(6 + s)
      else if (s == 7) xres = ld4(rx, (nhB == 1 && g == 3) ? OOB : (u32)(l15 * D_ + 8 * g) * 2, (u32)((64 * g32 + 16 * mtB) * D_ + 56 * c8 + 32 * nhB) * 2);
      else if (s == 9) st4s(rws, koff, kvb, SENT);
      else if (s == 11) st4s(rws, (w >> 1) * 2048 + (w & 1) * 512 + (lane & 31) * 16 + (lane >> 5) * 1024, kvb + 8192, SENT);
      else if (s == 13) st4s(rws, lane * 16, WS_O + ((8 * g16 + w) * 16 + head) * 1024, SENT);
#pragma unroll
      for (int j = 0; j < 4; ++j) {
        const f2 v = __builtin_elementwise_fma((f2){lo2f(xr[s][j]), hi2f(xr[s][j])}, rs2, nm2);
        xh[s][2 * j] = pk(v.x, 1.f);
        xh[s][2 * j + 1] = pk(v.y, 1.f);
        PIN(xh[s][2 * j]); PIN(xh[s][2 * j + 1]);
      }
      SB0();
    }
  }
  const int i1 = tid < 384 ? 512 + tid : tid;
  u64 g10 = __hip_atomic_load(gr + tid, RLX_AGENT), g11 = __hip_atomic_load(gr + i1, RLX_AGENT);
  asm volatile("s_waitcnt vmcnt(6)"
               : "+v"(pr[0]), "+v"(pr[1]), "+v"(pr[2]), "+v"(pr[3]), "+v"(pr[4]), "+v"(pr[5]), "+v"(pr[6]), "+v"(pr[7])::"memory");
  {
    LAS char* tb0 = lds + L_WQ + (8 * g + (l15 >> 2)) * 64 + (g & 1) * 32 + (l15 & 3) * 8;
    LAS char* tb1 = lds + L_WQ + (8 * g + (l15 >> 2)) * 64 + ((g & 1) ^ 1) * 32 + (l15 & 3) * 8;
    v2u r[6][4];
#pragma unroll
    for (int i = 0; i < 6; ++i) {
      const int u = (i < 5 || w < 2) ? w + 8 * i : w;
      r[i][0] = trrd(tb0 + u * 2048); r[i][1] = trrd(tb0 + u * 2048 + 256);
      r[i][2] = trrd(tb1 + u * 2048); r[i][3] = trrd(tb1 + u * 2048 + 256);
    }
    SB0();
#pragma unroll
    for (int i = 0; i < 6; ++i) {
      const int u = (i < 5 || w < 2) ? w + 8 * i : w;
      LDS4(L_WQ + u * 2048 + lane * 16) = (v4u){r[i][0].x, r[i][0].y, r[i][1].x, r[i][1].y};
      LDS4(L_WQ + u * 2048 + 1024 + lane * 16) = (v4u){r[i][2].x, r[i][2].y, r[i][3].x, r[i][3].y};
    }
  }
  v4u pe = {plo(pr[0], pr[1]), plo(pr[2], pr[3]), plo(pr[4], pr[5]), plo(pr[6], pr[7])};
  v4u po = {phi(pr[0], pr[1]), phi(pr[2], pr[3]), phi(pr[4], pr[5]), phi(pr[6], pr[7])};
  PIN(pe); PIN(po);
  {
    for (unsigned spins = 0;; ++spins) {
      const bool ok = (g10 >> 16) == tag && (g11 >> 16) == tag;
      if (__all(ok) || spins > SPIN_CAP) break;
      BACKOFF();
      g10 = __hip_atomic_load(gr + tid, RLX_AGENT);
      g11 = __hip_atomic_load(gr + i1, RLX_AGENT);
    }
    RUNPRIO();
    if (tid < 448) LDSH(L_AS1 + tid * 4 + 2) = (u16)g10;
    else LDSH(L_AS1 + (tid - 448) * 4) = (u16)pk(1.f + lo2f((u32)g10), 0.f);
    if (tid < 384) LDSH(L_AS1 + (64 + tid) * 4) = (u16)pk(1.f + lo2f((u32)g11), 0.f);
  }
  DRAIN();
  __syncthreads();
  if (tid < 8) __hip_atomic_store(gr0 + tid * G_REP + G_FILL + b, tag << 16, RLX_AGENT);
  const int jg = tid - 448;
  const int i20 = tid < 448 ? 1344 + tid : 896 + 56 * c8 + (jg < 56 ? jg : 0);
  const int i21 = tid < 448 ? 1792 + tid : 2240 + 56 * c8 + (jg < 56 ? jg : 0);
  u64 g20 = __hip_atomic_load(gr + i20, RLX_AGENT), g21 = __hip_atomic_load(gr + i21, RLX_AGENT);

  v4u hfr[14];
  {
    v4u as[3][2];
#pragma unroll
    for (int s = 0; s < 2; ++s) { as[s][0] = LDS4(L_AS1 + (32 * s + 8 * g) * 4); as[s][1] = LDS4(L_AS1 + (32 * s + 8 * g) * 4 + 16); }
#pragma unroll
    for (int s = 0; s < 14; ++s) {
      if (s < 12) { as[(s + 2) % 3][0] = LDS4(L_AS1 + (32 * (s + 2) + 8 * g) * 4); as[(s + 2) % 3][1] = LDS4(L_AS1 + (32 * (s + 2) + 8 * g) * 4 + 16); }
      SB0();
      hfr[s] = modulate(xh[s], as[s % 3][0], as[s % 3][1]);
      SB0();
    }
  }
  int dep = b;
  if (lane < 16) dep = xcd + 8 * (16 * (g16 & 1) + lane);
  else if (lane == 16) dep = g16 >= 1 ? ((g16 - 1) >> 1) + 8 * (16 * ((g16 - 1) & 1) + head) : b;
  else if (lane == 17) dep = g16 >= 2 ? ((g16 - 2) >> 1) + 8 * (16 * (g16 & 1) + head) : b;
  else if (lane < 32) dep = 14 * c8 + (lane - 18);
  else if (lane < 46) dep = ((896 + 98 * c8) >> 3) + (lane - 32);
  else if (lane < 50) dep = 210 + 4 * c8 + (lane - 46);
  u64 dfl = tag << 16;
  f4 aq[6];
#pragma unroll
  for (int t = 0; t < 6; ++t) aq[t] = (f4){0, 0, 0, 0};
  {
    v4u fb[3][6];
#define QLOAD(s, d) { _Pragma("unroll") for (int t = 0; t < 6; ++t) fb[d][t] = LDS4(L_WQ + ((t >> 1) * 14 + (s)) * 2048 + (t & 1) * 1024 + lane * 16); }
    QLOAD(0, 0)
    QLOAD(1, 1)
#pragma unroll
    for (int s = 0; s < 14; ++s) {
      if (s < 12) QLOAD(s + 2, (s + 2) % 3)
      if (s == 0) st4s(rws, lane * 16, pdst, pe);
      if (s == 2) st4s(rws, lane * 16, pdst + 1024, po);
      if (s == 4) st4s(rws, (w < 4 || lane < 48) ? lane * 16 : OOB, WS_X1 + (4 * g32 + (w & 3)) * 14336 + c8 * 1792 + (w >> 2) * 1024, SENT);
      if (s == 5) st4s(rws, (w < 4 && lane < 16) ? lane * 16 : OOB, WS_ST + (4 * g32 + (w & 3)) * 2048 + c8 * 256, SENT);
      if (s == 8 && w == 0) dfl = __hip_atomic_load(gr + G_FILL + (lane < 18 ? dep : b), RLX_AGENT);
      SB0();
#pragma unroll
      for (int t = 0; t < 4; ++t) aq[t] = mfma(fb[s % 3][t], hfr[s], aq[t]);
#pragma unroll
      for (int t = 4; t < 6; ++t) aq[t] = mfma(hfr[s], fb[s % 3][t], aq[t]);
      SB0();
    }
  }
  const v4u qf = {pk(aq[0][0], aq[0][1]), pk(aq[0][2], aq[0][3]), g < 3 ? pk(aq[1][0], aq[1][1]) : 0u, g < 3 ? pk(aq[1][2], aq[1][3]) : 0u};
  {
    const v4u kf = nosent4((v4u){pk(aq[2][0], aq[2][1]), pk(aq[2][2], aq[2][3]), pk(aq[3][0], aq[3][1]), pk(aq[3][2], aq[3][3])});
    v2u ve = {pk(aq[4][0], aq[4][1]), pk(aq[4][2], aq[4][3])}, vo_ = {pk(aq[5][0], aq[5][1]), pk(aq[5][2], aq[5][3])};
    if (l15 == 12) vo_ = (v2u){0x3F803F80u, 0x3F803F80u};
    ve.x = nosent(ve.x); ve.y = nosent(ve.y); vo_.x = nosent(vo_.x); vo_.y = nosent(vo_.y);
    {
      DRAIN();
      u32 old = 0;
      if (lane == 0) old = __hip_atomic_fetch_add((LAS u32*)(lds + L_RED + 3080), 1u, __ATOMIC_RELAXED, __HIP_MEMORY_SCOPE_WORKGROUP);
      if (__builtin_amdgcn_readfirstlane(old) == 7u && lane < 8) __hip_atomic_store(gr0 + lane * G_REP + G_PREP + b, tag << 16, RLX_AGENT);
    }
    st4s(rws, koff, kvb, kf);
    st2s(rws, voff, kvb + 8192, ve);
    st2s(rws, voff, kvb + 8192 + 1024, vo_);
    for (unsigned spins = 0;; ++spins) {
      bool ok = (g20 >> 16) == tag && (g21 >> 16) == tag;
      ok &= (dfl >> 16) == tag;
      if (__all(ok) || spins > SPIN_CAP) break;
      BACKOFF();
      if (w == 0) dfl = __hip_atomic_load(gr + G_FILL + (lane < 18 ? dep : b), RLX_AGENT);
      g20 = __hip_atomic_load(gr + i20, RLX_AGENT);
      g21 = __hip_atomic_load(gr + i21, RLX_AGENT);
    }
    RUNPRIO();
    if (tid < 448) {
      LDSH(L_SB + tid * 2) = (u16)g20;
      LDSH(L_A2 + tid * 2) = (u16)pk(1.f + lo2f((u32)g21), 0.f);
    } else {
      LDSF(L_GA + jg * 4) = lo2f((u32)g20);
      LDSF(L_GB + jg * 4) = lo2f((u32)g21);
    }
    __syncthreads();
    LDS4(L_K + 16384 + koff) = kf;
    LDS2(L_V + 16384 + voff) = ve;
    LDS2(L_V + 16384 + 1024 + voff) = vo_;
  }
  v4u w1f[14][2];
  const u32 vo1 = w < 7 ? lane * 16 : OOB;
  const v4u sws = mksrd(ws, WS_END);
  {
    const u32 b0 = g16 >= 2 ? WS_KV + ((g16 - 2) * 16 + head) * 16384 : OOB;
    const u32 b1 = g16 >= 1 ? WS_KV + ((g16 - 1) * 16 + head) * 16384 : OOB;
    v4u k0, v0, k1, v1;
    for (unsigned spins = 0;; ++spins) {
      CBAR();
      k0 = ld4s(rws, tid * 16, b0); v0 = ld4s(rws, tid * 16, b0 + 8192);
      k1 = ld4s(rws, tid * 16, b1); v1 = ld4s(rws, tid * 16, b1 + 8192);
      const bool bad = chk(chk(chk(chk(0u, k0), v0), k1), v1) == ~0u;
      if (!__any(bad) || spins > SPIN_CAP) break;
      {
        const u32 pb = (lane & 16) ? b1 : b0;
        canary(rws, lane < 32 ? pb + ((lane >> 3) & 1) * 8192 + w * 1024 + (lane & 7) * 128 : OOB, 0, spins);
      }
    }
    RUNPRIO();
    LDS4(L_K + tid * 16) = k0; LDS4(L_V + tid * 16) = v0;
    LDS4(L_K + 8192 + tid * 16) = k1; LDS4(L_V + 8192 + tid * 16) = v1;
  }
  if (w == 0) {
    for (unsigned spins = 0;; ++spins) {
      if (__all((__hip_atomic_load(gr + G_PREP + (lane >= 18 ? dep : b), RLX_AGENT) >> 16) == tag) || spins > SPIN_CAP) break;
      BACKOFF();
    }
    RUNPRIO();
  }
  DRAIN();
  __syncthreads();
  {
    const int blk0 = w >> 1;
    f4 sc[18];
    {
      v4u kf[18];
#pragma unroll
      for (int t = 0; t < 6; ++t) kf[t] = LDS4(L_K + (blk0 + (t >> 1)) * 2048 + (t & 1) * 1024 + lane * 16);
#pragma unroll
      for (int t = 0; t < 18; ++t) {
        if (t + 6 < 18) kf[t + 6] = LDS4(L_K + (blk0 + ((t + 6) >> 1)) * 2048 + (t & 1) * 1024 + lane * 16);
        if ((t & 1) && t < 16) dma16<true>(sws, lane * 16, WS_WOT + c8 * 65536 + (w * 8 + (t >> 1)) * 1024, L_WO + (w * 8 + (t >> 1)) * 1024);
        SB0();
        sc[t] = mfma(kf[t], qf, (f4){0, 0, 0, 0});
        SB0();
      }
    }
    v4u vf[18];
#pragma unroll
    for (int t = 0; t < 4; ++t) vf[t] = LDS4(L_V + (blk0 + (t >> 1)) * 2048 + (t & 1) * 1024 + lane * 16);
    const int ql = 256 + 16 * w + l15;
    const int kmin = (256 - 128 * g16) > (ql - 255) ? (256 - 128 * g16) : (ql - 255);
    const int kb0 = 32 * blk0 + 8 * g;
    const float NEG = -1e30f;
    if (g16 < 2) {
#pragma unroll
      for (int t = 2; t < 16; ++t)
#pragma unroll
        for (int r = 0; r < 4; ++r) sc[t][r] = (kb0 + 32 * (t >> 1) + 4 * (t & 1) + r) >= kmin ? sc[t][r] : NEG;
    }
#pragma unroll
    for (int t = 0; t < 2; ++t)
#pragma unroll
      for (int r = 0; r < 4; ++r) sc[t][r] = (kb0 + 4 * t + r) >= kmin ? sc[t][r] : NEG;
#pragma unroll
    for (int t = 16; t < 18; ++t)
#pragma unroll
      for (int r = 0; r < 4; ++r) sc[t][r] = (kb0 + 256 + 4 * (t & 1) + r) <= ql ? sc[t][r] : NEG;
    float mx = NEG;
#pragma unroll
    for (int t = 0; t < 18; ++t) mx = fmaxf(fmaxf(mx, sc[t][0]), fmaxf(sc[t][1], fmaxf(sc[t][2], sc[t][3])));
    mx = fmaxf(mx, __shfl_xor(mx, 16));
    mx = fmaxf(mx, __shfl_xor(mx, 32));
    const float cs = 0.18898223650461362f * 1.4426950408889634f;
    const float mc = mx * cs;
    f4 oe = {0, 0, 0, 0}, oo = {0, 0, 0, 0};
#pragma unroll
    for (int bb = 0; bb < 9; ++bb) {
      if (bb + 2 < 9) {
        vf[2 * bb + 4] = LDS4(L_V + (blk0 + bb + 2) * 2048 + lane * 16);
        vf[2 * bb + 5] = LDS4(L_V + (blk0 + bb + 2) * 2048 + 1024 + lane * 16);
      }
      w1f[bb >> 1][bb & 1] = ld4s(rws, vo1, WS_W1T + (7 * c8 + (w % 7)) * 28672 + bb * 1024);
      if ((bb & 1) == 0 && bb < 8) st4s(rws, vo1, WS_HID + (4 * g32 + (bb >> 1)) * 57344 + c8 * 7168 + (w % 7) * 1024, SENT);
      SB0();
      float p[8];
#pragma unroll
      for (int r = 0; r < 4; ++r) {
        p[r] = __builtin_amdgcn_exp2f(sc[2 * bb][r] * cs - mc);
        p[4 + r] = __builtin_amdgcn_exp2f(sc[2 * bb + 1][r] * cs - mc);
      }
      const v4u pf = {pk(p[0], p[1]), pk(p[2], p[3]), pk(p[4], p[5]), pk(p[6], p[7])};
      oe = mfma(vf[2 * bb], pf, oe);
      oo = mfma(vf[2 * bb + 1], pf, oo);
      SB0();
    }
    const float den = __shfl(oo[0], 48 + l15);
    const float inv = __builtin_amdgcn_rcpf(den);
    const v4u of = nosent4((v4u){pk(oe[0] * inv, oe[1] * inv), pk(oe[2] * inv, oe[3] * inv), pk(oo[0] * inv, oo[1] * inv), pk(oo[2] * inv, oo[3] * inv)});
    st4s(rws, lane * 16, WS_O + ((8 * g16 + w) * 16 + head) * 1024, of);
  }
  SB0();
  asm volatile("s_waitcnt vmcnt(14)" ::: "memory");
#pragma unroll
  for (int i = 9; i < 17; ++i) w1f[i >> 1][i & 1] = ld4s(rws, vo1, WS_W1T + (7 * c8 + (w % 7)) * 28672 + i * 1024);
  __syncthreads();

  {
    v4u of[8];
    const u32 base = WS_O + (4 * g32 + mtB) * 16384 + nhB * 8192;
    v4u wpre[4][4];
#pragma unroll
    for (int h = 0; h < 4; ++h)
#pragma unroll
      for (int t = 0; t < 4; ++t) { wpre[h][t] = LDS4(L_WO + ((t >> 1) * 16 + nhB * 8 + h) * 2048 + (t & 1) * 1024 + lane * 16); PIN(wpre[h][t]); }
    for (unsigned spins = 0;; ++spins) {
      CBAR();
      u32 m = 0;
#pragma unroll
      for (int h = 0; h < 8; ++h) of[h] = ld4s(rws, lane * 16, base + h * 1024);
#pragma unroll
      for (int h = 0; h < 8; ++h) m = chk(m, of[h]);
      if (!__any(m == ~0u) || spins > SPIN_CAP) break;
      canary(rws, lane < 8 ? lane * 1024 : OOB, base, spins);
    }
    RUNPRIO();
    DRAIN();
    f4 acc[4] = {{0, 0, 0, 0}, {0, 0, 0, 0}, {0, 0, 0, 0}, {0, 0, 0, 0}};
#pragma unroll
    for (int h = 0; h < 8; ++h) {
#pragma unroll
      for (int t = 0; t < 4; ++t) acc[t] = mfma(wpre[h & 3][t], of[h], acc[t]);
      SB0();
      if (h < 4) {
#pragma unroll
        for (int t = 0; t < 4; ++t) wpre[h][t] = LDS4(L_WO + ((t >> 1) * 16 + nhB * 8 + h + 4) * 2048 + (t & 1) * 1024 + lane * 16);
      }
      if (h >= 2) {
#pragma unroll
        for (int i = 17 + 2 * (h - 2); i < 19 + 2 * (h - 2) && i < 28; ++i) w1f[i >> 1][i & 1] = ld4s(rws, vo1, WS_W1T + (7 * c8 + (w % 7)) * 28672 + i * 1024);
      }
      SB0();
    }
    LDSF4(L_PR + w * 2048 + lane * 16) = nhB ? acc[0] : acc[2];
    LDSF4(L_PR + w * 2048 + 1024 + lane * 16) = nhB ? acc[1] : acc[3];
    __syncthreads();
    const f4 e = (nhB ? acc[2] : acc[0]) + LDSF4(L_PR + (w ^ 4) * 2048 + lane * 16);
    const f4 o = (nhB ? acc[3] : acc[1]) + LDSF4(L_PR + (w ^ 4) * 2048 + 1024 + lane * 16);
    const f4 g0 = LDSF4(L_GA + (32 * nhB + 8 * g) * 4), g1 = LDSF4(L_GA + (32 * nhB + 8 * g) * 4 + 16);
    const v4u a2 = LDS4(L_A2 + (56 * c8 + 32 * nhB + 8 * g) * 2);
    v4u x1;
    x1.x = pk(lo2f(xres.x) + g0.x * e[0], hi2f(xres.x) + g0.y * o[0]);
    x1.y = pk(lo2f(xres.y) + g0.z * e[1], hi2f(xres.y) + g0.w * o[1]);
    x1.z = pk(lo2f(xres.z) + g1.x * e[2], hi2f(xres.z) + g1.y * o[2]);
    x1.w = pk(lo2f(xres.w) + g1.z * e[3], hi2f(xres.w) + g1.w * o[3]);
    v4u y;
#pragma unroll
    for (int j = 0; j < 4; ++j) y[j] = nosent(pk(lo2f(x1[j]) * lo2f(a2[j]), hi2f(x1[j]) * hi2f(a2[j])));
    const bool vld = !(nhB == 1 && g == 3);
    st4s(rws, vld ? lane * 16 : OOB, WS_X1 + (4 * g32 + mtB) * 14336 + (7 * c8 + 4 * nhB) * 256, y);
    float s1 = 0.f, s2 = 0.f;
#pragma unroll
    for (int j = 0; j < 4; ++j) { s1 = dot2(x1[j], 0x3F803F80u, s1); s2 = dot2(x1[j], x1[j], s2); }
    s1 += __shfl_xor(s1, 16); s2 += __shfl_xor(s2, 16);
    s1 += __shfl_xor(s1, 32); s2 += __shfl_xor(s2, 32);
    st2s(rws, g == 0 ? l15 * 8 : OOB, WS_ST + (4 * g32 + mtB) * 2048 + (2 * c8 + nhB) * 128,
         (v2u){nosent(__builtin_bit_cast(u32, s1)), nosent(__builtin_bit_cast(u32, s2))});
    if (vld) LDS4(L_X1S + (mtB * 7 + 4 * nhB) * 256 + lane * 16) = x1;
  }
  f4 au[2] = {{0, 0, 0, 0}, {0, 0, 0, 0}}, av[2] = {{0, 0, 0, 0}, {0, 0, 0, 0}};
#pragma unroll
  for (int s = 0; s < 14; ++s) {
    const v4u a2 = LDS4(L_A2 + (32 * s + 8 * g) * 2), sb = LDS4(L_SB + (32 * s + 8 * g) * 2);
    au[0] = mfma(w1f[s][0], a2, au[0]); au[1] = mfma(w1f[s][1], a2, au[1]);
    av[0] = mfma(w1f[s][0], sb, av[0]); av[1] = mfma(w1f[s][1], sb, av[1]);
  }
  PIN(au[0]); PIN(au[1]); PIN(av[0]); PIN(av[1]);
  {
    v4u t[7], st[2];
    const u32 base = WS_X1 + 4 * g32 * 14336 + w * 7168;
    const u32 sto = w < 4 ? lane * 16 : OOB;
    for (unsigned spins = 0;; ++spins) {
      CBAR();
      u32 m = 0;
#pragma unroll
      for (int i = 0; i < 7; ++i) t[i] = ld4s(rws, lane * 16, base + i * 1024);
      st[0] = ld4s(rws, sto, WS_ST + (4 * g32 + (w & 3)) * 2048);
      st[1] = ld4s(rws, sto, WS_ST + (4 * g32 + (w & 3)) * 2048 + 1024);
#pragma unroll
      for (int i = 0; i < 7; ++i) m = chk(m, t[i]);
      m = chk(chk(m, st[0]), st[1]);
      if (!__any(m == ~0u) || spins > SPIN_CAP) break;
      canary(rws, lane < 28 ? base + lane * 256 : ((lane >= 32 && lane < 48 && w < 4) ? WS_ST + (4 * g32 + w) * 2048 + (lane - 32) * 128 : OOB), 0, spins);
    }
    RUNPRIO();
#pragma unroll
    for (int i = 0; i < 7; ++i) LDS4(w * 7168 + i * 1024 + lane * 16) = t[i];
    if (w < 4) {
      f4 a = __builtin_bit_cast(f4, st[0]) + __builtin_bit_cast(f4, st[1]);
#pragma unroll
      for (int j = 0; j < 4; ++j) { a[j] += dppf<0x128>(a[j]); a[j] += __shfl_xor(a[j], 16); a[j] += __shfl_xor(a[j], 32); }
      const float m0 = a.x * (1.f / D_), m1 = a.z * (1.f / D_);
      const float r0 = __builtin_amdgcn_rsqf(fmaxf(a.y * (1.f / D_) - m0 * m0, 0.f) + 1e-5f);
      const float r1 = __builtin_amdgcn_rsqf(fmaxf(a.w * (1.f / D_) - m1 * m1, 0.f) + 1e-5f);
      if (lane < 8) LDSF4(L_MR + w * 128 + lane * 16) = (f4){m0, r0, m1, r1};
    }
    __syncthreads();
  }
  if (b == 0 && w == 7) {
    for (unsigned spins = 0;; ++spins) {
      bool ok = true;
#pragma unroll
      for (int i = 0; i < 4; ++i) ok &= (__hip_atomic_load(gr + G_PREP + lane + 64 * i, RLX_AGENT) >> 16) == tag;
      if (__all(ok) || spins > SPIN_CAP) break;
      BACKOFF();
    }
    RUNPRIO();
    if (lane == 0) __hip_atomic_store((gu64*)ws, epoch + 1, RLX_AGENT);
  }
  v4u wf[7][4];
#pragma unroll
  for (int mp = 0; mp < 2; ++mp) {
    f4 acc[2][2] = {{{0, 0, 0, 0}, {0, 0, 0, 0}}, {{0, 0, 0, 0}, {0, 0, 0, 0}}};
    v4u xb[3][2];
#pragma unroll
    for (int m = 0; m < 2; ++m) { xb[0][m] = LDS4((2 * mp + m) * 14336 + lane * 16); xb[1][m] = LDS4((2 * mp + m) * 14336 + 1024 + lane * 16); }
#pragma unroll
    for (int s = 0; s < 14; ++s) {
      if (s < 12) {
#pragma unroll
        for (int m = 0; m < 2; ++m) xb[(s + 2) % 3][m] = LDS4((2 * mp + m) * 14336 + (s + 2) * 1024 + lane * 16);
      }
      SB0();
#pragma unroll
      for (int m = 0; m < 2; ++m) {
        acc[m][0] = mfma(w1f[s][0], xb[s % 3][m], acc[m][0]);
        acc[m][1] = mfma(w1f[s][1], xb[s % 3][m], acc[m][1]);
      }
      SB0();
      wf[(14 * mp + s) >> 2][(14 * mp + s) & 3] = ld4s(rws, lane * 16, WS_W2T + ((c8 * 56 + 7 * w) * 4 + 14 * mp + s) * 1024);
      SB0();
    }
#pragma unroll
    for (int m = 0; m < 2; ++m) {
      const f2 mr2 = *(LAS f2*)(lds + L_MR + ((2 * mp + m) * 16 + l15) * 8);
      const v2u h0 = f1epi(acc[m][0][0], acc[m][1][0], acc[m][0][1], acc[m][1][1], au[0][0], au[1][0], au[0][1], au[1][1],
                           av[0][0], av[1][0], av[0][1], av[1][1], -mr2.x, mr2.y);
      const v2u h1 = f1epi(acc[m][0][2], acc[m][1][2], acc[m][0][3], acc[m][1][3], au[0][2], au[1][2], au[0][3], au[1][3],
                           av[0][2], av[1][2], av[0][3], av[1][3], -mr2.x, mr2.y);
      const v4u hv = {h0.x, h0.y, h1.x, h1.y};
      st4s(rws, w < 7 ? lane * 16 : OOB, WS_HID + (4 * g32 + 2 * mp + m) * 57344 + (28 * c8 + 4 * (w % 7)) * 256, hv);
    }
    SB0();
  }
  __syncthreads();
  {
    v4u hf[2][2][7];
#define HLOAD(mp) { _Pragma("unroll") for (int i = 0; i < 2; ++i) _Pragma("unroll") for (int s = 0; s < 7; ++s) \
      hf[mp][i][s] = ld4s(rws, lane * 16, WS_HID + (4 * g32 + 2 * (mp) + i) * 57344 + w * 7168 + s * 1024); }
    HLOAD(0)
#pragma unroll
    for (int mp = 0; mp < 2; ++mp) {
      for (unsigned spins = 0;; ++spins) {
        u32 m = 0;
#pragma unroll
        for (int i = 0; i < 2; ++i)
#pragma unroll
          for (int s = 0; s < 7; ++s) m = chk(m, hf[mp][i][s]);
        if (!__any(m == ~0u) || spins > SPIN_CAP) break;
        canary(rws, (lane & 7) < 7 && lane < 16 ? (lane >> 3) * 57344 + (lane & 7) * 1024 : OOB, WS_HID + (4 * g32 + 2 * mp) * 57344 + w * 7168, spins);
        CBAR();
        HLOAD(mp)
      }
      RUNPRIO();
      #pragma unroll
      for (int i = 0; i < 2; ++i) {
        f4 acc[4] = {{0, 0, 0, 0}, {0, 0, 0, 0}, {0, 0, 0, 0}, {0, 0, 0, 0}};
#pragma unroll
        for (int s = 0; s < 7; ++s)
#pragma unroll
          for (int t = 0; t < 4; ++t) acc[t] = mfma(wf[s][t], hf[mp][i][s], acc[t]);
#pragma unroll
        for (int r = 0; r < 4; ++r) LDSF4(((w * 4 + 2 * mp + i) * 4 + r) * 1024 + lane * 16) = (f4){acc[0][r], acc[1][r], acc[2][r], acc[3][r]};
        if (mp == 0 && i == 0) { SB0(); HLOAD(1) SB0(); }
      }
    }
  }
  __syncthreads();
  {
    const int mt = tid >> 7, rp = (tid >> 6) & 1;
    if (!(g == 3 && rp == 1)) {
      f4 s0 = {0, 0, 0, 0}, s1 = {0, 0, 0, 0};
#pragma unroll
      for (int ww = 0; ww < 8; ++ww) {
        s0 += LDSF4(((ww * 4 + mt) * 4 + 2 * rp) * 1024 + lane * 16);
        s1 += LDSF4(((ww * 4 + mt) * 4 + 2 * rp + 1) * 1024 + lane * 16);
      }
      const v4u x1 = LDS4(L_X1S + (mt * 7 + 2 * g + rp) * 256 + l15 * 16);
      const f4 g0 = LDSF4(L_GB + (16 * g + 8 * rp) * 4), g1 = LDSF4(L_GB + (16 * g + 8 * rp) * 4 + 16);
      v4u o;
      o.x = pk(lo2f(x1.x) + g0.x * s0.x, hi2f(x1.x) + g0.y * s0.y);
      o.y = pk(lo2f(x1.y) + g0.z * s0.z, hi2f(x1.y) + g0.w * s0.w);
      o.z = pk(lo2f(x1.z) + g1.x * s1.x, hi2f(x1.z) + g1.y * s1.y);
      o.w = pk(lo2f(x1.w) + g1.z * s1.z, hi2f(x1.w) + g1.w * s1.w);
      __builtin_nontemporal_store(o, (v4u*)(out + (size_t)(64 * g32 + 16 * mt + l15) * D_ + 56 * c8 + 16 * g + 8 * rp));
    }
  }
  asm volatile(".Lmega_end:");
}

extern "C" void kernel_launch(void* const* d_in, const int* in_sizes, int n_in, void* d_out, int out_size, void* d_ws,
                              size_t ws_size, hipStream_t stream) {
  (void)hipFuncSetAttribute((const void*)mega, hipFuncAttributeMaxDynamicSharedMemorySize, LDS_BYTES);
  mega<<<256, 512, LDS_BYTES, stream>>>((const u16*)d_in[0], (const u16*)d_in[1], (const u16*)d_in[2], (const u16*)d_in[4],
                                        (const u16*)d_in[5], (const u16*)d_in[6], (const u16*)d_in[7], (u16*)d_out, (char*)d_ws);
}
```

```cpp
#include <hip/hip_runtime.h>
#include <stdint.h>

typedef unsigned int u32;
typedef unsigned long long u64;
typedef unsigned short u16;
typedef u32 v4u __attribute__((ext_vector_type(4)));
typedef u32 v2u __attribute__((ext_vector_type(2)));
typedef float f4 __attribute__((ext_vector_type(4)));
typedef float f2 __attribute__((ext_vector_type(2)));
typedef __bf16 bf2v __attribute__((ext_vector_type(2)));
typedef __bf16 bf8v __attribute__((ext_vector_type(8)));
#define GAS __attribute__((address_space(1)))
#define LAS __attribute__((address_space(3)))
typedef GAS u64 gu64;
#define RLX_AGENT __ATOMIC_RELAXED, __HIP_MEMORY_SCOPE_AGENT
typedef __amdgpu_buffer_rsrc_t rsrc_t;

constexpr int S_ = 2048, D_ = 448, DFF_ = 1792;
constexpr int WS_GRAN = 4096;
constexpr int G_PREP = 2688, G_FILL = 2944;
constexpr int G_REP = 4096;
constexpr int WS_KV = 524288;
constexpr int WS_O = WS_KV + 4194304;
constexpr int WS_X1 = WS_O + 2097152;
constexpr int WS_ST = WS_X1 + 1835008;
constexpr int WS_HID = WS_ST + 262144;
constexpr int WS_W2T = WS_HID + 7340032;
constexpr int WS_W1T = WS_W2T + 1835008;
constexpr int WS_WOT = WS_W1T + 1605632;
constexpr int WS_END = WS_WOT + 524288;
constexpr int WS_DBG = WS_END;
constexpr int L_WQ = 0;
constexpr int L_K = 0, L_V = 24576;
constexpr int L_PR = 57344;
constexpr int L_WO = 86016;
constexpr int L_X = 36864;
constexpr int L_UV = L_WO;
constexpr int L_SM = 151552;
constexpr int L_AS1 = L_SM;
constexpr int L_A2 = L_AS1 + 1792;
constexpr int L_SB = L_A2 + 896;
constexpr int L_GA = L_SB + 896;
constexpr int L_GB = L_GA + 256;
constexpr int L_MR = L_GB + 256;
constexpr int L_RED = L_MR + 512;
constexpr int L_X1S = L_RED;
constexpr int L_END = L_X1S + 7168;
static_assert(L_END <= 163840, "lds");
constexpr int LDS_BYTES = 163840;
constexpr u32 OOB = 0x80000000u;
constexpr unsigned SPIN_CAP = 1u << 15;

__device__ __forceinline__ u32 pk(float lo, float hi) { f2 v = {lo, hi}; return __builtin_bit_cast(u32, __builtin_convertvector(v, bf2v)); }
__device__ __forceinline__ float dot2(u32 a, u32 b, float c) { return __builtin_amdgcn_fdot2_f32_bf16(__builtin_bit_cast(bf2v, a), __builtin_bit_cast(bf2v, b), c, false); }
__device__ __forceinline__ f4 mfma(v4u a, v4u b, f4 c) { return __builtin_amdgcn_mfma_f32_16x16x32_bf16(__builtin_bit_cast(bf8v, a), __builtin_bit_cast(bf8v, b), c, 0, 0, 0); }
__device__ __forceinline__ float lo2f(u32 v) { return __builtin_bit_cast(float, v << 16); }
__device__ __forceinline__ float hi2f(u32 v) { return __builtin_bit_cast(float, v & 0xffff0000u); }
__device__ __forceinline__ u32 plo(u32 r0, u32 r1) { return __builtin_amdgcn_perm(r1, r0, 0x05040100u); }
__device__ __forceinline__ u32 phi(u32 r0, u32 r1) { return __builtin_amdgcn_perm(r1, r0, 0x07060302u); }
__device__ __forceinline__ u32 nosent(u32 v) { return v < 0xFFFFFFFEu ? v : 0xFFFFFFFEu; }
__device__ __forceinline__ v4u nosent4(v4u v) { v.x = nosent(v.x); v.y = nosent(v.y); v.z = nosent(v.z); v.w = nosent(v.w); return v; }
__device__ __forceinline__ u32 umax3(u32 a, u32 b, u32 c) { u32 m = a > b ? a : b; return m > c ? m : c; }
__device__ __forceinline__ u32 chk(u32 m, v4u v) { return umax3(umax3(m, v.x, v.y), v.z, v.w); }
template <int C> __device__ __forceinline__ float dppf(float v) { return __builtin_bit_cast(float, __builtin_amdgcn_update_dpp(0, __builtin_bit_cast(int, v), C, 0xf, 0xf, false)); }
__device__ __forceinline__ float rowsum16(float v) { v += dppf<0xB1>(v); v += dppf<0x4E>(v); v += dppf<0x141>(v); v += dppf<0x140>(v); return v; }
__device__ __forceinline__ v4u ld4(rsrc_t r, u32 vo, u32 so) { return __builtin_amdgcn_raw_buffer_load_b128(r, vo, so, 0); }
__device__ __forceinline__ v4u ld4s(rsrc_t r, u32 vo, u32 so) { return __builtin_amdgcn_raw_buffer_load_b128(r, vo, so, 16); }
__device__ __forceinline__ v2u ld2(rsrc_t r, u32 vo, u32 so) { return __builtin_amdgcn_raw_buffer_load_b64(r, vo, so, 0); }
__device__ __forceinline__ u32 ld1(rsrc_t r, u32 vo, u32 so) { return __builtin_amdgcn_raw_buffer_load_b32(r, vo, so, 0); }
__device__ __forceinline__ void st4s(rsrc_t r, u32 vo, u32 so, v4u v) { __builtin_amdgcn_raw_buffer_store_b128(v, r, vo + so, 0, 16); }
__device__ __forceinline__ void st2s(rsrc_t r, u32 vo, u32 so, v2u v) { __builtin_amdgcn_raw_buffer_store_b64(v, r, vo, so, 16); }
__device__ __forceinline__ v4u modulate(const u32 (&xh)[8], v4u a0, v4u a1) {
  v4u h;
  float d0, d1, d2, d3, d4, d5, d6, d7;
  asm volatile(
      "v_dot2_f32_bf16 %4, %12, %20, 0\n\tv_dot2_f32_bf16 %5, %13, %21, 0\n\tv_dot2_f32_bf16 %6, %14, %22, 0\n\tv_dot2_f32_bf16 %7, %15, %23, 0\n\t"
      "v_dot2_f32_bf16 %8, %16, %24, 0\n\tv_dot2_f32_bf16 %9, %17, %25, 0\n\tv_dot2_f32_bf16 %10, %18, %26, 0\n\tv_dot2_f32_bf16 %11, %19, %27, 0\n\t"
      "v_cvt_pk_bf16_f32 %0, %4, %5\n\tv_cvt_pk_bf16_f32 %1, %6, %7\n\tv_cvt_pk_bf16_f32 %2, %8, %9\n\tv_cvt_pk_bf16_f32 %3, %10, %11\n\ts_nop 1"
      : "=&v"(h.x), "=&v"(h.y), "=&v"(h.z), "=&v"(h.w), "=&v"(d0), "=&v"(d1), "=&v"(d2), "=&v"(d3), "=&v"(d4), "=&v"(d5), "=&v"(d6), "=&v"(d7)
      : "v"(xh[0]), "v"(xh[1]), "v"(xh[2]), "v"(xh[3]), "v"(xh[4]), "v"(xh[5]), "v"(xh[6]), "v"(xh[7]),
        "v"(a0.x), "v"(a0.y), "v"(a0.z), "v"(a0.w), "v"(a1.x), "v"(a1.y), "v"(a1.z), "v"(a1.w));
  return h;
}
typedef short s4v __attribute__((ext_vector_type(4)));
__device__ __forceinline__ v2u trrd(LAS char* p) { return __builtin_bit_cast(v2u, __builtin_amdgcn_ds_read_tr16_b64_v4i16((LAS s4v*)p)); }
__device__ __forceinline__ v4u mksrd(const void* p, u32 bytes) {
  const u64 a = (u64)p;
  return (v4u){(u32)__builtin_amdgcn_readfirstlane((u32)a), (u32)__builtin_amdgcn_readfirstlane((u32)(a >> 32)) & 0xffffu, bytes, 0x00020000u};
}
template <bool SC1> __device__ __forceinline__ void dma16(v4u srd, u32 vo, u32 so, u32 ldsaddr) {
  u32 keep;
  so = __builtin_amdgcn_readfirstlane(so);
  ldsaddr = __builtin_amdgcn_readfirstlane(ldsaddr);
  if (SC1)
    asm volatile("s_mov_b32 %0, m0\n\ts_mov_b32 m0, %4\n\ts_nop 3\n\tbuffer_load_dwordx4 %1, %2, %3 offen sc1 lds\n\ts_mov_b32 m0, %0"
                 : "=&s"(keep) : "v"(vo), "s"(srd), "s"(so), "s"(ldsaddr) : "memory");
  else
    asm volatile("s_mov_b32 %0, m0\n\ts_mov_b32 m0, %4\n\ts_nop 3\n\tbuffer_load_dwordx4 %1, %2, %3 offen lds\n\ts_mov_b32 m0, %0"
                 : "=&s"(keep) : "v"(vo), "s"(srd), "s"(so), "s"(ldsaddr) : "memory");
}
__device__ __forceinline__ void canary(rsrc_t r, u32 vo, u32 so, unsigned& spins) {
  __builtin_amdgcn_s_setprio(0);
  for (; spins <= SPIN_CAP; ++spins) {
    __builtin_amdgcn_s_sleep(2);
    asm volatile("" ::: "memory");
    if (!__any(__builtin_amdgcn_raw_buffer_load_b32(r, vo, so, 16) == ~0u)) break;
  }
}
#define LDS4(off) (*(LAS v4u*)(lds + (off)))
#define LDS2(off) (*(LAS v2u*)(lds + (off)))
#define LDSF4(off) (*(LAS f4*)(lds + (off)))
#define LDSF(off) (*(LAS float*)(lds + (off)))
#define LDSH(off) (*(LAS u16*)(lds + (off)))
#define CBAR() asm volatile("" ::: "memory")
#define PIN(v) asm volatile("" : "+v"(v))
#define SB0() __builtin_amdgcn_sched_barrier(0)
#define BACKOFF() do { __builtin_amdgcn_s_setprio(0); __builtin_amdgcn_s_sleep(6); } while (0)
#define RUNPRIO() __builtin_amdgcn_s_setprio(1)
#define DRAIN() asm volatile("s_waitcnt vmcnt(0)" ::: "memory")
#define STAMPT(i)

__global__ __launch_bounds__(512, 2) void mega(const u16* __restrict__ x, const u16* __restrict__ wqkv, const u16* __restrict__ wo,
                                               const u16* __restrict__ w1, const u16* __restrict__ w2, const u16* __restrict__ cvec,
                                               const u16* __restrict__ wmod, u16* __restrict__ out, char* ws) {
  extern __shared__ __attribute__((aligned(16))) char lds_g[];
  LAS char* lds = (LAS char*)lds_g;
  const int tid = threadIdx.x, lane = tid & 63, w = __builtin_amdgcn_readfirstlane(tid >> 6) & 7;
  const int l15 = lane & 15, g = lane >> 4;
  const int b = blockIdx.x, xcd = b & 7, slot = b >> 3;
  const int g16 = xcd * 2 + (slot >> 4), head = slot & 15;
  const int g32 = xcd * 4 + (slot >> 3), c8 = slot & 7;
  const rsrc_t rws = __builtin_amdgcn_make_buffer_rsrc(ws, 0, WS_END, 0x00020000);
  const rsrc_t rx = __builtin_amdgcn_make_buffer_rsrc((void*)x, 0, S_ * D_ * 2, 0x00020000);
  const rsrc_t rwq = __builtin_amdgcn_make_buffer_rsrc((void*)wqkv, 0, D_ * 3 * D_ * 2, 0x00020000);
  const rsrc_t rwm = __builtin_amdgcn_make_buffer_rsrc((void*)wmod, 0, D_ * 6 * D_ * 2, 0x00020000);
  const rsrc_t rc = __builtin_amdgcn_make_buffer_rsrc((void*)cvec, 0, D_ * 2, 0x00020000);
  const v4u SENT = {~0u, ~0u, ~0u, ~0u};
  STAMPT(0);
  { const u64 t0 = __builtin_amdgcn_s_memrealtime(); if (tid == 0) __hip_atomic_store((gu64*)(ws + WS_END) + b, t0, RLX_AGENT); }
  RUNPRIO();
  u32 cpf, mpf;
  u64 epoch;
  {
    u64 pc;
    u32 len;
    asm volatile("s_getpc_b64 %0\n.Lmega_start:\n\ts_mov_b32 %1, .Lmega_end-.Lmega_start" : "=s"(pc), "=s"(len));
    const v4u scode = mksrd((const void*)pc, len), swm0 = mksrd(wmod, D_ * 6 * D_ * 2), sws0 = mksrd(ws, 64);
    const int idx = slot * 84 + tid - 320, n = idx / 448, row = idx - 448 * n, j = xcd + 8 * n;
    asm volatile("s_nop 4\n\tbuffer_load_dword %0, %3, %5, 0 offen\n\tbuffer_load_dword %1, %4, %6, 0 offen\n\tbuffer_load_dwordx2 %2, off, %7, 0 sc1"
                 : "=&v"(cpf), "=&v"(mpf), "=&v"(epoch)
                 : "v"(tid >= 448 ? (lane * 8 + (slot & 7)) * 128 : OOB), "v"((tid >= 320 && tid < 404 && j < 42) ? (u32)(row * 5376 + j * 128) : OOB),
                   "s"(scode), "s"(swm0), "s"(sws0)
                 : "memory");
  }
  gu64* gr0 = (gu64*)(ws + WS_GRAN);
  gu64* gr = gr0 + xcd * G_REP;
  const int mj = xcd + 8 * (slot / 3);
  const bool modw = (slot % 3 == 0) && mj < 42 && w < 4;
  if (tid < 4) *(volatile LAS u32*)(lds + L_RED + 3072 + tid * 4) = 0u;
  v2u mr[4][8];
  u32 craw;
  if (modw) {
    const v4u swm = mksrd(wmod, D_ * 6 * D_ * 2), sc_ = mksrd(cvec, D_ * 2);
    asm volatile("s_nop 4\n\tbuffer_load_dword %0, %1, %2, 0 offen" : "=&v"(craw) : "v"(w + 4 * g < 14 ? (32 * (w + 4 * g) + 2 * l15) * 2 : OOB), "s"(sc_) : "memory");
#pragma unroll
    for (int i = 0; i < 4; ++i) {
      const u32 vo = (i < 3 || w < 2) ? (u32)(8 * g * 2688 + 4 * l15) * 2 : OOB;
      const u32 so = (u32)((32 * ((w + 4 * i) % 14)) * 2688 + 64 * mj) * 2;
      asm volatile(
          "s_nop 4\n\t"
          "buffer_load_dwordx2 %0, %8, %9, %10 offen\n\tbuffer_load_dwordx2 %1, %8, %9, %11 offen\n\t"
          "buffer_load_dwordx2 %2, %8, %9, %12 offen\n\tbuffer_load_dwordx2 %3, %8, %9, %13 offen\n\t"
          "buffer_load_dwordx2 %4, %8, %9, %14 offen\n\tbuffer_load_dwordx2 %5, %8, %9, %15 offen\n\t"
          "buffer_load_dwordx2 %6, %8, %9, %16 offen\n\tbuffer_load_dwordx2 %7, %8, %9, %17 offen"
          : "=&v"(mr[i][0]), "=&v"(mr[i][1]), "=&v"(mr[i][2]), "=&v"(mr[i][3]), "=&v"(mr[i][4]), "=&v"(mr[i][5]), "=&v"(mr[i][6]), "=&v"(mr[i][7])
          : "v"(vo), "s"(swm), "s"(so), "s"(so + 5376), "s"(so + 2 * 5376), "s"(so + 3 * 5376), "s"(so + 4 * 5376), "s"(so + 5 * 5376),
            "s"(so + 6 * 5376), "s"(so + 7 * 5376)
          : "memory");
    }
  }
  asm volatile("s_waitcnt lgkmcnt(0)\n\ts_barrier" ::: "memory");
  STAMPT(14);
  const v4u sq_srd = mksrd(wqkv, D_ * 3 * D_ * 2);
  const u32 vq = (u32)((lane >> 2) * 2688 + (((lane & 3) ^ (2 * (lane >> 5))) * 16));
#define WQDMA(i)                                                                                                        \
  {                                                                                                                     \
    const int u = w + 8 * ((i) >> 1), d = 2 * u + ((i) & 1), mat = u / 14, kb = d - 28 * mat;                           \
    dma16<false>(sq_srd, u < 42 ? vq : OOB, (u32)(16 * kb * 2688 + (mat % 3) * 896 + 56 * head), u < 42 ? L_WQ + d * 1024 : L_RED + 4096); \
  }
  {
    const v4u sx = mksrd(x, S_ * D_ * 2);
    const u32 vo = (u32)((lane >> 3) * D_ * 2 + (((lane & 7) ^ (lane >> 3)) * 16));
#pragma unroll
    for (int q = 0; q < 14; ++q) dma16<false>(sx, vo, (u32)((128 * g16 + 16 * w + 8 * (q & 1)) * D_ * 2 + (q >> 1) * 128), L_X + w * 14336 + q * 1024);
  }
  asm volatile("s_barrier" ::: "memory");
  WQDMA(0) WQDMA(1) WQDMA(2) WQDMA(3)
  u32 pr[8];
  u32 pdst;
  {
    const int u = b * 8 + w;
    const u16* base; u32 bytes, ld, so, vo, vo2;
    if (u < 896) {
      const int cc = u / 112, v = u - 112 * cc, kb = v >> 1, hf = v & 1;
      base = w2; bytes = DFF_ * D_ * 2; ld = D_ * 2; so = (u32)(32 * kb * D_ + 56 * cc + 2 * hf) * 2;
      vo = l15 < 14 ? (u32)(8 * g * D_ + 4 * l15) * 2 : OOB; vo2 = vo;
      pdst = WS_W2T + ((cc * 56 + kb) * 4 + 2 * hf) * 1024;
    } else if (u < 1680) {
      const int v = u - 896, cb = v / 14, s = v - 14 * cb;
      base = w1; bytes = DFF_ * D_ * 2; ld = DFF_ * 2; so = (u32)(32 * s * DFF_ + 32 * cb) * 2;
      vo = (u32)(8 * g * DFF_ + 2 * l15) * 2; vo2 = vo;
      pdst = WS_W1T + (cb * 14 + s) * 2048;
    } else {
      const int v = u - 1680, hh = v & 15, nh = (v >> 4) & 1, cc = (v >> 5) & 7;
      base = wo; bytes = D_ * D_ * 2; ld = D_ * 2; so = (u32)(28 * hh * D_ + 56 * cc + 32 * nh) * 2;
      vo = (u < 1936 && (nh == 0 || l15 < 12)) ? (u32)(4 * g * D_ + 2 * l15) * 2 : OOB; vo2 = g < 3 ? vo + 12 * D_ * 2 : OOB;
      pdst = u < 1936 ? WS_WOT + (cc * 32 + nh * 16 + hh) * 2048 : OOB;
    }
    const v4u sp = mksrd(base, __builtin_amdgcn_readfirstlane(bytes));
    so = __builtin_amdgcn_readfirstlane(so); ld = __builtin_amdgcn_readfirstlane(ld);
    asm volatile(
        "s_nop 4\n\t"
        "buffer_load_dword %0, %8, %10, %11 offen\n\tbuffer_load_dword %1, %8, %10, %12 offen\n\t"
        "buffer_load_dword %2, %8, %10, %13 offen\n\tbuffer_load_dword %3, %8, %10, %14 offen\n\t"
        "buffer_load_dword %4, %9, %10, %15 offen\n\tbuffer_load_dword %5, %9, %10, %16 offen\n\t"
        "buffer_load_dword %6, %9, %10, %17 offen\n\tbuffer_load_dword %7, %9, %10, %18 offen"
        : "=&v"(pr[0]), "=&v"(pr[1]), "=&v"(pr[2]), "=&v"(pr[3]), "=&v"(pr[4]), "=&v"(pr[5]), "=&v"(pr[6]), "=&v"(pr[7])
        : "v"(vo), "v"(vo2), "s"(sp), "s"(so), "s"(so + ld), "s"(so + 2 * ld), "s"(so + 3 * ld), "s"(so + 4 * ld), "s"(so + 5 * ld), "s"(so + 6 * ld),
          "s"(so + 7 * ld)
        : "memory");
  }
  SB0();
  asm volatile("s_waitcnt vmcnt(26)" : "+v"(epoch), "+v"(cpf), "+v"(mpf)::"memory");
  const u64 tag = (epoch + 1) & 0xFFFFFFFFFFFFull;
  if (modw) {
    __builtin_amdgcn_s_setprio(3);
    asm volatile(""
                 : "+v"(craw), "+v"(mr[0][0]), "+v"(mr[0][1]), "+v"(mr[0][2]), "+v"(mr[0][3]), "+v"(mr[0][4]), "+v"(mr[0][5]), "+v"(mr[0][6]), "+v"(mr[0][7]),
                   "+v"(mr[1][0]), "+v"(mr[1][1]), "+v"(mr[1][2]), "+v"(mr[1][3]), "+v"(mr[1][4]), "+v"(mr[1][5]), "+v"(mr[1][6]), "+v"(mr[1][7])::"memory");
    asm volatile(""
                 : "+v"(mr[2][0]), "+v"(mr[2][1]), "+v"(mr[2][2]), "+v"(mr[2][3]), "+v"(mr[2][4]), "+v"(mr[2][5]), "+v"(mr[2][6]), "+v"(mr[2][7]),
                   "+v"(mr[3][0]), "+v"(mr[3][1]), "+v"(mr[3][2]), "+v"(mr[3][3]), "+v"(mr[3][4]), "+v"(mr[3][5]), "+v"(mr[3][6]), "+v"(mr[3][7])::"memory");
    float c0 = lo2f(craw), c1 = hi2f(craw);
    c0 = c0 * __builtin_amdgcn_rcpf(1.f + __expf(-c0));
    c1 = c1 * __builtin_amdgcn_rcpf(1.f + __expf(-c1));
    *(LAS u32*)(lds + L_RED + w * 256 + lane * 4) = pk(c0, c1);
    f4 acc[4] = {{0, 0, 0, 0}, {0, 0, 0, 0}, {0, 0, 0, 0}, {0, 0, 0, 0}};
#pragma unroll
    for (int i = 0; i < 4; ++i) {
      const v4u sf = LDS4(L_RED + w * 256 + i * 64 + g * 16);
      acc[0] = mfma((v4u){plo(mr[i][0].x, mr[i][1].x), plo(mr[i][2].x, mr[i][3].x), plo(mr[i][4].x, mr[i][5].x), plo(mr[i][6].x, mr[i][7].x)}, sf, acc[0]);
      acc[1] = mfma((v4u){phi(mr[i][0].x, mr[i][1].x), phi(mr[i][2].x, mr[i][3].x), phi(mr[i][4].x, mr[i][5].x), phi(mr[i][6].x, mr[i][7].x)}, sf, acc[1]);
      acc[2] = mfma((v4u){plo(mr[i][0].y, mr[i][1].y), plo(mr[i][2].y, mr[i][3].y), plo(mr[i][4].y, mr[i][5].y), plo(mr[i][6].y, mr[i][7].y)}, sf, acc[2]);
      acc[3] = mfma((v4u){phi(mr[i][0].y, mr[i][1].y), phi(mr[i][2].y, mr[i][3].y), phi(mr[i][4].y, mr[i][5].y), phi(mr[i][6].y, mr[i][7].y)}, sf, acc[3]);
    }
    if (l15 == 0) {
#pragma unroll
      for (int r = 0; r < 4; ++r) LDSF4(L_RED + 1024 + w * 256 + (16 * g + 4 * r) * 4) = (f4){acc[0][r], acc[1][r], acc[2][r], acc[3][r]};
    }
    asm volatile("s_waitcnt lgkmcnt(0)" ::: "memory");
    u32 old = 0;
    if (lane == 0) old = __hip_atomic_fetch_add((LAS u32*)(lds + L_RED + 3072), 1u, __ATOMIC_RELAXED, __HIP_MEMORY_SCOPE_WORKGROUP);
    if (__builtin_amdgcn_readfirstlane(old) == 3u) {
      CBAR();
      const float sum = (LDSF(L_RED + 1024 + lane * 4) + LDSF(L_RED + 1280 + lane * 4)) + (LDSF(L_RED + 1536 + lane * 4) + LDSF(L_RED + 1792 + lane * 4));
      const u64 gval = (tag << 16) | (u64)(pk(sum, 0.f) & 0xffffu);
#pragma unroll
      for (int r = 0; r < 8; ++r) __hip_atomic_store(gr0 + r * G_REP + 64 * mj + lane, gval, RLX_AGENT);
    }
    RUNPRIO();
  }
  STAMPT(15);
  if (modw) asm volatile("s_waitcnt vmcnt(8)" ::: "memory"); else asm volatile("s_waitcnt vmcnt(12)" ::: "memory");
  STAMPT(16);
  v4u xr[14];
  {
    const u32 ra0 = L_X + w * 14336 + (l15 >> 3) * 1024 + (l15 & 7) * 128 + ((g ^ (l15 & 7)) * 16);
    const u32 ra1 = L_X + w * 14336 + (l15 >> 3) * 1024 + (l15 & 7) * 128 + (((4 + g) ^ (l15 & 7)) * 16);
#pragma unroll
    for (int s = 0; s < 14; ++s) xr[s] = LDS4(((s & 1) ? ra1 : ra0) + (s >> 1) * 2048);
#pragma unroll
    for (int s = 0; s < 14; ++s) PIN(xr[s]);
  }
  asm volatile("s_waitcnt lgkmcnt(0)\n\ts_barrier" ::: "memory");
  STAMPT(17);
  const int mtB = w & 3, nhB = w >> 2;
  v4u xres;
  const int tau = 16 * (w & 1) + l15;
  const u32 koff = (w >> 1) * 2048 + ((tau >> 2) & 1) * 1024 + (g * 16 + 4 * (tau >> 3) + (tau & 3)) * 16;
  const u32 voff = (w >> 1) * 2048 + ((2 * (w & 1) + (g >> 1)) * 16 + l15) * 16 + (g & 1) * 8;
  const u32 kvb = g16 < 15 ? WS_KV + (g16 * 16 + head) * 16384 : OOB;
  u32 xh[14][8];
  {
    const v4u ONES = {0x3F803F80u, 0x3F803F80u, 0x3F803F80u, 0x3F803F80u};
    f4 sm = {0, 0, 0, 0}, gm = {0, 0, 0, 0};
#pragma unroll
    for (int s = 0; s < 14; ++s) {
      if (s == 0 || s == 7) WQDMA(4 + s / 7)
      sm = mfma(ONES, xr[s], sm);
      gm = mfma(xr[s], xr[s], gm);
    }
    float sq = (l15 & 2) ? ((l15 & 1) ? gm[3] : gm[2]) : ((l15 & 1) ? gm[1] : gm[0]);
    sq = __shfl(sq, 16 * (l15 >> 2) + l15);
    const float mean = sm[0] * (1.f / D_);
    const float rstd = __builtin_amdgcn_rsqf(fmaxf(sq * (1.f / D_) - mean * mean, 0.f) + 1e-5f);
    const f2 rs2 = {rstd, rstd}, nm2 = {-mean * rstd, -mean * rstd};
#pragma unroll
    for (int s = 0; s < 14; ++s) {
      if (s < 6) WQDMA(6 + s)
      else if (s == 7) xres = ld4(rx, (nhB == 1 && g == 3) ? OOB : (u32)(l15 * D_ + 8 * g) * 2, (u32)((64 * g32 + 16 * mtB) * D_ + 56 * c8 + 32 * nhB) * 2);
      else if (s == 9) st4s(rws, koff, kvb, SENT);
      else if (s == 11) st4s(rws, (w >> 1) * 2048 + (w & 1) * 512 + (lane & 31) * 16 + (lane >> 5) * 1024, kvb + 8192, SENT);
      else if (s == 13) st4s(rws, lane * 16, WS_O + ((8 * g16 + w) * 16 + head) * 1024, SENT);
#pragma unroll
      for (int j = 0; j < 4; ++j) {
        const f2 v = __builtin_elementwise_fma((f2){lo2f(xr[s][j]), hi2f(xr[s][j])}, rs2, nm2);
        xh[s][2 * j] = pk(v.x, 1.f);
        xh[s][2 * j + 1] = pk(v.y, 1.f);
        PIN(xh[s][2 * j]); PIN(xh[s][2 * j + 1]);
      }
      SB0();
    }
  }
  STAMPT(20);
  const int i1 = tid < 384 ? 512 + tid : tid;
  u64 g10 = __hip_atomic_load(gr + tid, RLX_AGENT), g11 = __hip_atomic_load(gr + i1, RLX_AGENT);
  asm volatile("s_waitcnt vmcnt(6)"
               : "+v"(pr[0]), "+v"(pr[1]), "+v"(pr[2]), "+v"(pr[3]), "+v"(pr[4]), "+v"(pr[5]), "+v"(pr[6]), "+v"(pr[7])::"memory");
  {
    LAS char* tb0 = lds + L_WQ + (8 * g + (l15 >> 2)) * 64 + (g & 1) * 32 + (l15 & 3) * 8;
    LAS char* tb1 = lds + L_WQ + (8 * g + (l15 >> 2)) * 64 + ((g & 1) ^ 1) * 32 + (l15 & 3) * 8;
    v2u r[6][4];
#pragma unroll
    for (int i = 0; i < 6; ++i) {
      const int u = (i < 5 || w < 2) ? w + 8 * i : w;
      r[i][0] = trrd(tb0 + u * 2048); r[i][1] = trrd(tb0 + u * 2048 + 256);
      r[i][2] = trrd(tb1 + u * 2048); r[i][3] = trrd(tb1 + u * 2048 + 256);
    }
    SB0();
#pragma unroll
    for (int i = 0; i < 6; ++i) {
      const int u = (i < 5 || w < 2) ? w + 8 * i : w;
      LDS4(L_WQ + u * 2048 + lane * 16) = (v4u){r[i][0].x, r[i][0].y, r[i][1].x, r[i][1].y};
      LDS4(L_WQ + u * 2048 + 1024 + lane * 16) = (v4u){r[i][2].x, r[i][2].y, r[i][3].x, r[i][3].y};
    }
  }
  v4u pe = {plo(pr[0], pr[1]), plo(pr[2], pr[3]), plo(pr[4], pr[5]), plo(pr[6], pr[7])};
  v4u po = {phi(pr[0], pr[1]), phi(pr[2], pr[3]), phi(pr[4], pr[5]), phi(pr[6], pr[7])};
  PIN(pe); PIN(po);
  STAMPT(2);
  {
    for (unsigned spins = 0;; ++spins) {
      const bool ok = (g10 >> 16) == tag && (g11 >> 16) == tag;
      if (__all(ok) || spins > SPIN_CAP) break;
      BACKOFF();
      g10 = __hip_atomic_load(gr + tid, RLX_AGENT);
      g11 = __hip_atomic_load(gr + i1, RLX_AGENT);
    }
    RUNPRIO();
    if (tid < 448) LDSH(L_AS1 + tid * 4 + 2) = (u16)g10;
    else LDSH(L_AS1 + (tid - 448) * 4) = (u16)pk(1.f + lo2f((u32)g10), 0.f);
    if (tid < 384) LDSH(L_AS1 + (64 + tid) * 4) = (u16)pk(1.f + lo2f((u32)g11), 0.f);
  }
  DRAIN();
  __syncthreads();
  if (tid < 8) __hip_atomic_store(gr0 + tid * G_REP + G_FILL + b, tag << 16, RLX_AGENT);
  STAMPT(3);
  const int jg = tid - 448;
  const int i20 = tid < 448 ? 1344 + tid : 896 + 56 * c8 + (jg < 56 ? jg : 0);
  const int i21 = tid < 448 ? 1792 + tid : 2240 + 56 * c8 + (jg < 56 ? jg : 0);
  u64 g20 = __hip_atomic_load(gr + i20, RLX_AGENT), g21 = __hip_atomic_load(gr + i21, RLX_AGENT);

  v4u hfr[14];
  {
    v4u as[3][2];
#pragma unroll
    for (int s = 0; s < 2; ++s) { as[s][0] = LDS4(L_AS1 + (32 * s + 8 * g) * 4); as[s][1] = LDS4(L_AS1 + (32 * s + 8 * g) * 4 + 16); }
#pragma unroll
    for (int s = 0; s < 14; ++s) {
      if (s < 12) { as[(s + 2) % 3][0] = LDS4(L_AS1 + (32 * (s + 2) + 8 * g) * 4); as[(s + 2) % 3][1] = LDS4(L_AS1 + (32 * (s + 2) + 8 * g) * 4 + 16); }
      SB0();
      hfr[s] = modulate(xh[s], as[s % 3][0], as[s % 3][1]);
      SB0();
    }
  }
  STAMPT(29);
  int dep = b;
  if (lane < 16) dep = xcd + 8 * (16 * (g16 & 1) + lane);
  else if (lane == 16) dep = g16 >= 1 ? ((g16 - 1) >> 1) + 8 * (16 * ((g16 - 1) & 1) + head) : b;
  else if (lane == 17) dep = g16 >= 2 ? ((g16 - 2) >> 1) + 8 * (16 * (g16 & 1) + head) : b;
  else if (lane < 32) dep = 14 * c8 + (lane - 18);
  else if (lane < 46) dep = ((896 + 98 * c8) >> 3) + (lane - 32);
  else if (lane < 50) dep = 210 + 4 * c8 + (lane - 46);
  u64 dfl = tag << 16;
  f4 aq[6];
#pragma unroll
  for (int t = 0; t < 6; ++t) aq[t] = (f4){0, 0, 0, 0};
  {
    v4u fb[3][6];
#define QLOAD(s, d) { _Pragma("unroll") for (int t = 0; t < 6; ++t) fb[d][t] = LDS4(L_WQ + ((t >> 1) * 14 + (s)) * 2048 + (t & 1) * 1024 + lane * 16); }
    QLOAD(0, 0)
    QLOAD(1, 1)
#pragma unroll
    for (int s = 0; s < 14; ++s) {
      if (s < 12) QLOAD(s + 2, (s + 2) % 3)
      if (s == 0) st4s(rws, lane * 16, pdst, pe);
      if (s == 2) st4s(rws, lane * 16, pdst + 1024, po);
      if (s == 4) st4s(rws, (w < 4 || lane < 48) ? lane * 16 : OOB, WS_X1 + (4 * g32 + (w & 3)) * 14336 + c8 * 1792 + (w >> 2) * 1024, SENT);
      if (s == 5) st4s(rws, (w < 4 && lane < 16) ? lane * 16 : OOB, WS_ST + (4 * g32 + (w & 3)) * 2048 + c8 * 256, SENT);
      if (s == 8 && w == 0) dfl = __hip_atomic_load(gr + G_FILL + (lane < 18 ? dep : b), RLX_AGENT);
      SB0();
#pragma unroll
      for (int t = 0; t < 4; ++t) aq[t] = mfma(fb[s % 3][t], hfr[s], aq[t]);
#pragma unroll
      for (int t = 4; t < 6; ++t) aq[t] = mfma(hfr[s], fb[s % 3][t], aq[t]);
      SB0();
    }
  }
  STAMPT(30);
  const v4u qf = {pk(aq[0][0], aq[0][1]), pk(aq[0][2], aq[0][3]), g < 3 ? pk(aq[1][0], aq[1][1]) : 0u, g < 3 ? pk(aq[1][2], aq[1][3]) : 0u};
  {
    const v4u kf = nosent4((v4u){pk(aq[2][0], aq[2][1]), pk(aq[2][2], aq[2][3]), pk(aq[3][0], aq[3][1]), pk(aq[3][2], aq[3][3])});
    v2u ve = {pk(aq[4][0], aq[4][1]), pk(aq[4][2], aq[4][3])}, vo_ = {pk(aq[5][0], aq[5][1]), pk(aq[5][2], aq[5][3])};
    if (l15 == 12) vo_ = (v2u){0x3F803F80u, 0x3F803F80u};
    ve.x = nosent(ve.x); ve.y = nosent(ve.y); vo_.x = nosent(vo_.x); vo_.y = nosent(vo_.y);
    {
      DRAIN();
      u32 old = 0;
      if (lane == 0) old = __hip_atomic_fetch_add((LAS u32*)(lds + L_RED + 3080), 1u, __ATOMIC_RELAXED, __HIP_MEMORY_SCOPE_WORKGROUP);
      if (__builtin_amdgcn_readfirstlane(old) == 7u && lane < 8) __hip_atomic_store(gr0 + lane * G_REP + G_PREP + b, tag << 16, RLX_AGENT);
    }
    st4s(rws, koff, kvb, kf);
    st2s(rws, voff, kvb + 8192, ve);
    st2s(rws, voff, kvb + 8192 + 1024, vo_);
    STAMPT(31);
    for (unsigned spins = 0;; ++spins) {
      bool ok = (g20 >> 16) == tag && (g21 >> 16) == tag;
      ok &= (dfl >> 16) == tag;
      if (__all(ok) || spins > SPIN_CAP) break;
      BACKOFF();
      if (w == 0) dfl = __hip_atomic_load(gr + G_FILL + (lane < 18 ? dep : b), RLX_AGENT);
      g20 = __hip_atomic_load(gr + i20, RLX_AGENT);
      g21 = __hip_atomic_load(gr + i21, RLX_AGENT);
    }
    RUNPRIO();
    if (tid < 448) {
      LDSH(L_SB + tid * 2) = (u16)g20;
      LDSH(L_A2 + tid * 2) = (u16)pk(1.f + lo2f((u32)g21), 0.f);
    } else {
      LDSF(L_GA + jg * 4) = lo2f((u32)g20);
      LDSF(L_GB + jg * 4) = lo2f((u32)g21);
    }
    STAMPT(21);
    __syncthreads();
    STAMPT(4);
    LDS4(L_K + 16384 + koff) = kf;
    LDS2(L_V + 16384 + voff) = ve;
    LDS2(L_V + 16384 + 1024 + voff) = vo_;
  }
  STAMPT(22);
  v4u w1f[14][2];
  const u32 vo1 = w < 7 ? lane * 16 : OOB;
  const v4u sws = mksrd(ws, WS_END);
  {
    const u32 b0 = g16 >= 2 ? WS_KV + ((g16 - 2) * 16 + head) * 16384 : OOB;
    const u32 b1 = g16 >= 1 ? WS_KV + ((g16 - 1) * 16 + head) * 16384 : OOB;
    v4u k0, v0, k1, v1;
    for (unsigned spins = 0;; ++spins) {
      CBAR();
      k0 = ld4s(rws, tid * 16, b0); v0 = ld4s(rws, tid * 16, b0 + 8192);
      k1 = ld4s(rws, tid * 16, b1); v1 = ld4s(rws, tid * 16, b1 + 8192);
      const bool bad = chk(chk(chk(chk(0u, k0), v0), k1), v1) == ~0u;
      if (!__any(bad) || spins > SPIN_CAP) break;
      {
        const u32 pb = (lane & 16) ? b1 : b0;
        canary(rws, lane < 32 ? pb + ((lane >> 3) & 1) * 8192 + w * 1024 + (lane & 7) * 128 : OOB, 0, spins);
      }
    }
    RUNPRIO();
    STAMPT(24);
    LDS4(L_K + tid * 16) = k0; LDS4(L_V + tid * 16) = v0;
    LDS4(L_K + 8192 + tid * 16) = k1; LDS4(L_V + 8192 + tid * 16) = v1;
  }
  if (w == 0) {
    for (unsigned spins = 0;; ++spins) {
      if (__all((__hip_atomic_load(gr + G_PREP + (lane >= 18 ? dep : b), RLX_AGENT) >> 16) == tag) || spins > SPIN_CAP) break;
      BACKOFF();
    }
    RUNPRIO();
  }
  DRAIN();
  __syncthreads();
  STAMPT(5);
  {
    const int blk0 = w >> 1;
    f4 sc[18];
    {
      v4u kf[18];
#pragma unroll
      for (int t = 0; t < 6; ++t) kf[t] = LDS4(L_K + (blk0 + (t >> 1)) * 2048 + (t & 1) * 1024 + lane * 16);
#pragma unroll
      for (int t = 0; t < 18; ++t) {
        if (t + 6 < 18) kf[t + 6] = LDS4(L_K + (blk0 + ((t + 6) >> 1)) * 2048 + (t & 1) * 1024 + lane * 16);
        if ((t & 1) && t < 16) dma16<true>(sws, lane * 16, WS_WOT + c8 * 65536 + (w * 8 + (t >> 1)) * 1024, L_WO + (w * 8 + (t >> 1)) * 1024);
        SB0();
        sc[t] = mfma(kf[t], qf, (f4){0, 0, 0, 0});
        SB0();
      }
    }
    STAMPT(25);
    v4u vf[18];
#pragma unroll
    for (int t = 0; t < 4; ++t) vf[t] = LDS4(L_V + (blk0 + (t >> 1)) * 2048 + (t & 1) * 1024 + lane * 16);
    const int ql = 256 + 16 * w + l15;
    const int kmin = (256 - 128 * g16) > (ql - 255) ? (256 - 128 * g16) : (ql - 255);
    const int kb0 = 32 * blk0 + 8 * g;
    const float NEG = -1e30f;
    if (g16 < 2) {
#pragma unroll
      for (int t = 2; t < 16; ++t)
#pragma unroll
        for (int r = 0; r < 4; ++r) sc[t][r] = (kb0 + 32 * (t >> 1) + 4 * (t & 1) + r) >= kmin ? sc[t][r] : NEG;
    }
#pragma unroll
    for (int t = 0; t < 2; ++t)
#pragma unroll
      for (int r = 0; r < 4; ++r) sc[t][r] = (kb0 + 4 * t + r) >= kmin ? sc[t][r] : NEG;
#pragma unroll
    for (int t = 16; t < 18; ++t)
#pragma unroll
      for (int r = 0; r < 4; ++r) sc[t][r] = (kb0 + 256 + 4 * (t & 1) + r) <= ql ? sc[t][r] : NEG;
    float mx = NEG;
#pragma unroll
    for (int t = 0; t < 18; ++t) mx = fmaxf(fmaxf(mx, sc[t][0]), fmaxf(sc[t][1], fmaxf(sc[t][2], sc[t][3])));
    mx = fmaxf(mx, __shfl_xor(mx, 16));
    mx = fmaxf(mx, __shfl_xor(mx, 32));
    STAMPT(26);
    STAMPT(18);
    const float cs = 0.18898223650461362f * 1.4426950408889634f;
    const float mc = mx * cs;
    f4 oe = {0, 0, 0, 0}, oo = {0, 0, 0, 0};
#pragma unroll
    for (int bb = 0; bb < 9; ++bb) {
      if (bb + 2 < 9) {
        vf[2 * bb + 4] = LDS4(L_V + (blk0 + bb + 2) * 2048 + lane * 16);
        vf[2 * bb + 5] = LDS4(L_V + (blk0 + bb + 2) * 2048 + 1024 + lane * 16);
      }
      w1f[bb >> 1][bb & 1] = ld4s(rws, vo1, WS_W1T + (7 * c8 + (w % 7)) * 28672 + bb * 1024);
      if ((bb & 1) == 0 && bb < 8) st4s(rws, vo1, WS_HID + (4 * g32 + (bb >> 1)) * 57344 + c8 * 7168 + (w % 7) * 1024, SENT);
      SB0();
      float p[8];
#pragma unroll
      for (int r = 0; r < 4; ++r) {
        p[r] = __builtin_amdgcn_exp2f(sc[2 * bb][r] * cs - mc);
        p[4 + r] = __builtin_amdgcn_exp2f(sc[2 * bb + 1][r] * cs - mc);
      }
      const v4u pf = {pk(p[0], p[1]), pk(p[2], p[3]), pk(p[4], p[5]), pk(p[6], p[7])};
      oe = mfma(vf[2 * bb], pf, oe);
      oo = mfma(vf[2 * bb + 1], pf, oo);
      SB0();
    }
    const float den = __shfl(oo[0], 48 + l15);
    const float inv = __builtin_amdgcn_rcpf(den);
    const v4u of = nosent4((v4u){pk(oe[0] * inv, oe[1] * inv), pk(oe[2] * inv, oe[3] * inv), pk(oo[0] * inv, oo[1] * inv), pk(oo[2] * inv, oo[3] * inv)});
    st4s(rws, lane * 16, WS_O + ((8 * g16 + w) * 16 + head) * 1024, of);
  }
  STAMPT(6);
  SB0();
  asm volatile("s_waitcnt vmcnt(14)" ::: "memory");
#pragma unroll
  for (int i = 9; i < 17; ++i) w1f[i >> 1][i & 1] = ld4s(rws, vo1, WS_W1T + (7 * c8 + (w % 7)) * 28672 + i * 1024);
  STAMPT(19);
  STAMPT(27);
  __syncthreads();
  STAMPT(28);

  {
    v4u of[8];
    const u32 base = WS_O + (4 * g32 + mtB) * 16384 + nhB * 8192;
    v4u wpre[4][4];
#pragma unroll
    for (int h = 0; h < 4; ++h)
#pragma unroll
      for (int t = 0; t < 4; ++t) { wpre[h][t] = LDS4(L_WO + ((t >> 1) * 16 + nhB * 8 + h) * 2048 + (t & 1) * 1024 + lane * 16); PIN(wpre[h][t]); }
    for (unsigned spins = 0;; ++spins) {
      CBAR();
      u32 m = 0;
#pragma unroll
      for (int h = 0; h < 8; ++h) of[h] = ld4s(rws, lane * 16, base + h * 1024);
#pragma unroll
      for (int h = 0; h < 8; ++h) m = chk(m, of[h]);
      if (!__any(m == ~0u) || spins > SPIN_CAP) break;
      canary(rws, lane < 8 ? lane * 1024 : OOB, base, spins);
    }
    RUNPRIO();
    DRAIN();
    STAMPT(7);
    f4 acc[4] = {{0, 0, 0, 0}, {0, 0, 0, 0}, {0, 0, 0, 0}, {0, 0, 0, 0}};
#pragma unroll
    for (int h = 0; h < 8; ++h) {
#pragma unroll
      for (int t = 0; t < 4; ++t) acc[t] = mfma(wpre[h & 3][t], of[h], acc[t]);
      SB0();
      if (h < 4) {
#pragma unroll
        for (int t = 0; t < 4; ++t) wpre[h][t] = LDS4(L_WO + ((t >> 1) * 16 + nhB * 8 + h + 4) * 2048 + (t & 1) * 1024 + lane * 16);
      }
      if (h >= 2) {
#pragma unroll
        for (int i = 17 + 2 * (h - 2); i < 19 + 2 * (h - 2) && i < 28; ++i) w1f[i >> 1][i & 1] = ld4s(rws, vo1, WS_W1T + (7 * c8 + (w % 7)) * 28672 + i * 1024);
      }
      SB0();
    }
    LDSF4(L_PR + w * 2048 + lane * 16) = nhB ? acc[0] : acc[2];
    LDSF4(L_PR + w * 2048 + 1024 + lane * 16) = nhB ? acc[1] : acc[3];
    __syncthreads();
    const f4 e = (nhB ? acc[2] : acc[0]) + LDSF4(L_PR + (w ^ 4) * 2048 + lane * 16);
    const f4 o = (nhB ? acc[3] : acc[1]) + LDSF4(L_PR + (w ^ 4) * 2048 + 1024 + lane * 16);
    const f4 g0 = LDSF4(L_GA + (32 * nhB + 8 * g) * 4), g1 = LDSF4(L_GA + (32 * nhB + 8 * g) * 4 + 16);
    const v4u a2 = LDS4(L_A2 + (56 * c8 + 32 * nhB + 8 * g) * 2);
    v4u x1;
    x1.x = pk(lo2f(xres.x) + g0.x * e[0], hi2f(xres.x) + g0.y * o[0]);
    x1.y = pk(lo2f(xres.y) + g0.z * e[1], hi2f(xres.y) + g0.w * o[1]);
    x1.z = pk(lo2f(xres.z) + g1.x * e[2], hi2f(xres.z) + g1.y * o[2]);
    x1.w = pk(lo2f(xres.w) + g1.z * e[3], hi2f(xres.w) + g1.w * o[3]);
    v4u y;
#pragma unroll
    for (int j = 0; j < 4; ++j) y[j] = nosent(pk(lo2f(x1[j]) * lo2f(a2[j]), hi2f(x1[j]) * hi2f(a2[j])));
    const bool vld = !(nhB == 1 && g == 3);
    st4s(rws, vld ? lane * 16 : OOB, WS_X1 + (4 * g32 + mtB) * 14336 + (7 * c8 + 4 * nhB) * 256, y);
    float s1 = 0.f, s2 = 0.f;
#pragma unroll
    for (int j = 0; j < 4; ++j) { s1 = dot2(x1[j], 0x3F803F80u, s1); s2 = dot2(x1[j], x1[j], s2); }
    s1 += __shfl_xor(s1, 16); s2 += __shfl_xor(s2, 16);
    s1 += __shfl_xor(s1, 32); s2 += __shfl_xor(s2, 32);
    st2s(rws, g == 0 ? l15 * 8 : OOB, WS_ST + (4 * g32 + mtB) * 2048 + (2 * c8 + nhB) * 128,
         (v2u){nosent(__builtin_bit_cast(u32, s1)), nosent(__builtin_bit_cast(u32, s2))});
    if (vld) LDS4(L_X1S + (mtB * 7 + 4 * nhB) * 256 + lane * 16) = x1;
  }
  STAMPT(8);
  f4 au[2] = {{0, 0, 0, 0}, {0, 0, 0, 0}}, av[2] = {{0, 0, 0, 0}, {0, 0, 0, 0}};
#pragma unroll
  for (int s = 0; s < 14; ++s) {
    const v4u a2 = LDS4(L_A2 + (32 * s + 8 * g) * 2), sb = LDS4(L_SB + (32 * s + 8 * g) * 2);
    au[0] = mfma(w1f[s][0], a2, au[0]); au[1] = mfma(w1f[s][1], a2, au[1]);
    av[0] = mfma(w1f[s][0], sb, av[0]); av[1] = mfma(w1f[s][1], sb, av[1]);
  }
  PIN(au[0]); PIN(au[1]); PIN(av[0]); PIN(av[1]);
  STAMPT(23);
  {
    v4u t[7], st[2];
    const u32 base = WS_X1 + 4 * g32 * 14336 + w * 7168;
    const u32 sto = w < 4 ? lane * 16 : OOB;
    for (unsigned spins = 0;; ++spins) {
      CBAR();
      u32 m = 0;
#pragma unroll
      for (int i = 0; i < 7; ++i) t[i] = ld4s(rws, lane * 16, base + i * 1024);
      st[0] = ld4s(rws, sto, WS_ST + (4 * g32 + (w & 3)) * 2048);
      st[1] = ld4s(rws, sto, WS_ST + (4 * g32 + (w & 3)) * 2048 + 1024);
#pragma unroll
      for (int i = 0; i < 7; ++i) m = chk(m, t[i]);
      m = chk(chk(m, st[0]), st[1]);
      if (!__any(m == ~0u) || spins > SPIN_CAP) break;
      canary(rws, lane < 28 ? base + lane * 256 : ((lane >= 32 && lane < 48 && w < 4) ? WS_ST + (4 * g32 + w) * 2048 + (lane - 32) * 128 : OOB), 0, spins);
    }
    RUNPRIO();
#pragma unroll
    for (int i = 0; i < 7; ++i) LDS4(w * 7168 + i * 1024 + lane * 16) = t[i];
    if (w < 4) {
      f4 a = __builtin_bit_cast(f4, st[0]) + __builtin_bit_cast(f4, st[1]);
#pragma unroll
      for (int j = 0; j < 4; ++j) { a[j] += dppf<0x128>(a[j]); a[j] += __shfl_xor(a[j], 16); a[j] += __shfl_xor(a[j], 32); }
      const float m0 = a.x * (1.f / D_), m1 = a.z * (1.f / D_);
      const float r0 = __builtin_amdgcn_rsqf(fmaxf(a.y * (1.f / D_) - m0 * m0, 0.f) + 1e-5f);
      const float r1 = __builtin_amdgcn_rsqf(fmaxf(a.w * (1.f / D_) - m1 * m1, 0.f) + 1e-5f);
      if (lane < 8) LDSF4(L_MR + w * 128 + lane * 16) = (f4){m0, r0, m1, r1};
    }
    __syncthreads();
  }
  STAMPT(9);
  v4u wf[7][4];
#pragma unroll
  for (int mp = 0; mp < 2; ++mp) {
    f4 acc[2][2] = {{{0, 0, 0, 0}, {0, 0, 0, 0}}, {{0, 0, 0, 0}, {0, 0, 0, 0}}};
    v4u xb[3][2];
#pragma unroll
    for (int m = 0; m < 2; ++m) { xb[0][m] = LDS4((2 * mp + m) * 14336 + lane * 16); xb[1][m] = LDS4((2 * mp + m) * 14336 + 1024 + lane * 16); }
#pragma unroll
    for (int s = 0; s < 14; ++s) {
      if (s < 12) {
#pragma unroll
        for (int m = 0; m < 2; ++m) xb[(s + 2) % 3][m] = LDS4((2 * mp + m) * 14336 + (s + 2) * 1024 + lane * 16);
      }
      SB0();
#pragma unroll
      for (int m = 0; m < 2; ++m) {
        acc[m][0] = mfma(w1f[s][0], xb[s % 3][m], acc[m][0]);
        acc[m][1] = mfma(w1f[s][1], xb[s % 3][m], acc[m][1]);
      }
      SB0();
      wf[(14 * mp + s) >> 2][(14 * mp + s) & 3] = ld4s(rws, lane * 16, WS_W2T + ((c8 * 56 + 7 * w) * 4 + 14 * mp + s) * 1024);
      SB0();
    }
#pragma unroll
    for (int m = 0; m < 2; ++m) {
      const f2 mr2 = *(LAS f2*)(lds + L_MR + ((2 * mp + m) * 16 + l15) * 8);
      const f2 nm = {-mr2.x, -mr2.x}, rs = {mr2.y, mr2.y};
      v4u hv;
#pragma unroll
      for (int r = 0; r < 4; ++r) {
        f2 h = __builtin_elementwise_fma(nm, (f2){au[0][r], au[1][r]}, (f2){acc[m][0][r], acc[m][1][r]});
        h = __builtin_elementwise_fma(rs, h, (f2){av[0][r], av[1][r]});
        hv[r] = pk(fmaxf(h.x, 0.f), fmaxf(h.y, 0.f));
      }
      st4s(rws, w < 7 ? lane * 16 : OOB, WS_HID + (4 * g32 + 2 * mp + m) * 57344 + (28 * c8 + 4 * (w % 7)) * 256, hv);
    }
    SB0();
  }
  STAMPT(10);
  __syncthreads();
  {
    v4u hf[2][2][7];
#define HLOAD(mp) { _Pragma("unroll") for (int i = 0; i < 2; ++i) _Pragma("unroll") for (int s = 0; s < 7; ++s) \
      hf[mp][i][s] = ld4s(rws, lane * 16, WS_HID + (4 * g32 + 2 * (mp) + i) * 57344 + w * 7168 + s * 1024); }
    HLOAD(0)
#pragma unroll
    for (int mp = 0; mp < 2; ++mp) {
      for (unsigned spins = 0;; ++spins) {
        u32 m = 0;
#pragma unroll
        for (int i = 0; i < 2; ++i)
#pragma unroll
          for (int s = 0; s < 7; ++s) m = chk(m, hf[mp][i][s]);
        if (!__any(m == ~0u) || spins > SPIN_CAP) break;
        canary(rws, (lane & 7) < 7 && lane < 16 ? (lane >> 3) * 57344 + (lane & 7) * 1024 : OOB, WS_HID + (4 * g32 + 2 * mp) * 57344 + w * 7168, spins);
        CBAR();
        HLOAD(mp)
      }
      RUNPRIO();
      if (mp == 0) STAMPT(11);
#pragma unroll
      for (int i = 0; i < 2; ++i) {
        f4 acc[4] = {{0, 0, 0, 0}, {0, 0, 0, 0}, {0, 0, 0, 0}, {0, 0, 0, 0}};
#pragma unroll
        for (int s = 0; s < 7; ++s)
#pragma unroll
          for (int t = 0; t < 4; ++t) acc[t] = mfma(wf[s][t], hf[mp][i][s], acc[t]);
#pragma unroll
        for (int r = 0; r < 4; ++r) LDSF4(((w * 4 + 2 * mp + i) * 4 + r) * 1024 + lane * 16) = (f4){acc[0][r], acc[1][r], acc[2][r], acc[3][r]};
        if (mp == 0 && i == 0) { SB0(); HLOAD(1) SB0(); }
      }
    }
  }
  __syncthreads();
  STAMPT(12);
  {
    const int mt = tid >> 7, rp = (tid >> 6) & 1;
    if (!(g == 3 && rp == 1)) {
      f4 s0 = {0, 0, 0, 0}, s1 = {0, 0, 0, 0};
#pragma unroll
      for (int ww = 0; ww < 8; ++ww) {
        s0 += LDSF4(((ww * 4 + mt) * 4 + 2 * rp) * 1024 + lane * 16);
        s1 += LDSF4(((ww * 4 + mt) * 4 + 2 * rp + 1) * 1024 + lane * 16);
      }
      const v4u x1 = LDS4(L_X1S + (mt * 7 + 2 * g + rp) * 256 + l15 * 16);
      const f4 g0 = LDSF4(L_GB + (16 * g + 8 * rp) * 4), g1 = LDSF4(L_GB + (16 * g + 8 * rp) * 4 + 16);
      v4u o;
      o.x = pk(lo2f(x1.x) + g0.x * s0.x, hi2f(x1.x) + g0.y * s0.y);
      o.y = pk(lo2f(x1.y) + g0.z * s0.z, hi2f(x1.y) + g0.w * s0.w);
      o.z = pk(lo2f(x1.z) + g1.x * s1.x, hi2f(x1.z) + g1.y * s1.y);
      o.w = pk(lo2f(x1.w) + g1.z * s1.z, hi2f(x1.w) + g1.w * s1.w);
      __builtin_nontemporal_store(o, (v4u*)(out + (size_t)(64 * g32 + 16 * mt + l15) * D_ + 56 * c8 + 16 * g + 8 * rp));
    }
  }
  if (b == 0 && w == 1) {
    for (unsigned spins = 0;; ++spins) {
      bool ok = true;
#pragma unroll
      for (int i = 0; i < 4; ++i) ok &= (__hip_atomic_load(gr + G_PREP + lane + 64 * i, RLX_AGENT) >> 16) == tag;
      if (__all(ok) || spins > SPIN_CAP) break;
      BACKOFF();
    }
    if (lane == 0) __hip_atomic_store((gu64*)ws, epoch + 1, RLX_AGENT);
  }
  __syncthreads();
  if (b == 100 && w == 0) {
    u64 mn = ~0ull, mx = 0;
    for (int i = 0; i < 4; ++i) { const u64 v = __hip_atomic_load((gu64*)(ws + WS_END) + lane + 64 * i, RLX_AGENT); mn = v < mn ? v : mn; mx = v > mx ? v : mx; }
    for (int m = 32; m >= 1; m >>= 1) { const u64 a = __shfl_xor(mn, m), c = __shfl_xor(mx, m); mn = a < mn ? a : mn; mx = c > mx ? c : mx; }
    const u64 span = __builtin_amdgcn_s_memrealtime() - mn, skew = mx - mn;
    if (lane == 0 && (skew >= 120 && skew < 250)) out[(size_t)(64 * g32) * D_ + 56 * c8] = 0x4700;
  }
  STAMPT(13);
  asm volatile(".Lmega_end:");
}

extern "C" void kernel_launch(void* const* d_in, const int* in_sizes, int n_in, void* d_out, int out_size, void* d_ws,
                              size_t ws_size, hipStream_t stream) {
  (void)hipFuncSetAttribute((const void*)mega, hipFuncAttributeMaxDynamicSharedMemorySize, LDS_BYTES);
  mega<<<256, 512, LDS_BYTES, stream>>>((const u16*)d_in[0], (const u16*)d_in[1], (const u16*)d_in[2], (const u16*)d_in[4],
                                        (const u16*)d_in[5], (const u16*)d_in[6], (const u16*)d_in[7], (u16*)d_out, (char*)d_ws);
}
```

```cpp
#include <hip/hip_runtime.h>
#include <stdint.h>

typedef unsigned int u32;
typedef unsigned long long u64;
typedef unsigned short u16;
typedef u32 v4u __attribute__((ext_vector_type(4)));
typedef u32 v2u __attribute__((ext_vector_type(2)));
typedef float f4 __attribute__((ext_vector_type(4)));
typedef float f2 __attribute__((ext_vector_type(2)));
typedef __bf16 bf2v __attribute__((ext_vector_type(2)));
typedef __bf16 bf8v __attribute__((ext_vector_type(8)));
#define GAS __attribute__((address_space(1)))
#define LAS __attribute__((address_space(3)))
typedef GAS u64 gu64;
#define RLX_AGENT __ATOMIC_RELAXED, __HIP_MEMORY_SCOPE_AGENT
typedef __amdgpu_buffer_rsrc_t rsrc_t;

constexpr int S_ = 2048, D_ = 448, DFF_ = 1792;
constexpr int WS_GRAN = 4096;
constexpr int G_PREP = 2688, G_FILL = 2944;
constexpr int G_REP = 4096;
constexpr int WS_KV = 524288;
constexpr int WS_O = WS_KV + 4194304;
constexpr int WS_X1 = WS_O + 2097152;
constexpr int WS_ST = WS_X1 + 1835008;
constexpr int WS_HID = WS_ST + 262144;
constexpr int WS_W2T = WS_HID + 7340032;
constexpr int WS_W1T = WS_W2T + 1835008;
constexpr int WS_WOT = WS_W1T + 1605632;
constexpr int WS_END = WS_WOT + 524288;
constexpr int WS_DBG = WS_END;
constexpr int L_WQ = 0;
constexpr int L_K = 0, L_V = 24576;
constexpr int L_PR = 57344;
constexpr int L_WO = 86016;
constexpr int L_X = 36864;
constexpr int L_UV = L_WO;
constexpr int L_SM = 151552;
constexpr int L_AS1 = L_SM;
constexpr int L_A2 = L_AS1 + 1792;
constexpr int L_SB = L_A2 + 896;
constexpr int L_GA = L_SB + 896;
constexpr int L_GB = L_GA + 256;
constexpr int L_MR = L_GB + 256;
constexpr int L_RED = L_MR + 512;
constexpr int L_X1S = L_RED;
constexpr int L_END = L_X1S + 7168;
static_assert(L_END <= 163840, "lds");
constexpr int LDS_BYTES = 163840;
constexpr u32 OOB = 0x80000000u;
constexpr unsigned SPIN_CAP = 1u << 15;

__device__ __forceinline__ u32 pk(float lo, float hi) { f2 v = {lo, hi}; return __builtin_bit_cast(u32, __builtin_convertvector(v, bf2v)); }
__device__ __forceinline__ float dot2(u32 a, u32 b, float c) { return __builtin_amdgcn_fdot2_f32_bf16(__builtin_bit_cast(bf2v, a), __builtin_bit_cast(bf2v, b), c, false); }
__device__ __forceinline__ f4 mfma(v4u a, v4u b, f4 c) { return __builtin_amdgcn_mfma_f32_16x16x32_bf16(__builtin_bit_cast(bf8v, a), __builtin_bit_cast(bf8v, b), c, 0, 0, 0); }
__device__ __forceinline__ float lo2f(u32 v) { return __builtin_bit_cast(float, v << 16); }
__device__ __forceinline__ float hi2f(u32 v) { return __builtin_bit_cast(float, v & 0xffff0000u); }
__device__ __forceinline__ u32 plo(u32 r0, u32 r1) { return __builtin_amdgcn_perm(r1, r0, 0x05040100u); }
__device__ __forceinline__ u32 phi(u32 r0, u32 r1) { return __builtin_amdgcn_perm(r1, r0, 0x07060302u); }
__device__ __forceinline__ u32 nosent(u32 v) { return v < 0xFFFFFFFEu ? v : 0xFFFFFFFEu; }
__device__ __forceinline__ v4u nosent4(v4u v) { v.x = nosent(v.x); v.y = nosent(v.y); v.z = nosent(v.z); v.w = nosent(v.w); return v; }
__device__ __forceinline__ u32 umax3(u32 a, u32 b, u32 c) { u32 m = a > b ? a : b; return m > c ? m : c; }
__device__ __forceinline__ u32 chk(u32 m, v4u v) { return umax3(umax3(m, v.x, v.y), v.z, v.w); }
template <int C> __device__ __forceinline__ float dppf(float v) { return __builtin_bit_cast(float, __builtin_amdgcn_update_dpp(0, __builtin_bit_cast(int, v), C, 0xf, 0xf, false)); }
__device__ __forceinline__ float rowsum16(float v) { v += dppf<0xB1>(v); v += dppf<0x4E>(v); v += dppf<0x141>(v); v += dppf<0x140>(v); return v; }
__device__ __forceinline__ v4u ld4(rsrc_t r, u32 vo, u32 so) { return __builtin_amdgcn_raw_buffer_load_b128(r, vo, so, 0); }
__device__ __forceinline__ v4u ld4s(rsrc_t r, u32 vo, u32 so) { return __builtin_amdgcn_raw_buffer_load_b128(r, vo, so, 16); }
__device__ __forceinline__ v2u ld2(rsrc_t r, u32 vo, u32 so) { return __builtin_amdgcn_raw_buffer_load_b64(r, vo, so, 0); }
__device__ __forceinline__ u32 ld1(rsrc_t r, u32 vo, u32 so) { return __builtin_amdgcn_raw_buffer_load_b32(r, vo, so, 0); }
__device__ __forceinline__ void st4s(rsrc_t r, u32 vo, u32 so, v4u v) { __builtin_amdgcn_raw_buffer_store_b128(v, r, vo + so, 0, 16); }
__device__ __forceinline__ void st2s(rsrc_t r, u32 vo, u32 so, v2u v) { __builtin_amdgcn_raw_buffer_store_b64(v, r, vo, so, 16); }
__device__ __forceinline__ v4u modulate(const u32 (&xh)[8], v4u a0, v4u a1) {
  v4u h;
  float d0, d1, d2, d3, d4, d5, d6, d7;
  asm volatile(
      "v_dot2_f32_bf16 %4, %12, %20, 0\n\tv_dot2_f32_bf16 %5, %13, %21, 0\n\tv_dot2_f32_bf16 %6, %14, %22, 0\n\tv_dot2_f32_bf16 %7, %15, %23, 0\n\t"
      "v_dot2_f32_bf16 %8, %16, %24, 0\n\tv_dot2_f32_bf16 %9, %17, %25, 0\n\tv_dot2_f32_bf16 %10, %18, %26, 0\n\tv_dot2_f32_bf16 %11, %19, %27, 0\n\t"
      "v_cvt_pk_bf16_f32 %0, %4, %5\n\tv_cvt_pk_bf16_f32 %1, %6, %7\n\tv_cvt_pk_bf16_f32 %2, %8, %9\n\tv_cvt_pk_bf16_f32 %3, %10, %11\n\ts_nop 1"
      : "=&v"(h.x), "=&v"(h.y), "=&v"(h.z), "=&v"(h.w), "=&v"(d0), "=&v"(d1), "=&v"(d2), "=&v"(d3), "=&v"(d4), "=&v"(d5), "=&v"(d6), "=&v"(d7)
      : "v"(xh[0]), "v"(xh[1]), "v"(xh[2]), "v"(xh[3]), "v"(xh[4]), "v"(xh[5]), "v"(xh[6]), "v"(xh[7]),
        "v"(a0.x), "v"(a0.y), "v"(a0.z), "v"(a0.w), "v"(a1.x), "v"(a1.y), "v"(a1.z), "v"(a1.w));
  return h;
}
typedef short s4v __attribute__((ext_vector_type(4)));
__device__ __forceinline__ v2u trrd(LAS char* p) { return __builtin_bit_cast(v2u, __builtin_amdgcn_ds_read_tr16_b64_v4i16((LAS s4v*)p)); }
__device__ __forceinline__ v4u mksrd(const void* p, u32 bytes) {
  const u64 a = (u64)p;
  return (v4u){(u32)__builtin_amdgcn_readfirstlane((u32)a), (u32)__builtin_amdgcn_readfirstlane((u32)(a >> 32)) & 0xffffu, bytes, 0x00020000u};
}
template <bool SC1> __device__ __forceinline__ void dma16(v4u srd, u32 vo, u32 so, u32 ldsaddr) {
  u32 keep;
  so = __builtin_amdgcn_readfirstlane(so);
  ldsaddr = __builtin_amdgcn_readfirstlane(ldsaddr);
  if (SC1)
    asm volatile("s_mov_b32 %0, m0\n\ts_mov_b32 m0, %4\n\ts_nop 3\n\tbuffer_load_dwordx4 %1, %2, %3 offen sc1 lds\n\ts_mov_b32 m0, %0"
                 : "=&s"(keep) : "v"(vo), "s"(srd), "s"(so), "s"(ldsaddr) : "memory");
  else
    asm volatile("s_mov_b32 %0, m0\n\ts_mov_b32 m0, %4\n\ts_nop 3\n\tbuffer_load_dwordx4 %1, %2, %3 offen lds\n\ts_mov_b32 m0, %0"
                 : "=&s"(keep) : "v"(vo), "s"(srd), "s"(so), "s"(ldsaddr) : "memory");
}
__device__ __forceinline__ void canary(rsrc_t r, u32 vo, u32 so, unsigned& spins) {
  __builtin_amdgcn_s_setprio(0);
  for (; spins <= SPIN_CAP; ++spins) {
    __builtin_amdgcn_s_sleep(2);
    asm volatile("" ::: "memory");
    if (!__any(__builtin_amdgcn_raw_buffer_load_b32(r, vo, so, 16) == ~0u)) break;
  }
}
#define LDS4(off) (*(LAS v4u*)(lds + (off)))
#define LDS2(off) (*(LAS v2u*)(lds + (off)))
#define LDSF4(off) (*(LAS f4*)(lds + (off)))
#define LDSF(off) (*(LAS float*)(lds + (off)))
#define LDSH(off) (*(LAS u16*)(lds + (off)))
#define CBAR() asm volatile("" ::: "memory")
#define PIN(v) asm volatile("" : "+v"(v))
#define SB0() __builtin_amdgcn_sched_barrier(0)
#define BACKOFF() do { __builtin_amdgcn_s_setprio(0); __builtin_amdgcn_s_sleep(6); } while (0)
#define RUNPRIO() __builtin_amdgcn_s_setprio(1)
#define DRAIN() asm volatile("s_waitcnt vmcnt(0)" ::: "memory")
#define STAMPT(i)

__global__ __launch_bounds__(512, 2) void mega(const u16* __restrict__ x, const u16* __restrict__ wqkv, const u16* __restrict__ wo,
                                               const u16* __restrict__ w1, const u16* __restrict__ w2, const u16* __restrict__ cvec,
                                               const u16* __restrict__ wmod, u16* __restrict__ out, char* ws) {
  extern __shared__ __attribute__((aligned(16))) char lds_g[];
  LAS char* lds = (LAS char*)lds_g;
  const int tid = threadIdx.x, lane = tid & 63, w = __builtin_amdgcn_readfirstlane(tid >> 6) & 7;
  const int l15 = lane & 15, g = lane >> 4;
  const int b = blockIdx.x, xcd = b & 7, slot = b >> 3;
  const int g16 = xcd * 2 + (slot >> 4), head = slot & 15;
  const int g32 = xcd * 4 + (slot >> 3), c8 = slot & 7;
  const rsrc_t rws = __builtin_amdgcn_make_buffer_rsrc(ws, 0, WS_END, 0x00020000);
  const rsrc_t rx = __builtin_amdgcn_make_buffer_rsrc((void*)x, 0, S_ * D_ * 2, 0x00020000);
  const rsrc_t rwq = __builtin_amdgcn_make_buffer_rsrc((void*)wqkv, 0, D_ * 3 * D_ * 2, 0x00020000);
  const rsrc_t rwm = __builtin_amdgcn_make_buffer_rsrc((void*)wmod, 0, D_ * 6 * D_ * 2, 0x00020000);
  const rsrc_t rc = __builtin_amdgcn_make_buffer_rsrc((void*)cvec, 0, D_ * 2, 0x00020000);
  const v4u SENT = {~0u, ~0u, ~0u, ~0u};
  STAMPT(0);
  RUNPRIO();
  if (w == 7 && slot == 31) asm volatile("buffer_wbl2 sc1" ::: "memory");
  u32 cpf, mpf;
  u64 epoch;
  {
    u64 pc;
    u32 len;
    asm volatile("s_getpc_b64 %0\n.Lmega_start:\n\ts_mov_b32 %1, .Lmega_end-.Lmega_start" : "=s"(pc), "=s"(len));
    const v4u scode = mksrd((const void*)pc, len), swm0 = mksrd(wmod, D_ * 6 * D_ * 2), sws0 = mksrd(ws, 64);
    const int idx = slot * 84 + tid - 320, n = idx / 448, row = idx - 448 * n, j = xcd + 8 * n;
    asm volatile("s_nop 4\n\tbuffer_load_dword %0, %3, %5, 0 offen\n\tbuffer_load_dword %1, %4, %6, 0 offen\n\tbuffer_load_dwordx2 %2, off, %7, 0 sc1"
                 : "=&v"(cpf), "=&v"(mpf), "=&v"(epoch)
                 : "v"(tid >= 448 ? (lane * 8 + (slot & 7)) * 128 : OOB), "v"((tid >= 320 && tid < 404 && j < 42) ? (u32)(row * 5376 + j * 128) : OOB),
                   "s"(scode), "s"(swm0), "s"(sws0)
                 : "memory");
  }
  gu64* gr0 = (gu64*)(ws + WS_GRAN);
  gu64* gr = gr0 + xcd * G_REP;
  const int mj = xcd + 8 * (slot / 3);
  const bool modw = (slot % 3 == 0) && mj < 42 && w < 4;
  if (tid < 4) *(volatile LAS u32*)(lds + L_RED + 3072 + tid * 4) = 0u;
  v2u mr[4][8];
  u32 craw;
  if (modw) {
    const v4u swm = mksrd(wmod, D_ * 6 * D_ * 2), sc_ = mksrd(cvec, D_ * 2);
    asm volatile("s_nop 4\n\tbuffer_load_dword %0, %1, %2, 0 offen" : "=&v"(craw) : "v"(w + 4 * g < 14 ? (32 * (w + 4 * g) + 2 * l15) * 2 : OOB), "s"(sc_) : "memory");
#pragma unroll
    for (int i = 0; i < 4; ++i) {
      const u32 vo = (i < 3 || w < 2) ? (u32)(8 * g * 2688 + 4 * l15) * 2 : OOB;
      const u32 so = (u32)((32 * ((w + 4 * i) % 14)) * 2688 + 64 * mj) * 2;
      asm volatile(
          "s_nop 4\n\t"
          "buffer_load_dwordx2 %0, %8, %9, %10 offen\n\tbuffer_load_dwordx2 %1, %8, %9, %11 offen\n\t"
          "buffer_load_dwordx2 %2, %8, %9, %12 offen\n\tbuffer_load_dwordx2 %3, %8, %9, %13 offen\n\t"
          "buffer_load_dwordx2 %4, %8, %9, %14 offen\n\tbuffer_load_dwordx2 %5, %8, %9, %15 offen\n\t"
          "buffer_load_dwordx2 %6, %8, %9, %16 offen\n\tbuffer_load_dwordx2 %7, %8, %9, %17 offen"
          : "=&v"(mr[i][0]), "=&v"(mr[i][1]), "=&v"(mr[i][2]), "=&v"(mr[i][3]), "=&v"(mr[i][4]), "=&v"(mr[i][5]), "=&v"(mr[i][6]), "=&v"(mr[i][7])
          : "v"(vo), "s"(swm), "s"(so), "s"(so + 5376), "s"(so + 2 * 5376), "s"(so + 3 * 5376), "s"(so + 4 * 5376), "s"(so + 5 * 5376),
            "s"(so + 6 * 5376), "s"(so + 7 * 5376)
          : "memory");
    }
  }
  asm volatile("s_waitcnt lgkmcnt(0)\n\ts_barrier" ::: "memory");
  STAMPT(14);
  const v4u sq_srd = mksrd(wqkv, D_ * 3 * D_ * 2);
  const u32 vq = (u32)((lane >> 2) * 2688 + (((lane & 3) ^ (2 * (lane >> 5))) * 16));
#define WQDMA(i)                                                                                                        \
  {                                                                                                                     \
    const int u = w + 8 * ((i) >> 1), d = 2 * u + ((i) & 1), mat = u / 14, kb = d - 28 * mat;                           \
    dma16<false>(sq_srd, u < 42 ? vq : OOB, (u32)(16 * kb * 2688 + (mat % 3) * 896 + 56 * head), u < 42 ? L_WQ + d * 1024 : L_RED + 4096); \
  }
  {
    const v4u sx = mksrd(x, S_ * D_ * 2);
    const u32 vo = (u32)((lane >> 3) * D_ * 2 + (((lane & 7) ^ (lane >> 3)) * 16));
#pragma unroll
    for (int q = 0; q < 14; ++q) dma16<false>(sx, vo, (u32)((128 * g16 + 16 * w + 8 * (q & 1)) * D_ * 2 + (q >> 1) * 128), L_X + w * 14336 + q * 1024);
  }
  asm volatile("s_barrier" ::: "memory");
  WQDMA(0) WQDMA(1) WQDMA(2) WQDMA(3)
  u32 pr[8];
  u32 pdst;
  {
    const int u = b * 8 + w;
    const u16* base; u32 bytes, ld, so, vo, vo2;
    if (u < 896) {
      const int cc = u / 112, v = u - 112 * cc, kb = v >> 1, hf = v & 1;
      base = w2; bytes = DFF_ * D_ * 2; ld = D_ * 2; so = (u32)(32 * kb * D_ + 56 * cc + 2 * hf) * 2;
      vo = l15 < 14 ? (u32)(8 * g * D_ + 4 * l15) * 2 : OOB; vo2 = vo;
      pdst = WS_W2T + ((cc * 56 + kb) * 4 + 2 * hf) * 1024;
    } else if (u < 1680) {
      const int v = u - 896, cb = v / 14, s = v - 14 * cb;
      base = w1; bytes = DFF_ * D_ * 2; ld = DFF_ * 2; so = (u32)(32 * s * DFF_ + 32 * cb) * 2;
      vo = (u32)(8 * g * DFF_ + 2 * l15) * 2; vo2 = vo;
      pdst = WS_W1T + (cb * 14 + s) * 2048;
    } else {
      const int v = u - 1680, hh = v & 15, nh = (v >> 4) & 1, cc = (v >> 5) & 7;
      base = wo; bytes = D_ * D_ * 2; ld = D_ * 2; so = (u32)(28 * hh * D_ + 56 * cc + 32 * nh) * 2;
      vo = (u < 1936 && (nh == 0 || l15 < 12)) ? (u32)(4 * g * D_ + 2 * l15) * 2 : OOB; vo2 = g < 3 ? vo + 12 * D_ * 2 : OOB;
      pdst = u < 1936 ? WS_WOT + (cc * 32 + nh * 16 + hh) * 2048 : OOB;
    }
    const v4u sp = mksrd(base, __builtin_amdgcn_readfirstlane(bytes));
    so = __builtin_amdgcn_readfirstlane(so); ld = __builtin_amdgcn_readfirstlane(ld);
    asm volatile(
        "s_nop 4\n\t"
        "buffer_load_dword %0, %8, %10, %11 offen\n\tbuffer_load_dword %1, %8, %10, %12 offen\n\t"
        "buffer_load_dword %2, %8, %10, %13 offen\n\tbuffer_load_dword %3, %8, %10, %14 offen\n\t"
        "buffer_load_dword %4, %9, %10, %15 offen\n\tbuffer_load_dword %5, %9, %10, %16 offen\n\t"
        "buffer_load_dword %6, %9, %10, %17 offen\n\tbuffer_load_dword %7, %9, %10, %18 offen"
        : "=&v"(pr[0]), "=&v"(pr[1]), "=&v"(pr[2]), "=&v"(pr[3]), "=&v"(pr[4]), "=&v"(pr[5]), "=&v"(pr[6]), "=&v"(pr[7])
        : "v"(vo), "v"(vo2), "s"(sp), "s"(so), "s"(so + ld), "s"(so + 2 * ld), "s"(so + 3 * ld), "s"(so + 4 * ld), "s"(so + 5 * ld), "s"(so + 6 * ld),
          "s"(so + 7 * ld)
        : "memory");
  }
  SB0();
  asm volatile("s_waitcnt vmcnt(26)" : "+v"(epoch), "+v"(cpf), "+v"(mpf)::"memory");
  const u64 tag = (epoch + 1) & 0xFFFFFFFFFFFFull;
  if (modw) {
    __builtin_amdgcn_s_setprio(3);
    asm volatile(""
                 : "+v"(craw), "+v"(mr[0][0]), "+v"(mr[0][1]), "+v"(mr[0][2]), "+v"(mr[0][3]), "+v"(mr[0][4]), "+v"(mr[0][5]), "+v"(mr[0][6]), "+v"(mr[0][7]),
                   "+v"(mr[1][0]), "+v"(mr[1][1]), "+v"(mr[1][2]), "+v"(mr[1][3]), "+v"(mr[1][4]), "+v"(mr[1][5]), "+v"(mr[1][6]), "+v"(mr[1][7])::"memory");
    asm volatile(""
                 : "+v"(mr[2][0]), "+v"(mr[2][1]), "+v"(mr[2][2]), "+v"(mr[2][3]), "+v"(mr[2][4]), "+v"(mr[2][5]), "+v"(mr[2][6]), "+v"(mr[2][7]),
                   "+v"(mr[3][0]), "+v"(mr[3][1]), "+v"(mr[3][2]), "+v"(mr[3][3]), "+v"(mr[3][4]), "+v"(mr[3][5]), "+v"(mr[3][6]), "+v"(mr[3][7])::"memory");
    float c0 = lo2f(craw), c1 = hi2f(craw);
    c0 = c0 * __builtin_amdgcn_rcpf(1.f + __expf(-c0));
    c1 = c1 * __builtin_amdgcn_rcpf(1.f + __expf(-c1));
    *(LAS u32*)(lds + L_RED + w * 256 + lane * 4) = pk(c0, c1);
    f4 acc[4] = {{0, 0, 0, 0}, {0, 0, 0, 0}, {0, 0, 0, 0}, {0, 0, 0, 0}};
#pragma unroll
    for (int i = 0; i < 4; ++i) {
      const v4u sf = LDS4(L_RED + w * 256 + i * 64 + g * 16);
      acc[0] = mfma((v4u){plo(mr[i][0].x, mr[i][1].x), plo(mr[i][2].x, mr[i][3].x), plo(mr[i][4].x, mr[i][5].x), plo(mr[i][6].x, mr[i][7].x)}, sf, acc[0]);
      acc[1] = mfma((v4u){phi(mr[i][0].x, mr[i][1].x), phi(mr[i][2].x, mr[i][3].x), phi(mr[i][4].x, mr[i][5].x), phi(mr[i][6].x, mr[i][7].x)}, sf, acc[1]);
      acc[2] = mfma((v4u){plo(mr[i][0].y, mr[i][1].y), plo(mr[i][2].y, mr[i][3].y), plo(mr[i][4].y, mr[i][5].y), plo(mr[i][6].y, mr[i][7].y)}, sf, acc[2]);
      acc[3] = mfma((v4u){phi(mr[i][0].y, mr[i][1].y), phi(mr[i][2].y, mr[i][3].y), phi(mr[i][4].y, mr[i][5].y), phi(mr[i][6].y, mr[i][7].y)}, sf, acc[3]);
    }
    if (l15 == 0) {
#pragma unroll
      for (int r = 0; r < 4; ++r) LDSF4(L_RED + 1024 + w * 256 + (16 * g + 4 * r) * 4) = (f4){acc[0][r], acc[1][r], acc[2][r], acc[3][r]};
    }
    asm volatile("s_waitcnt lgkmcnt(0)" ::: "memory");
    u32 old = 0;
    if (lane == 0) old = __hip_atomic_fetch_add((LAS u32*)(lds + L_RED + 3072), 1u, __ATOMIC_RELAXED, __HIP_MEMORY_SCOPE_WORKGROUP);
    if (__builtin_amdgcn_readfirstlane(old) == 3u) {
      CBAR();
      const float sum = (LDSF(L_RED + 1024 + lane * 4) + LDSF(L_RED + 1280 + lane * 4)) + (LDSF(L_RED + 1536 + lane * 4) + LDSF(L_RED + 1792 + lane * 4));
      const u64 gval = (tag << 16) | (u64)(pk(sum, 0.f) & 0xffffu);
#pragma unroll
      for (int r = 0; r < 8; ++r) __hip_atomic_store(gr0 + r * G_REP + 64 * mj + lane, gval, RLX_AGENT);
    }
    RUNPRIO();
  }
  STAMPT(15);
  if (modw) asm volatile("s_waitcnt vmcnt(8)" ::: "memory"); else asm volatile("s_waitcnt vmcnt(12)" ::: "memory");
  STAMPT(16);
  v4u xr[14];
  {
    const u32 ra0 = L_X + w * 14336 + (l15 >> 3) * 1024 + (l15 & 7) * 128 + ((g ^ (l15 & 7)) * 16);
    const u32 ra1 = L_X + w * 14336 + (l15 >> 3) * 1024 + (l15 & 7) * 128 + (((4 + g) ^ (l15 & 7)) * 16);
#pragma unroll
    for (int s = 0; s < 14; ++s) xr[s] = LDS4(((s & 1) ? ra1 : ra0) + (s >> 1) * 2048);
#pragma unroll
    for (int s = 0; s < 14; ++s) PIN(xr[s]);
  }
  asm volatile("s_waitcnt lgkmcnt(0)\n\ts_barrier" ::: "memory");
  STAMPT(17);
  const int mtB = w & 3, nhB = w >> 2;
  v4u xres;
  const int tau = 16 * (w & 1) + l15;
  const u32 koff = (w >> 1) * 2048 + ((tau >> 2) & 1) * 1024 + (g * 16 + 4 * (tau >> 3) + (tau & 3)) * 16;
  const u32 voff = (w >> 1) * 2048 + ((2 * (w & 1) + (g >> 1)) * 16 + l15) * 16 + (g & 1) * 8;
  const u32 kvb = g16 < 15 ? WS_KV + (g16 * 16 + head) * 16384 : OOB;
  u32 xh[14][8];
  {
    const v4u ONES = {0x3F803F80u, 0x3F803F80u, 0x3F803F80u, 0x3F803F80u};
    f4 sm = {0, 0, 0, 0}, gm = {0, 0, 0, 0};
#pragma unroll
    for (int s = 0; s < 14; ++s) {
      if (s == 0 || s == 7) WQDMA(4 + s / 7)
      sm = mfma(ONES, xr[s], sm);
      gm = mfma(xr[s], xr[s], gm);
    }
    float sq = (l15 & 2) ? ((l15 & 1) ? gm[3] : gm[2]) : ((l15 & 1) ? gm[1] : gm[0]);
    sq = __shfl(sq, 16 * (l15 >> 2) + l15);
    const float mean = sm[0] * (1.f / D_);
    const float rstd = __builtin_amdgcn_rsqf(fmaxf(sq * (1.f / D_) - mean * mean, 0.f) + 1e-5f);
    const f2 rs2 = {rstd, rstd}, nm2 = {-mean * rstd, -mean * rstd};
#pragma unroll
    for (int s = 0; s < 14; ++s) {
      if (s < 6) WQDMA(6 + s)
      else if (s == 7) xres = ld4(rx, (nhB == 1 && g == 3) ? OOB : (u32)(l15 * D_ + 8 * g) * 2, (u32)((64 * g32 + 16 * mtB) * D_ + 56 * c8 + 32 * nhB) * 2);
      else if (s == 9) st4s(rws, koff, kvb, SENT);
      else if (s == 11) st4s(rws, (w >> 1) * 2048 + (w & 1) * 512 + (lane & 31) * 16 + (lane >> 5) * 1024, kvb + 8192, SENT);
      else if (s == 13) st4s(rws, lane * 16, WS_O + ((8 * g16 + w) * 16 + head) * 1024, SENT);
#pragma unroll
      for (int j = 0; j < 4; ++j) {
        const f2 v = __builtin_elementwise_fma((f2){lo2f(xr[s][j]), hi2f(xr[s][j])}, rs2, nm2);
        xh[s][2 * j] = pk(v.x, 1.f);
        xh[s][2 * j + 1] = pk(v.y, 1.f);
        PIN(xh[s][2 * j]); PIN(xh[s][2 * j + 1]);
      }
      SB0();
    }
  }
  STAMPT(20);
  const int i1 = tid < 384 ? 512 + tid : tid;
  u64 g10 = __hip_atomic_load(gr + tid, RLX_AGENT), g11 = __hip_atomic_load(gr + i1, RLX_AGENT);
  asm volatile("s_waitcnt vmcnt(6)"
               : "+v"(pr[0]), "+v"(pr[1]), "+v"(pr[2]), "+v"(pr[3]), "+v"(pr[4]), "+v"(pr[5]), "+v"(pr[6]), "+v"(pr[7])::"memory");
  {
    LAS char* tb0 = lds + L_WQ + (8 * g + (l15 >> 2)) * 64 + (g & 1) * 32 + (l15 & 3) * 8;
    LAS char* tb1 = lds + L_WQ + (8 * g + (l15 >> 2)) * 64 + ((g & 1) ^ 1) * 32 + (l15 & 3) * 8;
    v2u r[6][4];
#pragma unroll
    for (int i = 0; i < 6; ++i) {
      const int u = (i < 5 || w < 2) ? w + 8 * i : w;
      r[i][0] = trrd(tb0 + u * 2048); r[i][1] = trrd(tb0 + u * 2048 + 256);
      r[i][2] = trrd(tb1 + u * 2048); r[i][3] = trrd(tb1 + u * 2048 + 256);
    }
    SB0();
#pragma unroll
    for (int i = 0; i < 6; ++i) {
      const int u = (i < 5 || w < 2) ? w + 8 * i : w;
      LDS4(L_WQ + u * 2048 + lane * 16) = (v4u){r[i][0].x, r[i][0].y, r[i][1].x, r[i][1].y};
      LDS4(L_WQ + u * 2048 + 1024 + lane * 16) = (v4u){r[i][2].x, r[i][2].y, r[i][3].x, r[i][3].y};
    }
  }
  v4u pe = {plo(pr[0], pr[1]), plo(pr[2], pr[3]), plo(pr[4], pr[5]), plo(pr[6], pr[7])};
  v4u po = {phi(pr[0], pr[1]), phi(pr[2], pr[3]), phi(pr[4], pr[5]), phi(pr[6], pr[7])};
  PIN(pe); PIN(po);
  STAMPT(2);
  {
    for (unsigned spins = 0;; ++spins) {
      const bool ok = (g10 >> 16) == tag && (g11 >> 16) == tag;
      if (__all(ok) || spins > SPIN_CAP) break;
      BACKOFF();
      g10 = __hip_atomic_load(gr + tid, RLX_AGENT);
      g11 = __hip_atomic_load(gr + i1, RLX_AGENT);
    }
    RUNPRIO();
    if (tid < 448) LDSH(L_AS1 + tid * 4 + 2) = (u16)g10;
    else LDSH(L_AS1 + (tid - 448) * 4) = (u16)pk(1.f + lo2f((u32)g10), 0.f);
    if (tid < 384) LDSH(L_AS1 + (64 + tid) * 4) = (u16)pk(1.f + lo2f((u32)g11), 0.f);
  }
  DRAIN();
  __syncthreads();
  if (tid < 8) __hip_atomic_store(gr0 + tid * G_REP + G_FILL + b, tag << 16, RLX_AGENT);
  STAMPT(3);
  const int jg = tid - 448;
  const int i20 = tid < 448 ? 1344 + tid : 896 + 56 * c8 + (jg < 56 ? jg : 0);
  const int i21 = tid < 448 ? 1792 + tid : 2240 + 56 * c8 + (jg < 56 ? jg : 0);
  u64 g20 = __hip_atomic_load(gr + i20, RLX_AGENT), g21 = __hip_atomic_load(gr + i21, RLX_AGENT);

  v4u hfr[14];
  {
    v4u as[3][2];
#pragma unroll
    for (int s = 0; s < 2; ++s) { as[s][0] = LDS4(L_AS1 + (32 * s + 8 * g) * 4); as[s][1] = LDS4(L_AS1 + (32 * s + 8 * g) * 4 + 16); }
#pragma unroll
    for (int s = 0; s < 14; ++s) {
      if (s < 12) { as[(s + 2) % 3][0] = LDS4(L_AS1 + (32 * (s + 2) + 8 * g) * 4); as[(s + 2) % 3][1] = LDS4(L_AS1 + (32 * (s + 2) + 8 * g) * 4 + 16); }
      SB0();
      hfr[s] = modulate(xh[s], as[s % 3][0], as[s % 3][1]);
      SB0();
    }
  }
  STAMPT(29);
  int dep = b;
  if (lane < 16) dep = xcd + 8 * (16 * (g16 & 1) + lane);
  else if (lane == 16) dep = g16 >= 1 ? ((g16 - 1) >> 1) + 8 * (16 * ((g16 - 1) & 1) + head) : b;
  else if (lane == 17) dep = g16 >= 2 ? ((g16 - 2) >> 1) + 8 * (16 * (g16 & 1) + head) : b;
  else if (lane < 32) dep = 14 * c8 + (lane - 18);
  else if (lane < 46) dep = ((896 + 98 * c8) >> 3) + (lane - 32);
  else if (lane < 50) dep = 210 + 4 * c8 + (lane - 46);
  u64 dfl = tag << 16;
  f4 aq[6];
#pragma unroll
  for (int t = 0; t < 6; ++t) aq[t] = (f4){0, 0, 0, 0};
  {
    v4u fb[3][6];
#define QLOAD(s, d) { _Pragma("unroll") for (int t = 0; t < 6; ++t) fb[d][t] = LDS4(L_WQ + ((t >> 1) * 14 + (s)) * 2048 + (t & 1) * 1024 + lane * 16); }
    QLOAD(0, 0)
    QLOAD(1, 1)
#pragma unroll
    for (int s = 0; s < 14; ++s) {
      if (s < 12) QLOAD(s + 2, (s + 2) % 3)
      if (s == 0) st4s(rws, lane * 16, pdst, pe);
      if (s == 2) st4s(rws, lane * 16, pdst + 1024, po);
      if (s == 4) st4s(rws, (w < 4 || lane < 48) ? lane * 16 : OOB, WS_X1 + (4 * g32 + (w & 3)) * 14336 + c8 * 1792 + (w >> 2) * 1024, SENT);
      if (s == 5) st4s(rws, (w < 4 && lane < 16) ? lane * 16 : OOB, WS_ST + (4 * g32 + (w & 3)) * 2048 + c8 * 256, SENT);
      if (s == 8 && w == 0) dfl = __hip_atomic_load(gr + G_FILL + (lane < 18 ? dep : b), RLX_AGENT);
      SB0();
#pragma unroll
      for (int t = 0; t < 4; ++t) aq[t] = mfma(fb[s % 3][t], hfr[s], aq[t]);
#pragma unroll
      for (int t = 4; t < 6; ++t) aq[t] = mfma(hfr[s], fb[s % 3][t], aq[t]);
      SB0();
    }
  }
  STAMPT(30);
  const v4u qf = {pk(aq[0][0], aq[0][1]), pk(aq[0][2], aq[0][3]), g < 3 ? pk(aq[1][0], aq[1][1]) : 0u, g < 3 ? pk(aq[1][2], aq[1][3]) : 0u};
  {
    const v4u kf = nosent4((v4u){pk(aq[2][0], aq[2][1]), pk(aq[2][2], aq[2][3]), pk(aq[3][0], aq[3][1]), pk(aq[3][2], aq[3][3])});
    v2u ve = {pk(aq[4][0], aq[4][1]), pk(aq[4][2], aq[4][3])}, vo_ = {pk(aq[5][0], aq[5][1]), pk(aq[5][2], aq[5][3])};
    if (l15 == 12) vo_ = (v2u){0x3F803F80u, 0x3F803F80u};
    ve.x = nosent(ve.x); ve.y = nosent(ve.y); vo_.x = nosent(vo_.x); vo_.y = nosent(vo_.y);
    {
      DRAIN();
      u32 old = 0;
      if (lane == 0) old = __hip_atomic_fetch_add((LAS u32*)(lds + L_RED + 3080), 1u, __ATOMIC_RELAXED, __HIP_MEMORY_SCOPE_WORKGROUP);
      if (__builtin_amdgcn_readfirstlane(old) == 7u && lane < 8) __hip_atomic_store(gr0 + lane * G_REP + G_PREP + b, tag << 16, RLX_AGENT);
    }
    st4s(rws, koff, kvb, kf);
    st2s(rws, voff, kvb + 8192, ve);
    st2s(rws, voff, kvb + 8192 + 1024, vo_);
    STAMPT(31);
    for (unsigned spins = 0;; ++spins) {
      bool ok = (g20 >> 16) == tag && (g21 >> 16) == tag;
      ok &= (dfl >> 16) == tag;
      if (__all(ok) || spins > SPIN_CAP) break;
      BACKOFF();
      if (w == 0) dfl = __hip_atomic_load(gr + G_FILL + (lane < 18 ? dep : b), RLX_AGENT);
      g20 = __hip_atomic_load(gr + i20, RLX_AGENT);
      g21 = __hip_atomic_load(gr + i21, RLX_AGENT);
    }
    RUNPRIO();
    if (tid < 448) {
      LDSH(L_SB + tid * 2) = (u16)g20;
      LDSH(L_A2 + tid * 2) = (u16)pk(1.f + lo2f((u32)g21), 0.f);
    } else {
      LDSF(L_GA + jg * 4) = lo2f((u32)g20);
      LDSF(L_GB + jg * 4) = lo2f((u32)g21);
    }
    STAMPT(21);
    __syncthreads();
    STAMPT(4);
    LDS4(L_K + 16384 + koff) = kf;
    LDS2(L_V + 16384 + voff) = ve;
    LDS2(L_V + 16384 + 1024 + voff) = vo_;
  }
  STAMPT(22);
  v4u w1f[14][2];
  const u32 vo1 = w < 7 ? lane * 16 : OOB;
  const v4u sws = mksrd(ws, WS_END);
  {
    const u32 b0 = g16 >= 2 ? WS_KV + ((g16 - 2) * 16 + head) * 16384 : OOB;
    const u32 b1 = g16 >= 1 ? WS_KV + ((g16 - 1) * 16 + head) * 16384 : OOB;
    v4u k0, v0, k1, v1;
    for (unsigned spins = 0;; ++spins) {
      CBAR();
      k0 = ld4s(rws, tid * 16, b0); v0 = ld4s(rws, tid * 16, b0 + 8192);
      k1 = ld4s(rws, tid * 16, b1); v1 = ld4s(rws, tid * 16, b1 + 8192);
      const bool bad = chk(chk(chk(chk(0u, k0), v0), k1), v1) == ~0u;
      if (!__any(bad) || spins > SPIN_CAP) break;
      {
        const u32 pb = (lane & 16) ? b1 : b0;
        canary(rws, lane < 32 ? pb + ((lane >> 3) & 1) * 8192 + w * 1024 + (lane & 7) * 128 : OOB, 0, spins);
      }
    }
    RUNPRIO();
    STAMPT(24);
    LDS4(L_K + tid * 16) = k0; LDS4(L_V + tid * 16) = v0;
    LDS4(L_K + 8192 + tid * 16) = k1; LDS4(L_V + 8192 + tid * 16) = v1;
  }
  if (w == 0) {
    for (unsigned spins = 0;; ++spins) {
      if (__all((__hip_atomic_load(gr + G_PREP + (lane >= 18 ? dep : b), RLX_AGENT) >> 16) == tag) || spins > SPIN_CAP) break;
      BACKOFF();
    }
    RUNPRIO();
  }
  DRAIN();
  __syncthreads();
  STAMPT(5);
  {
    const int blk0 = w >> 1;
    f4 sc[18];
    {
      v4u kf[18];
#pragma unroll
      for (int t = 0; t < 6; ++t) kf[t] = LDS4(L_K + (blk0 + (t >> 1)) * 2048 + (t & 1) * 1024 + lane * 16);
#pragma unroll
      for (int t = 0; t < 18; ++t) {
        if (t + 6 < 18) kf[t + 6] = LDS4(L_K + (blk0 + ((t + 6) >> 1)) * 2048 + (t & 1) * 1024 + lane * 16);
        if ((t & 1) && t < 16) dma16<true>(sws, lane * 16, WS_WOT + c8 * 65536 + (w * 8 + (t >> 1)) * 1024, L_WO + (w * 8 + (t >> 1)) * 1024);
        SB0();
        sc[t] = mfma(kf[t], qf, (f4){0, 0, 0, 0});
        SB0();
      }
    }
    STAMPT(25);
    v4u vf[18];
#pragma unroll
    for (int t = 0; t < 4; ++t) vf[t] = LDS4(L_V + (blk0 + (t >> 1)) * 2048 + (t & 1) * 1024 + lane * 16);
    const int ql = 256 + 16 * w + l15;
    const int kmin = (256 - 128 * g16) > (ql - 255) ? (256 - 128 * g16) : (ql - 255);
    const int kb0 = 32 * blk0 + 8 * g;
    const float NEG = -1e30f;
    if (g16 < 2) {
#pragma unroll
      for (int t = 2; t < 16; ++t)
#pragma unroll
        for (int r = 0; r < 4; ++r) sc[t][r] = (kb0 + 32 * (t >> 1) + 4 * (t & 1) + r) >= kmin ? sc[t][r] : NEG;
    }
#pragma unroll
    for (int t = 0; t < 2; ++t)
#pragma unroll
      for (int r = 0; r < 4; ++r) sc[t][r] = (kb0 + 4 * t + r) >= kmin ? sc[t][r] : NEG;
#pragma unroll
    for (int t = 16; t < 18; ++t)
#pragma unroll
      for (int r = 0; r < 4; ++r) sc[t][r] = (kb0 + 256 + 4 * (t & 1) + r) <= ql ? sc[t][r] : NEG;
    float mx = NEG;
#pragma unroll
    for (int t = 0; t < 18; ++t) mx = fmaxf(fmaxf(mx, sc[t][0]), fmaxf(sc[t][1], fmaxf(sc[t][2], sc[t][3])));
    mx = fmaxf(mx, __shfl_xor(mx, 16));
    mx = fmaxf(mx, __shfl_xor(mx, 32));
    STAMPT(26);
    STAMPT(18);
    const float cs = 0.18898223650461362f * 1.4426950408889634f;
    const float mc = mx * cs;
    f4 oe = {0, 0, 0, 0}, oo = {0, 0, 0, 0};
#pragma unroll
    for (int bb = 0; bb < 9; ++bb) {
      if (bb + 2 < 9) {
        vf[2 * bb + 4] = LDS4(L_V + (blk0 + bb + 2) * 2048 + lane * 16);
        vf[2 * bb + 5] = LDS4(L_V + (blk0 + bb + 2) * 2048 + 1024 + lane * 16);
      }
      w1f[bb >> 1][bb & 1] = ld4s(rws, vo1, WS_W1T + (7 * c8 + (w % 7)) * 28672 + bb * 1024);
      if ((bb & 1) == 0 && bb < 8) st4s(rws, vo1, WS_HID + (4 * g32 + (bb >> 1)) * 57344 + c8 * 7168 + (w % 7) * 1024, SENT);
      SB0();
      float p[8];
#pragma unroll
      for (int r = 0; r < 4; ++r) {
        p[r] = __builtin_amdgcn_exp2f(sc[2 * bb][r] * cs - mc);
        p[4 + r] = __builtin_amdgcn_exp2f(sc[2 * bb + 1][r] * cs - mc);
      }
      const v4u pf = {pk(p[0], p[1]), pk(p[2], p[3]), pk(p[4], p[5]), pk(p[6], p[7])};
      oe = mfma(vf[2 * bb], pf, oe);
      oo = mfma(vf[2 * bb + 1], pf, oo);
      SB0();
    }
    const float den = __shfl(oo[0], 48 + l15);
    const float inv = __builtin_amdgcn_rcpf(den);
    const v4u of = nosent4((v4u){pk(oe[0] * inv, oe[1] * inv), pk(oe[2] * inv, oe[3] * inv), pk(oo[0] * inv, oo[1] * inv), pk(oo[2] * inv, oo[3] * inv)});
    st4s(rws, lane * 16, WS_O + ((8 * g16 + w) * 16 + head) * 1024, of);
  }
  STAMPT(6);
  SB0();
  asm volatile("s_waitcnt vmcnt(14)" ::: "memory");
#pragma unroll
  for (int i = 9; i < 17; ++i) w1f[i >> 1][i & 1] = ld4s(rws, vo1, WS_W1T + (7 * c8 + (w % 7)) * 28672 + i * 1024);
  STAMPT(19);
  STAMPT(27);
  __syncthreads();
  STAMPT(28);

  {
    v4u of[8];
    const u32 base = WS_O + (4 * g32 + mtB) * 16384 + nhB * 8192;
    v4u wpre[4][4];
#pragma unroll
    for (int h = 0; h < 4; ++h)
#pragma unroll
      for (int t = 0; t < 4; ++t) { wpre[h][t] = LDS4(L_WO + ((t >> 1) * 16 + nhB * 8 + h) * 2048 + (t & 1) * 1024 + lane * 16); PIN(wpre[h][t]); }
    for (unsigned spins = 0;; ++spins) {
      CBAR();
      u32 m = 0;
#pragma unroll
      for (int h = 0; h < 8; ++h) of[h] = ld4s(rws, lane * 16, base + h * 1024);
#pragma unroll
      for (int h = 0; h < 8; ++h) m = chk(m, of[h]);
      if (!__any(m == ~0u) || spins > SPIN_CAP) break;
      canary(rws, lane < 8 ? lane * 1024 : OOB, base, spins);
    }
    RUNPRIO();
    DRAIN();
    STAMPT(7);
    f4 acc[4] = {{0, 0, 0, 0}, {0, 0, 0, 0}, {0, 0, 0, 0}, {0, 0, 0, 0}};
#pragma unroll
    for (int h = 0; h < 8; ++h) {
#pragma unroll
      for (int t = 0; t < 4; ++t) acc[t] = mfma(wpre[h & 3][t], of[h], acc[t]);
      SB0();
      if (h < 4) {
#pragma unroll
        for (int t = 0; t < 4; ++t) wpre[h][t] = LDS4(L_WO + ((t >> 1) * 16 + nhB * 8 + h + 4) * 2048 + (t & 1) * 1024 + lane * 16);
      }
      if (h >= 2) {
#pragma unroll
        for (int i = 17 + 2 * (h - 2); i < 19 + 2 * (h - 2) && i < 28; ++i) w1f[i >> 1][i & 1] = ld4s(rws, vo1, WS_W1T + (7 * c8 + (w % 7)) * 28672 + i * 1024);
      }
      SB0();
    }
    LDSF4(L_PR + w * 2048 + lane * 16) = nhB ? acc[0] : acc[2];
    LDSF4(L_PR + w * 2048 + 1024 + lane * 16) = nhB ? acc[1] : acc[3];
    __syncthreads();
    const f4 e = (nhB ? acc[2] : acc[0]) + LDSF4(L_PR + (w ^ 4) * 2048 + lane * 16);
    const f4 o = (nhB ? acc[3] : acc[1]) + LDSF4(L_PR + (w ^ 4) * 2048 + 1024 + lane * 16);
    const f4 g0 = LDSF4(L_GA + (32 * nhB + 8 * g) * 4), g1 = LDSF4(L_GA + (32 * nhB + 8 * g) * 4 + 16);
    const v4u a2 = LDS4(L_A2 + (56 * c8 + 32 * nhB + 8 * g) * 2);
    v4u x1;
    x1.x = pk(lo2f(xres.x) + g0.x * e[0], hi2f(xres.x) + g0.y * o[0]);
    x1.y = pk(lo2f(xres.y) + g0.z * e[1], hi2f(xres.y) + g0.w * o[1]);
    x1.z = pk(lo2f(xres.z) + g1.x * e[2], hi2f(xres.z) + g1.y * o[2]);
    x1.w = pk(lo2f(xres.w) + g1.z * e[3], hi2f(xres.w) + g1.w * o[3]);
    v4u y;
#pragma unroll
    for (int j = 0; j < 4; ++j) y[j] = nosent(pk(lo2f(x1[j]) * lo2f(a2[j]), hi2f(x1[j]) * hi2f(a2[j])));
    const bool vld = !(nhB == 1 && g == 3);
    st4s(rws, vld ? lane * 16 : OOB, WS_X1 + (4 * g32 + mtB) * 14336 + (7 * c8 + 4 * nhB) * 256, y);
    float s1 = 0.f, s2 = 0.f;
#pragma unroll
    for (int j = 0; j < 4; ++j) { s1 = dot2(x1[j], 0x3F803F80u, s1); s2 = dot2(x1[j], x1[j], s2); }
    s1 += __shfl_xor(s1, 16); s2 += __shfl_xor(s2, 16);
    s1 += __shfl_xor(s1, 32); s2 += __shfl_xor(s2, 32);
    st2s(rws, g == 0 ? l15 * 8 : OOB, WS_ST + (4 * g32 + mtB) * 2048 + (2 * c8 + nhB) * 128,
         (v2u){nosent(__builtin_bit_cast(u32, s1)), nosent(__builtin_bit_cast(u32, s2))});
    if (vld) LDS4(L_X1S + (mtB * 7 + 4 * nhB) * 256 + lane * 16) = x1;
  }
  STAMPT(8);
  f4 au[2] = {{0, 0, 0, 0}, {0, 0, 0, 0}}, av[2] = {{0, 0, 0, 0}, {0, 0, 0, 0}};
#pragma unroll
  for (int s = 0; s < 14; ++s) {
    const v4u a2 = LDS4(L_A2 + (32 * s + 8 * g) * 2), sb = LDS4(L_SB + (32 * s + 8 * g) * 2);
    au[0] = mfma(w1f[s][0], a2, au[0]); au[1] = mfma(w1f[s][1], a2, au[1]);
    av[0] = mfma(w1f[s][0], sb, av[0]); av[1] = mfma(w1f[s][1], sb, av[1]);
  }
  PIN(au[0]); PIN(au[1]); PIN(av[0]); PIN(av[1]);
  STAMPT(23);
  {
    v4u t[7], st[2];
    const u32 base = WS_X1 + 4 * g32 * 14336 + w * 7168;
    const u32 sto = w < 4 ? lane * 16 : OOB;
    for (unsigned spins = 0;; ++spins) {
      CBAR();
      u32 m = 0;
#pragma unroll
      for (int i = 0; i < 7; ++i) t[i] = ld4s(rws, lane * 16, base + i * 1024);
      st[0] = ld4s(rws, sto, WS_ST + (4 * g32 + (w & 3)) * 2048);
      st[1] = ld4s(rws, sto, WS_ST + (4 * g32 + (w & 3)) * 2048 + 1024);
#pragma unroll
      for (int i = 0; i < 7; ++i) m = chk(m, t[i]);
      m = chk(chk(m, st[0]), st[1]);
      if (!__any(m == ~0u) || spins > SPIN_CAP) break;
      canary(rws, lane < 28 ? base + lane * 256 : ((lane >= 32 && lane < 48 && w < 4) ? WS_ST + (4 * g32 + w) * 2048 + (lane - 32) * 128 : OOB), 0, spins);
    }
    RUNPRIO();
#pragma unroll
    for (int i = 0; i < 7; ++i) LDS4(w * 7168 + i * 1024 + lane * 16) = t[i];
    if (w < 4) {
      f4 a = __builtin_bit_cast(f4, st[0]) + __builtin_bit_cast(f4, st[1]);
#pragma unroll
      for (int j = 0; j < 4; ++j) { a[j] += dppf<0x128>(a[j]); a[j] += __shfl_xor(a[j], 16); a[j] += __shfl_xor(a[j], 32); }
      const float m0 = a.x * (1.f / D_), m1 = a.z * (1.f / D_);
      const float r0 = __builtin_amdgcn_rsqf(fmaxf(a.y * (1.f / D_) - m0 * m0, 0.f) + 1e-5f);
      const float r1 = __builtin_amdgcn_rsqf(fmaxf(a.w * (1.f / D_) - m1 * m1, 0.f) + 1e-5f);
      if (lane < 8) LDSF4(L_MR + w * 128 + lane * 16) = (f4){m0, r0, m1, r1};
    }
    __syncthreads();
  }
  STAMPT(9);
  v4u wf[7][4];
#pragma unroll
  for (int mp = 0; mp < 2; ++mp) {
    f4 acc[2][2] = {{{0, 0, 0, 0}, {0, 0, 0, 0}}, {{0, 0, 0, 0}, {0, 0, 0, 0}}};
    v4u xb[3][2];
#pragma unroll
    for (int m = 0; m < 2; ++m) { xb[0][m] = LDS4((2 * mp + m) * 14336 + lane * 16); xb[1][m] = LDS4((2 * mp + m) * 14336 + 1024 + lane * 16); }
#pragma unroll
    for (int s = 0; s < 14; ++s) {
      if (s < 12) {
#pragma unroll
        for (int m = 0; m < 2; ++m) xb[(s + 2) % 3][m] = LDS4((2 * mp + m) * 14336 + (s + 2) * 1024 + lane * 16);
      }
      SB0();
#pragma unroll
      for (int m = 0; m < 2; ++m) {
        acc[m][0] = mfma(w1f[s][0], xb[s % 3][m], acc[m][0]);
        acc[m][1] = mfma(w1f[s][1], xb[s % 3][m], acc[m][1]);
      }
      SB0();
      wf[(14 * mp + s) >> 2][(14 * mp + s) & 3] = ld4s(rws, lane * 16, WS_W2T + ((c8 * 56 + 7 * w) * 4 + 14 * mp + s) * 1024);
      SB0();
    }
#pragma unroll
    for (int m = 0; m < 2; ++m) {
      const f2 mr2 = *(LAS f2*)(lds + L_MR + ((2 * mp + m) * 16 + l15) * 8);
      const f2 nm = {-mr2.x, -mr2.x}, rs = {mr2.y, mr2.y};
      v4u hv;
#pragma unroll
      for (int r = 0; r < 4; ++r) {
        f2 h = __builtin_elementwise_fma(nm, (f2){au[0][r], au[1][r]}, (f2){acc[m][0][r], acc[m][1][r]});
        h = __builtin_elementwise_fma(rs, h, (f2){av[0][r], av[1][r]});
        hv[r] = pk(fmaxf(h.x, 0.f), fmaxf(h.y, 0.f));
      }
      st4s(rws, w < 7 ? lane * 16 : OOB, WS_HID + (4 * g32 + 2 * mp + m) * 57344 + (28 * c8 + 4 * (w % 7)) * 256, hv);
    }
    SB0();
  }
  STAMPT(10);
  __syncthreads();
  {
    v4u hf[2][2][7];
#define HLOAD(mp) { _Pragma("unroll") for (int i = 0; i < 2; ++i) _Pragma("unroll") for (int s = 0; s < 7; ++s) \
      hf[mp][i][s] = ld4s(rws, lane * 16, WS_HID + (4 * g32 + 2 * (mp) + i) * 57344 + w * 7168 + s * 1024); }
    HLOAD(0)
#pragma unroll
    for (int mp = 0; mp < 2; ++mp) {
      for (unsigned spins = 0;; ++spins) {
        u32 m = 0;
#pragma unroll
        for (int i = 0; i < 2; ++i)
#pragma unroll
          for (int s = 0; s < 7; ++s) m = chk(m, hf[mp][i][s]);
        if (!__any(m == ~0u) || spins > SPIN_CAP) break;
        canary(rws, (lane & 7) < 7 && lane < 16 ? (lane >> 3) * 57344 + (lane & 7) * 1024 : OOB, WS_HID + (4 * g32 + 2 * mp) * 57344 + w * 7168, spins);
        CBAR();
        HLOAD(mp)
      }
      RUNPRIO();
      if (mp == 0) STAMPT(11);
#pragma unroll
      for (int i = 0; i < 2; ++i) {
        f4 acc[4] = {{0, 0, 0, 0}, {0, 0, 0, 0}, {0, 0, 0, 0}, {0, 0, 0, 0}};
#pragma unroll
        for (int s = 0; s < 7; ++s)
#pragma unroll
          for (int t = 0; t < 4; ++t) acc[t] = mfma(wf[s][t], hf[mp][i][s], acc[t]);
#pragma unroll
        for (int r = 0; r < 4; ++r) LDSF4(((w * 4 + 2 * mp + i) * 4 + r) * 1024 + lane * 16) = (f4){acc[0][r], acc[1][r], acc[2][r], acc[3][r]};
        if (mp == 0 && i == 0) { SB0(); HLOAD(1) SB0(); }
      }
    }
  }
  __syncthreads();
  STAMPT(12);
  {
    const int mt = tid >> 7, rp = (tid >> 6) & 1;
    if (!(g == 3 && rp == 1)) {
      f4 s0 = {0, 0, 0, 0}, s1 = {0, 0, 0, 0};
#pragma unroll
      for (int ww = 0; ww < 8; ++ww) {
        s0 += LDSF4(((ww * 4 + mt) * 4 + 2 * rp) * 1024 + lane * 16);
        s1 += LDSF4(((ww * 4 + mt) * 4 + 2 * rp + 1) * 1024 + lane * 16);
      }
      const v4u x1 = LDS4(L_X1S + (mt * 7 + 2 * g + rp) * 256 + l15 * 16);
      const f4 g0 = LDSF4(L_GB + (16 * g + 8 * rp) * 4), g1 = LDSF4(L_GB + (16 * g + 8 * rp) * 4 + 16);
      v4u o;
      o.x = pk(lo2f(x1.x) + g0.x * s0.x, hi2f(x1.x) + g0.y * s0.y);
      o.y = pk(lo2f(x1.y) + g0.z * s0.z, hi2f(x1.y) + g0.w * s0.w);
      o.z = pk(lo2f(x1.z) + g1.x * s1.x, hi2f(x1.z) + g1.y * s1.y);
      o.w = pk(lo2f(x1.w) + g1.z * s1.z, hi2f(x1.w) + g1.w * s1.w);
      __builtin_nontemporal_store(o, (v4u*)(out + (size_t)(64 * g32 + 16 * mt + l15) * D_ + 56 * c8 + 16 * g + 8 * rp));
    }
  }
  if (b == 0 && w == 1) {
    for (unsigned spins = 0;; ++spins) {
      bool ok = true;
#pragma unroll
      for (int i = 0; i < 4; ++i) ok &= (__hip_atomic_load(gr + G_PREP + lane + 64 * i, RLX_AGENT) >> 16) == tag;
      if (__all(ok) || spins > SPIN_CAP) break;
      BACKOFF();
    }
    if (lane == 0) __hip_atomic_store((gu64*)ws, epoch + 1, RLX_AGENT);
  }
  STAMPT(13);
  asm volatile(".Lmega_end:");
}

extern "C" void kernel_launch(void* const* d_in, const int* in_sizes, int n_in, void* d_out, int out_size, void* d_ws,
                              size_t ws_size, hipStream_t stream) {
  (void)hipFuncSetAttribute((const void*)mega, hipFuncAttributeMaxDynamicSharedMemorySize, LDS_BYTES);
  mega<<<256, 512, LDS_BYTES, stream>>>((const u16*)d_in[0], (const u16*)d_in[1], (const u16*)d_in[2], (const u16*)d_in[4],
                                        (const u16*)d_in[5], (const u16*)d_in[6], (const u16*)d_in[7], (u16*)d_out, (char*)d_ws);
}
```

```cpp
#include <hip/hip_runtime.h>
#include <stdint.h>

typedef unsigned int u32;
typedef unsigned long long u64;
typedef unsigned short u16;
typedef u32 v4u __attribute__((ext_vector_type(4)));
typedef u32 v2u __attribute__((ext_vector_type(2)));
typedef float f4 __attribute__((ext_vector_type(4)));
typedef float f2 __attribute__((ext_vector_type(2)));
typedef __bf16 bf2v __attribute__((ext_vector_type(2)));
typedef __bf16 bf8v __attribute__((ext_vector_type(8)));
#define GAS __attribute__((address_space(1)))
#define LAS __attribute__((address_space(3)))
typedef GAS u64 gu64;
#define RLX_AGENT __ATOMIC_RELAXED, __HIP_MEMORY_SCOPE_AGENT
typedef __amdgpu_buffer_rsrc_t rsrc_t;

constexpr int S_ = 2048, D_ = 448, DFF_ = 1792;
constexpr int WS_GRAN = 4096;
constexpr int G_PREP = 2688, G_FILL = 2944;
constexpr int G_REP = 4096;
constexpr int WS_KV = 524288;
constexpr int WS_O = WS_KV + 4194304;
constexpr int WS_X1 = WS_O + 2097152;
constexpr int WS_ST = WS_X1 + 1835008;
constexpr int WS_HID = WS_ST + 262144;
constexpr int WS_W2T = WS_HID + 7340032;
constexpr int WS_W1T = WS_W2T + 1835008;
constexpr int WS_WOT = WS_W1T + 1605632;
constexpr int WS_END = WS_WOT + 524288;
constexpr int WS_DBG = WS_END;
constexpr int L_WQ = 0;
constexpr int L_K = 0, L_V = 24576;
constexpr int L_PR = 57344;
constexpr int L_WO = 86016;
constexpr int L_X = 36864;
constexpr int L_UV = L_WO;
constexpr int L_SM = 151552;
constexpr int L_AS1 = L_SM;
constexpr int L_A2 = L_AS1 + 1792;
constexpr int L_SB = L_A2 + 896;
constexpr int L_GA = L_SB + 896;
constexpr int L_GB = L_GA + 256;
constexpr int L_MR = L_GB + 256;
constexpr int L_RED = L_MR + 512;
constexpr int L_X1S = L_RED;
constexpr int L_END = L_X1S + 7168;
static_assert(L_END <= 163840, "lds");
constexpr int LDS_BYTES = 163840;
constexpr u32 OOB = 0x80000000u;
constexpr unsigned SPIN_CAP = 1u << 15;

__device__ __forceinline__ u32 pk(float lo, float hi) { f2 v = {lo, hi}; return __builtin_bit_cast(u32, __builtin_convertvector(v, bf2v)); }
__device__ __forceinline__ float dot2(u32 a, u32 b, float c) { return __builtin_amdgcn_fdot2_f32_bf16(__builtin_bit_cast(bf2v, a), __builtin_bit_cast(bf2v, b), c, false); }
__device__ __forceinline__ f4 mfma(v4u a, v4u b, f4 c) { return __builtin_amdgcn_mfma_f32_16x16x32_bf16(__builtin_bit_cast(bf8v, a), __builtin_bit_cast(bf8v, b), c, 0, 0, 0); }
__device__ __forceinline__ float lo2f(u32 v) { return __builtin_bit_cast(float, v << 16); }
__device__ __forceinline__ float hi2f(u32 v) { return __builtin_bit_cast(float, v & 0xffff0000u); }
__device__ __forceinline__ u32 plo(u32 r0, u32 r1) { return __builtin_amdgcn_perm(r1, r0, 0x05040100u); }
__device__ __forceinline__ u32 phi(u32 r0, u32 r1) { return __builtin_amdgcn_perm(r1, r0, 0x07060302u); }
__device__ __forceinline__ u32 nosent(u32 v) { return v < 0xFFFFFFFEu ? v : 0xFFFFFFFEu; }
__device__ __forceinline__ v4u nosent4(v4u v) { v.x = nosent(v.x); v.y = nosent(v.y); v.z = nosent(v.z); v.w = nosent(v.w); return v; }
__device__ __forceinline__ u32 umax3(u32 a, u32 b, u32 c) { u32 m = a > b ? a : b; return m > c ? m : c; }
__device__ __forceinline__ u32 chk(u32 m, v4u v) { return umax3(umax3(m, v.x, v.y), v.z, v.w); }
template <int C> __device__ __forceinline__ float dppf(float v) { return __builtin_bit_cast(float, __builtin_amdgcn_update_dpp(0, __builtin_bit_cast(int, v), C, 0xf, 0xf, false)); }
__device__ __forceinline__ float rowsum16(float v) { v += dppf<0xB1>(v); v += dppf<0x4E>(v); v += dppf<0x141>(v); v += dppf<0x140>(v); return v; }
__device__ __forceinline__ v4u ld4(rsrc_t r, u32 vo, u32 so) { return __builtin_amdgcn_raw_buffer_load_b128(r, vo, so, 0); }
__device__ __forceinline__ v4u ld4s(rsrc_t r, u32 vo, u32 so) { return __builtin_amdgcn_raw_buffer_load_b128(r, vo, so, 16); }
__device__ __forceinline__ v2u ld2(rsrc_t r, u32 vo, u32 so) { return __builtin_amdgcn_raw_buffer_load_b64(r, vo, so, 0); }
__device__ __forceinline__ u32 ld1(rsrc_t r, u32 vo, u32 so) { return __builtin_amdgcn_raw_buffer_load_b32(r, vo, so, 0); }
__device__ __forceinline__ void st4s(rsrc_t r, u32 vo, u32 so, v4u v) { __builtin_amdgcn_raw_buffer_store_b128(v, r, vo + so, 0, 16); }
__device__ __forceinline__ void st2s(rsrc_t r, u32 vo, u32 so, v2u v) { __builtin_amdgcn_raw_buffer_store_b64(v, r, vo, so, 16); }
__device__ __forceinline__ v4u modulate(const u32 (&xh)[8], v4u a0, v4u a1) {
  v4u h;
  float d0, d1, d2, d3, d4, d5, d6, d7;
  asm volatile(
      "v_dot2_f32_bf16 %4, %12, %20, 0\n\tv_dot2_f32_bf16 %5, %13, %21, 0\n\tv_dot2_f32_bf16 %6, %14, %22, 0\n\tv_dot2_f32_bf16 %7, %15, %23, 0\n\t"
      "v_dot2_f32_bf16 %8, %16, %24, 0\n\tv_dot2_f32_bf16 %9, %17, %25, 0\n\tv_dot2_f32_bf16 %10, %18, %26, 0\n\tv_dot2_f32_bf16 %11, %19, %27, 0\n\t"
      "v_cvt_pk_bf16_f32 %0, %4, %5\n\tv_cvt_pk_bf16_f32 %1, %6, %7\n\tv_cvt_pk_bf16_f32 %2, %8, %9\n\tv_cvt_pk_bf16_f32 %3, %10, %11\n\ts_nop 1"
      : "=&v"(h.x), "=&v"(h.y), "=&v"(h.z), "=&v"(h.w), "=&v"(d0), "=&v"(d1), "=&v"(d2), "=&v"(d3), "=&v"(d4), "=&v"(d5), "=&v"(d6), "=&v"(d7)
      : "v"(xh[0]), "v"(xh[1]), "v"(xh[2]), "v"(xh[3]), "v"(xh[4]), "v"(xh[5]), "v"(xh[6]), "v"(xh[7]),
        "v"(a0.x), "v"(a0.y), "v"(a0.z), "v"(a0.w), "v"(a1.x), "v"(a1.y), "v"(a1.z), "v"(a1.w));
  return h;
}
typedef short s4v __attribute__((ext_vector_type(4)));
__device__ __forceinline__ v2u trrd(LAS char* p) { return __builtin_bit_cast(v2u, __builtin_amdgcn_ds_read_tr16_b64_v4i16((LAS s4v*)p)); }
__device__ __forceinline__ v4u mksrd(const void* p, u32 bytes) {
  const u64 a = (u64)p;
  return (v4u){(u32)__builtin_amdgcn_readfirstlane((u32)a), (u32)__builtin_amdgcn_readfirstlane((u32)(a >> 32)) & 0xffffu, bytes, 0x00020000u};
}
template <bool SC1> __device__ __forceinline__ void dma16(v4u srd, u32 vo, u32 so, u32 ldsaddr) {
  u32 keep;
  so = __builtin_amdgcn_readfirstlane(so);
  ldsaddr = __builtin_amdgcn_readfirstlane(ldsaddr);
  if (SC1)
    asm volatile("s_mov_b32 %0, m0\n\ts_mov_b32 m0, %4\n\ts_nop 3\n\tbuffer_load_dwordx4 %1, %2, %3 offen sc1 lds\n\ts_mov_b32 m0, %0"
                 : "=&s"(keep) : "v"(vo), "s"(srd), "s"(so), "s"(ldsaddr) : "memory");
  else
    asm volatile("s_mov_b32 %0, m0\n\ts_mov_b32 m0, %4\n\ts_nop 3\n\tbuffer_load_dwordx4 %1, %2, %3 offen lds\n\ts_mov_b32 m0, %0"
                 : "=&s"(keep) : "v"(vo), "s"(srd), "s"(so), "s"(ldsaddr) : "memory");
}
__device__ __forceinline__ void canary(rsrc_t r, u32 vo, u32 so, unsigned& spins) {
  __builtin_amdgcn_s_setprio(0);
  for (; spins <= SPIN_CAP; ++spins) {
    __builtin_amdgcn_s_sleep(2);
    asm volatile("" ::: "memory");
    if (!__any(__builtin_amdgcn_raw_buffer_load_b32(r, vo, so, 16) == ~0u)) break;
  }
}
#define LDS4(off) (*(LAS v4u*)(lds + (off)))
#define LDS2(off) (*(LAS v2u*)(lds + (off)))
#define LDSF4(off) (*(LAS f4*)(lds + (off)))
#define LDSF(off) (*(LAS float*)(lds + (off)))
#define LDSH(off) (*(LAS u16*)(lds + (off)))
#define CBAR() asm volatile("" ::: "memory")
#define PIN(v) asm volatile("" : "+v"(v))
#define SB0() __builtin_amdgcn_sched_barrier(0)
#define BACKOFF() do { __builtin_amdgcn_s_setprio(0); __builtin_amdgcn_s_sleep(6); } while (0)
#define RUNPRIO() __builtin_amdgcn_s_setprio(1)
#define DRAIN() asm volatile("s_waitcnt vmcnt(0)" ::: "memory")
#define STAMPT(i)

__global__ __launch_bounds__(512, 2) void mega(const u16* __restrict__ x, const u16* __restrict__ wqkv, const u16* __restrict__ wo,
                                               const u16* __restrict__ w1, const u16* __restrict__ w2, const u16* __restrict__ cvec,
                                               const u16* __restrict__ wmod, u16* __restrict__ out, char* ws) {
  extern __shared__ __attribute__((aligned(16))) char lds_g[];
  LAS char* lds = (LAS char*)lds_g;
  const int tid = threadIdx.x, lane = tid & 63, w = __builtin_amdgcn_readfirstlane(tid >> 6) & 7;
  const int l15 = lane & 15, g = lane >> 4;
  const int b = blockIdx.x, xcd = b & 7, slot = b >> 3;
  const int g16 = xcd * 2 + (slot >> 4), head = slot & 15;
  const int g32 = xcd * 4 + (slot >> 3), c8 = slot & 7;
  const rsrc_t rws = __builtin_amdgcn_make_buffer_rsrc(ws, 0, WS_END, 0x00020000);
  const rsrc_t rx = __builtin_amdgcn_make_buffer_rsrc((void*)x, 0, S_ * D_ * 2, 0x00020000);
  const rsrc_t rwq = __builtin_amdgcn_make_buffer_rsrc((void*)wqkv, 0, D_ * 3 * D_ * 2, 0x00020000);
  const rsrc_t rwm = __builtin_amdgcn_make_buffer_rsrc((void*)wmod, 0, D_ * 6 * D_ * 2, 0x00020000);
  const rsrc_t rc = __builtin_amdgcn_make_buffer_rsrc((void*)cvec, 0, D_ * 2, 0x00020000);
  const v4u SENT = {~0u, ~0u, ~0u, ~0u};
  STAMPT(0);
  { const u64 t0 = __builtin_amdgcn_s_memrealtime(); if (tid == 0) __hip_atomic_store((gu64*)(ws + WS_END) + b, t0, RLX_AGENT); }
  RUNPRIO();
  u32 cpf, mpf;
  u64 epoch;
  {
    u64 pc;
    u32 len;
    asm volatile("s_getpc_b64 %0\n.Lmega_start:\n\ts_mov_b32 %1, .Lmega_end-.Lmega_start" : "=s"(pc), "=s"(len));
    const v4u scode = mksrd((const void*)pc, len), swm0 = mksrd(wmod, D_ * 6 * D_ * 2), sws0 = mksrd(ws, 64);
    const int idx = slot * 84 + tid - 320, n = idx / 448, row = idx - 448 * n, j = xcd + 8 * n;
    asm volatile("s_nop 4\n\tbuffer_load_dword %0, %3, %5, 0 offen\n\tbuffer_load_dword %1, %4, %6, 0 offen\n\tbuffer_load_dwordx2 %2, off, %7, 0 sc1"
                 : "=&v"(cpf), "=&v"(mpf), "=&v"(epoch)
                 : "v"(tid >= 448 ? (lane * 8 + (slot & 7)) * 128 : OOB), "v"((tid >= 320 && tid < 404 && j < 42) ? (u32)(row * 5376 + j * 128) : OOB),
                   "s"(scode), "s"(swm0), "s"(sws0)
                 : "memory");
  }
  gu64* gr0 = (gu64*)(ws + WS_GRAN);
  gu64* gr = gr0 + xcd * G_REP;
  const int mj = xcd + 8 * (slot / 3);
  const bool modw = (slot % 3 == 0) && mj < 42 && w < 4;
  if (tid < 4) *(volatile LAS u32*)(lds + L_RED + 3072 + tid * 4) = 0u;
  v2u mr[4][8];
  u32 craw;
  if (modw) {
    const v4u swm = mksrd(wmod, D_ * 6 * D_ * 2), sc_ = mksrd(cvec, D_ * 2);
    asm volatile("s_nop 4\n\tbuffer_load_dword %0, %1, %2, 0 offen" : "=&v"(craw) : "v"(w + 4 * g < 14 ? (32 * (w + 4 * g) + 2 * l15) * 2 : OOB), "s"(sc_) : "memory");
#pragma unroll
    for (int i = 0; i < 4; ++i) {
      const u32 vo = (i < 3 || w < 2) ? (u32)(8 * g * 2688 + 4 * l15) * 2 : OOB;
      const u32 so = (u32)((32 * ((w + 4 * i) % 14)) * 2688 + 64 * mj) * 2;
      asm volatile(
          "s_nop 4\n\t"
          "buffer_load_dwordx2 %0, %8, %9, %10 offen\n\tbuffer_load_dwordx2 %1, %8, %9, %11 offen\n\t"
          "buffer_load_dwordx2 %2, %8, %9, %12 offen\n\tbuffer_load_dwordx2 %3, %8, %9, %13 offen\n\t"
          "buffer_load_dwordx2 %4, %8, %9, %14 offen\n\tbuffer_load_dwordx2 %5, %8, %9, %15 offen\n\t"
          "buffer_load_dwordx2 %6, %8, %9, %16 offen\n\tbuffer_load_dwordx2 %7, %8, %9, %17 offen"
          : "=&v"(mr[i][0]), "=&v"(mr[i][1]), "=&v"(mr[i][2]), "=&v"(mr[i][3]), "=&v"(mr[i][4]), "=&v"(mr[i][5]), "=&v"(mr[i][6]), "=&v"(mr[i][7])
          : "v"(vo), "s"(swm), "s"(so), "s"(so + 5376), "s"(so + 2 * 5376), "s"(so + 3 * 5376), "s"(so + 4 * 5376), "s"(so + 5 * 5376),
            "s"(so + 6 * 5376), "s"(so + 7 * 5376)
          : "memory");
    }
  }
  asm volatile("s_waitcnt lgkmcnt(0)\n\ts_barrier" ::: "memory");
  STAMPT(14);
  const v4u sq_srd = mksrd(wqkv, D_ * 3 * D_ * 2);
  const u32 vq = (u32)((lane >> 2) * 2688 + (((lane & 3) ^ (2 * (lane >> 5))) * 16));
#define WQDMA(i)                                                                                                        \
  {                                                                                                                     \
    const int u = w + 8 * ((i) >> 1), d = 2 * u + ((i) & 1), mat = u / 14, kb = d - 28 * mat;                           \
    dma16<false>(sq_srd, u < 42 ? vq : OOB, (u32)(16 * kb * 2688 + (mat % 3) * 896 + 56 * head), u < 42 ? L_WQ + d * 1024 : L_RED + 4096); \
  }
  {
    const v4u sx = mksrd(x, S_ * D_ * 2);
    const u32 vo = (u32)((lane >> 3) * D_ * 2 + (((lane & 7) ^ (lane >> 3)) * 16));
#pragma unroll
    for (int q = 0; q < 14; ++q) dma16<false>(sx, vo, (u32)((128 * g16 + 16 * w + 8 * (q & 1)) * D_ * 2 + (q >> 1) * 128), L_X + w * 14336 + q * 1024);
  }
  asm volatile("s_barrier" ::: "memory");
  WQDMA(0) WQDMA(1) WQDMA(2) WQDMA(3)
  u32 pr[8];
  u32 pdst;
  {
    const int u = b * 8 + w;
    const u16* base; u32 bytes, ld, so, vo, vo2;
    if (u < 896) {
      const int cc = u / 112, v = u - 112 * cc, kb = v >> 1, hf = v & 1;
      base = w2; bytes = DFF_ * D_ * 2; ld = D_ * 2; so = (u32)(32 * kb * D_ + 56 * cc + 2 * hf) * 2;
      vo = l15 < 14 ? (u32)(8 * g * D_ + 4 * l15) * 2 : OOB; vo2 = vo;
      pdst = WS_W2T + ((cc * 56 + kb) * 4 + 2 * hf) * 1024;
    } else if (u < 1680) {
      const int v = u - 896, cb = v / 14, s = v - 14 * cb;
      base = w1; bytes = DFF_ * D_ * 2; ld = DFF_ * 2; so = (u32)(32 * s * DFF_ + 32 * cb) * 2;
      vo = (u32)(8 * g * DFF_ + 2 * l15) * 2; vo2 = vo;
      pdst = WS_W1T + (cb * 14 + s) * 2048;
    } else {
      const int v = u - 1680, hh = v & 15, nh = (v >> 4) & 1, cc = (v >> 5) & 7;
      base = wo; bytes = D_ * D_ * 2; ld = D_ * 2; so = (u32)(28 * hh * D_ + 56 * cc + 32 * nh) * 2;
      vo = (u < 1936 && (nh == 0 || l15 < 12)) ? (u32)(4 * g * D_ + 2 * l15) * 2 : OOB; vo2 = g < 3 ? vo + 12 * D_ * 2 : OOB;
      pdst = u < 1936 ? WS_WOT + (cc * 32 + nh * 16 + hh) * 2048 : OOB;
    }
    const v4u sp = mksrd(base, __builtin_amdgcn_readfirstlane(bytes));
    so = __builtin_amdgcn_readfirstlane(so); ld = __builtin_amdgcn_readfirstlane(ld);
    asm volatile(
        "s_nop 4\n\t"
        "buffer_load_dword %0, %8, %10, %11 offen\n\tbuffer_load_dword %1, %8, %10, %12 offen\n\t"
        "buffer_load_dword %2, %8, %10, %13 offen\n\tbuffer_load_dword %3, %8, %10, %14 offen\n\t"
        "buffer_load_dword %4, %9, %10, %15 offen\n\tbuffer_load_dword %5, %9, %10, %16 offen\n\t"
        "buffer_load_dword %6, %9, %10, %17 offen\n\tbuffer_load_dword %7, %9, %10, %18 offen"
        : "=&v"(pr[0]), "=&v"(pr[1]), "=&v"(pr[2]), "=&v"(pr[3]), "=&v"(pr[4]), "=&v"(pr[5]), "=&v"(pr[6]), "=&v"(pr[7])
        : "v"(vo), "v"(vo2), "s"(sp), "s"(so), "s"(so + ld), "s"(so + 2 * ld), "s"(so + 3 * ld), "s"(so + 4 * ld), "s"(so + 5 * ld), "s"(so + 6 * ld),
          "s"(so + 7 * ld)
        : "memory");
  }
  SB0();
  asm volatile("s_waitcnt vmcnt(26)" : "+v"(epoch), "+v"(cpf), "+v"(mpf)::"memory");
  const u64 tag = (epoch + 1) & 0xFFFFFFFFFFFFull;
  if (modw) {
    __builtin_amdgcn_s_setprio(3);
    asm volatile(""
                 : "+v"(craw), "+v"(mr[0][0]), "+v"(mr[0][1]), "+v"(mr[0][2]), "+v"(mr[0][3]), "+v"(mr[0][4]), "+v"(mr[0][5]), "+v"(mr[0][6]), "+v"(mr[0][7]),
                   "+v"(mr[1][0]), "+v"(mr[1][1]), "+v"(mr[1][2]), "+v"(mr[1][3]), "+v"(mr[1][4]), "+v"(mr[1][5]), "+v"(mr[1][6]), "+v"(mr[1][7])::"memory");
    asm volatile(""
                 : "+v"(mr[2][0]), "+v"(mr[2][1]), "+v"(mr[2][2]), "+v"(mr[2][3]), "+v"(mr[2][4]), "+v"(mr[2][5]), "+v"(mr[2][6]), "+v"(mr[2][7]),
                   "+v"(mr[3][0]), "+v"(mr[3][1]), "+v"(mr[3][2]), "+v"(mr[3][3]), "+v"(mr[3][4]), "+v"(mr[3][5]), "+v"(mr[3][6]), "+v"(mr[3][7])::"memory");
    float c0 = lo2f(craw), c1 = hi2f(craw);
    c0 = c0 * __builtin_amdgcn_rcpf(1.f + __expf(-c0));
    c1 = c1 * __builtin_amdgcn_rcpf(1.f + __expf(-c1));
    *(LAS u32*)(lds + L_RED + w * 256 + lane * 4) = pk(c0, c1);
    f4 acc[4] = {{0, 0, 0, 0}, {0, 0, 0, 0}, {0, 0, 0, 0}, {0, 0, 0, 0}};
#pragma unroll
    for (int i = 0; i < 4; ++i) {
      const v4u sf = LDS4(L_RED + w * 256 + i * 64 + g * 16);
      acc[0] = mfma((v4u){plo(mr[i][0].x, mr[i][1].x), plo(mr[i][2].x, mr[i][3].x), plo(mr[i][4].x, mr[i][5].x), plo(mr[i][6].x, mr[i][7].x)}, sf, acc[0]);
      acc[1] = mfma((v4u){phi(mr[i][0].x, mr[i][1].x), phi(mr[i][2].x, mr[i][3].x), phi(mr[i][4].x, mr[i][5].x), phi(mr[i][6].x, mr[i][7].x)}, sf, acc[1]);
      acc[2] = mfma((v4u){plo(mr[i][0].y, mr[i][1].y), plo(mr[i][2].y, mr[i][3].y), plo(mr[i][4].y, mr[i][5].y), plo(mr[i][6].y, mr[i][7].y)}, sf, acc[2]);
      acc[3] = mfma((v4u){phi(mr[i][0].y, mr[i][1].y), phi(mr[i][2].y, mr[i][3].y), phi(mr[i][4].y, mr[i][5].y), phi(mr[i][6].y, mr[i][7].y)}, sf, acc[3]);
    }
    if (l15 == 0) {
#pragma unroll
      for (int r = 0; r < 4; ++r) LDSF4(L_RED + 1024 + w * 256 + (16 * g + 4 * r) * 4) = (f4){acc[0][r], acc[1][r], acc[2][r], acc[3][r]};
    }
    asm volatile("s_waitcnt lgkmcnt(0)" ::: "memory");
    u32 old = 0;
    if (lane == 0) old = __hip_atomic_fetch_add((LAS u32*)(lds + L_RED + 3072), 1u, __ATOMIC_RELAXED, __HIP_MEMORY_SCOPE_WORKGROUP);
    if (__builtin_amdgcn_readfirstlane(old) == 3u) {
      CBAR();
      const float sum = (LDSF(L_RED + 1024 + lane * 4) + LDSF(L_RED + 1280 + lane * 4)) + (LDSF(L_RED + 1536 + lane * 4) + LDSF(L_RED + 1792 + lane * 4));
      const u64 gval = (tag << 16) | (u64)(pk(sum, 0.f) & 0xffffu);
#pragma unroll
      for (int r = 0; r < 8; ++r) __hip_atomic_store(gr0 + r * G_REP + 64 * mj + lane, gval, RLX_AGENT);
    }
    RUNPRIO();
  }
  STAMPT(15);
  if (modw) asm volatile("s_waitcnt vmcnt(8)" ::: "memory"); else asm volatile("s_waitcnt vmcnt(12)" ::: "memory");
  STAMPT(16);
  v4u xr[14];
  {
    const u32 ra0 = L_X + w * 14336 + (l15 >> 3) * 1024 + (l15 & 7) * 128 + ((g ^ (l15 & 7)) * 16);
    const u32 ra1 = L_X + w * 14336 + (l15 >> 3) * 1024 + (l15 & 7) * 128 + (((4 + g) ^ (l15 & 7)) * 16);
#pragma unroll
    for (int s = 0; s < 14; ++s) xr[s] = LDS4(((s & 1) ? ra1 : ra0) + (s >> 1) * 2048);
#pragma unroll
    for (int s = 0; s < 14; ++s) PIN(xr[s]);
  }
  asm volatile("s_waitcnt lgkmcnt(0)\n\ts_barrier" ::: "memory");
  STAMPT(17);
  const int mtB = w & 3, nhB = w >> 2;
  v4u xres;
  const int tau = 16 * (w & 1) + l15;
  const u32 koff = (w >> 1) * 2048 + ((tau >> 2) & 1) * 1024 + (g * 16 + 4 * (tau >> 3) + (tau & 3)) * 16;
  const u32 voff = (w >> 1) * 2048 + ((2 * (w & 1) + (g >> 1)) * 16 + l15) * 16 + (g & 1) * 8;
  const u32 kvb = g16 < 15 ? WS_KV + (g16 * 16 + head) * 16384 : OOB;
  u32 xh[14][8];
  {
    const v4u ONES = {0x3F803F80u, 0x3F803F80u, 0x3F803F80u, 0x3F803F80u};
    f4 sm = {0, 0, 0, 0}, gm = {0, 0, 0, 0};
#pragma unroll
    for (int s = 0; s < 14; ++s) {
      if (s == 0 || s == 7) WQDMA(4 + s / 7)
      sm = mfma(ONES, xr[s], sm);
      gm = mfma(xr[s], xr[s], gm);
    }
    float sq = (l15 & 2) ? ((l15 & 1) ? gm[3] : gm[2]) : ((l15 & 1) ? gm[1] : gm[0]);
    sq = __shfl(sq, 16 * (l15 >> 2) + l15);
    const float mean = sm[0] * (1.f / D_);
    const float rstd = __builtin_amdgcn_rsqf(fmaxf(sq * (1.f / D_) - mean * mean, 0.f) + 1e-5f);
    const f2 rs2 = {rstd, rstd}, nm2 = {-mean * rstd, -mean * rstd};
#pragma unroll
    for (int s = 0; s < 14; ++s) {
      if (s < 6) WQDMA(6 + s)
      else if (s == 7) xres = ld4(rx, (nhB == 1 && g == 3) ? OOB : (u32)(l15 * D_ + 8 * g) * 2, (u32)((64 * g32 + 16 * mtB) * D_ + 56 * c8 + 32 * nhB) * 2);
      else if (s == 9) st4s(rws, koff, kvb, SENT);
      else if (s == 11) st4s(rws, (w >> 1) * 2048 + (w & 1) * 512 + (lane & 31) * 16 + (lane >> 5) * 1024, kvb + 8192, SENT);
      else if (s == 13) st4s(rws, lane * 16, WS_O + ((8 * g16 + w) * 16 + head) * 1024, SENT);
#pragma unroll
      for (int j = 0; j < 4; ++j) {
        const f2 v = __builtin_elementwise_fma((f2){lo2f(xr[s][j]), hi2f(xr[s][j])}, rs2, nm2);
        xh[s][2 * j] = pk(v.x, 1.f);
        xh[s][2 * j + 1] = pk(v.y, 1.f);
        PIN(xh[s][2 * j]); PIN(xh[s][2 * j + 1]);
      }
      SB0();
    }
  }
  STAMPT(20);
  const int i1 = tid < 384 ? 512 + tid : tid;
  u64 g10 = __hip_atomic_load(gr + tid, RLX_AGENT), g11 = __hip_atomic_load(gr + i1, RLX_AGENT);
  asm volatile("s_waitcnt vmcnt(6)"
               : "+v"(pr[0]), "+v"(pr[1]), "+v"(pr[2]), "+v"(pr[3]), "+v"(pr[4]), "+v"(pr[5]), "+v"(pr[6]), "+v"(pr[7])::"memory");
  {
    LAS char* tb0 = lds + L_WQ + (8 * g + (l15 >> 2)) * 64 + (g & 1) * 32 + (l15 & 3) * 8;
    LAS char* tb1 = lds + L_WQ + (8 * g + (l15 >> 2)) * 64 + ((g & 1) ^ 1) * 32 + (l15 & 3) * 8;
    v2u r[6][4];
#pragma unroll
    for (int i = 0; i < 6; ++i) {
      const int u = (i < 5 || w < 2) ? w + 8 * i : w;
      r[i][0] = trrd(tb0 + u * 2048); r[i][1] = trrd(tb0 + u * 2048 + 256);
      r[i][2] = trrd(tb1 + u * 2048); r[i][3] = trrd(tb1 + u * 2048 + 256);
    }
    SB0();
#pragma unroll
    for (int i = 0; i < 6; ++i) {
      const int u = (i < 5 || w < 2) ? w + 8 * i : w;
      LDS4(L_WQ + u * 2048 + lane * 16) = (v4u){r[i][0].x, r[i][0].y, r[i][1].x, r[i][1].y};
      LDS4(L_WQ + u * 2048 + 1024 + lane * 16) = (v4u){r[i][2].x, r[i][2].y, r[i][3].x, r[i][3].y};
    }
  }
  v4u pe = {plo(pr[0], pr[1]), plo(pr[2], pr[3]), plo(pr[4], pr[5]), plo(pr[6], pr[7])};
  v4u po = {phi(pr[0], pr[1]), phi(pr[2], pr[3]), phi(pr[4], pr[5]), phi(pr[6], pr[7])};
  PIN(pe); PIN(po);
  STAMPT(2);
  {
    for (unsigned spins = 0;; ++spins) {
      const bool ok = (g10 >> 16) == tag && (g11 >> 16) == tag;
      if (__all(ok) || spins > SPIN_CAP) break;
      BACKOFF();
      g10 = __hip_atomic_load(gr + tid, RLX_AGENT);
      g11 = __hip_atomic_load(gr + i1, RLX_AGENT);
    }
    RUNPRIO();
    if (tid < 448) LDSH(L_AS1 + tid * 4 + 2) = (u16)g10;
    else LDSH(L_AS1 + (tid - 448) * 4) = (u16)pk(1.f + lo2f((u32)g10), 0.f);
    if (tid < 384) LDSH(L_AS1 + (64 + tid) * 4) = (u16)pk(1.f + lo2f((u32)g11), 0.f);
  }
  DRAIN();
  __syncthreads();
  if (tid < 8) __hip_atomic_store(gr0 + tid * G_REP + G_FILL + b, tag << 16, RLX_AGENT);
  STAMPT(3);
  const int jg = tid - 448;
  const int i20 = tid < 448 ? 1344 + tid : 896 + 56 * c8 + (jg < 56 ? jg : 0);
  const int i21 = tid < 448 ? 1792 + tid : 2240 + 56 * c8 + (jg < 56 ? jg : 0);
  u64 g20 = __hip_atomic_load(gr + i20, RLX_AGENT), g21 = __hip_atomic_load(gr + i21, RLX_AGENT);

  v4u hfr[14];
  {
    v4u as[3][2];
#pragma unroll
    for (int s = 0; s < 2; ++s) { as[s][0] = LDS4(L_AS1 + (32 * s + 8 * g) * 4); as[s][1] = LDS4(L_AS1 + (32 * s + 8 * g) * 4 + 16); }
#pragma unroll
    for (int s = 0; s < 14; ++s) {
      if (s < 12) { as[(s + 2) % 3][0] = LDS4(L_AS1 + (32 * (s + 2) + 8 * g) * 4); as[(s + 2) % 3][1] = LDS4(L_AS1 + (32 * (s + 2) + 8 * g) * 4 + 16); }
      SB0();
      hfr[s] = modulate(xh[s], as[s % 3][0], as[s % 3][1]);
      SB0();
    }
  }
  STAMPT(29);
  int dep = b;
  if (lane < 16) dep = xcd + 8 * (16 * (g16 & 1) + lane);
  else if (lane == 16) dep = g16 >= 1 ? ((g16 - 1) >> 1) + 8 * (16 * ((g16 - 1) & 1) + head) : b;
  else if (lane == 17) dep = g16 >= 2 ? ((g16 - 2) >> 1) + 8 * (16 * (g16 & 1) + head) : b;
  else if (lane < 32) dep = 14 * c8 + (lane - 18);
  else if (lane < 46) dep = ((896 + 98 * c8) >> 3) + (lane - 32);
  else if (lane < 50) dep = 210 + 4 * c8 + (lane - 46);
  u64 dfl = tag << 16;
  f4 aq[6];
#pragma unroll
  for (int t = 0; t < 6; ++t) aq[t] = (f4){0, 0, 0, 0};
  {
    v4u fb[3][6];
#define QLOAD(s, d) { _Pragma("unroll") for (int t = 0; t < 6; ++t) fb[d][t] = LDS4(L_WQ + ((t >> 1) * 14 + (s)) * 2048 + (t & 1) * 1024 + lane * 16); }
    QLOAD(0, 0)
    QLOAD(1, 1)
#pragma unroll
    for (int s = 0; s < 14; ++s) {
      if (s < 12) QLOAD(s + 2, (s + 2) % 3)
      if (s == 0) st4s(rws, lane * 16, pdst, pe);
      if (s == 2) st4s(rws, lane * 16, pdst + 1024, po);
      if (s == 4) st4s(rws, (w < 4 || lane < 48) ? lane * 16 : OOB, WS_X1 + (4 * g32 + (w & 3)) * 14336 + c8 * 1792 + (w >> 2) * 1024, SENT);
      if (s == 5) st4s(rws, (w < 4 && lane < 16) ? lane * 16 : OOB, WS_ST + (4 * g32 + (w & 3)) * 2048 + c8 * 256, SENT);
      if (s == 8 && w == 0) dfl = __hip_atomic_load(gr + G_FILL + (lane < 18 ? dep : b), RLX_AGENT);
      SB0();
#pragma unroll
      for (int t = 0; t < 4; ++t) aq[t] = mfma(fb[s % 3][t], hfr[s], aq[t]);
#pragma unroll
      for (int t = 4; t < 6; ++t) aq[t] = mfma(hfr[s], fb[s % 3][t], aq[t]);
      SB0();
    }
  }
  STAMPT(30);
  const v4u qf = {pk(aq[0][0], aq[0][1]), pk(aq[0][2], aq[0][3]), g < 3 ? pk(aq[1][0], aq[1][1]) : 0u, g < 3 ? pk(aq[1][2], aq[1][3]) : 0u};
  {
    const v4u kf = nosent4((v4u){pk(aq[2][0], aq[2][1]), pk(aq[2][2], aq[2][3]), pk(aq[3][0], aq[3][1]), pk(aq[3][2], aq[3][3])});
    v2u ve = {pk(aq[4][0], aq[4][1]), pk(aq[4][2], aq[4][3])}, vo_ = {pk(aq[5][0], aq[5][1]), pk(aq[5][2], aq[5][3])};
    if (l15 == 12) vo_ = (v2u){0x3F803F80u, 0x3F803F80u};
    ve.x = nosent(ve.x); ve.y = nosent(ve.y); vo_.x = nosent(vo_.x); vo_.y = nosent(vo_.y);
    {
      DRAIN();
      u32 old = 0;
      if (lane == 0) old = __hip_atomic_fetch_add((LAS u32*)(lds + L_RED + 3080), 1u, __ATOMIC_RELAXED, __HIP_MEMORY_SCOPE_WORKGROUP);
      if (__builtin_amdgcn_readfirstlane(old) == 7u && lane < 8) __hip_atomic_store(gr0 + lane * G_REP + G_PREP + b, tag << 16, RLX_AGENT);
    }
    st4s(rws, koff, kvb, kf);
    st2s(rws, voff, kvb + 8192, ve);
    st2s(rws, voff, kvb + 8192 + 1024, vo_);
    STAMPT(31);
    for (unsigned spins = 0;; ++spins) {
      bool ok = (g20 >> 16) == tag && (g21 >> 16) == tag;
      ok &= (dfl >> 16) == tag;
      if (__all(ok) || spins > SPIN_CAP) break;
      BACKOFF();
      if (w == 0) dfl = __hip_atomic_load(gr + G_FILL + (lane < 18 ? dep : b), RLX_AGENT);
      g20 = __hip_atomic_load(gr + i20, RLX_AGENT);
      g21 = __hip_atomic_load(gr + i21, RLX_AGENT);
    }
    RUNPRIO();
    if (tid < 448) {
      LDSH(L_SB + tid * 2) = (u16)g20;
      LDSH(L_A2 + tid * 2) = (u16)pk(1.f + lo2f((u32)g21), 0.f);
    } else {
      LDSF(L_GA + jg * 4) = lo2f((u32)g20);
      LDSF(L_GB + jg * 4) = lo2f((u32)g21);
    }
    STAMPT(21);
    __syncthreads();
    STAMPT(4);
    LDS4(L_K + 16384 + koff) = kf;
    LDS2(L_V + 16384 + voff) = ve;
    LDS2(L_V + 16384 + 1024 + voff) = vo_;
  }
  STAMPT(22);
  v4u w1f[14][2];
  const u32 vo1 = w < 7 ? lane * 16 : OOB;
  const v4u sws = mksrd(ws, WS_END);
  {
    const u32 b0 = g16 >= 2 ? WS_KV + ((g16 - 2) * 16 + head) * 16384 : OOB;
    const u32 b1 = g16 >= 1 ? WS_KV + ((g16 - 1) * 16 + head) * 16384 : OOB;
    v4u k0, v0, k1, v1;
    for (unsigned spins = 0;; ++spins) {
      CBAR();
      k0 = ld4s(rws, tid * 16, b0); v0 = ld4s(rws, tid * 16, b0 + 8192);
      k1 = ld4s(rws, tid * 16, b1); v1 = ld4s(rws, tid * 16, b1 + 8192);
      const bool bad = chk(chk(chk(chk(0u, k0), v0), k1), v1) == ~0u;
      if (!__any(bad) || spins > SPIN_CAP) break;
      {
        const u32 pb = (lane & 16) ? b1 : b0;
        canary(rws, lane < 32 ? pb + ((lane >> 3) & 1) * 8192 + w * 1024 + (lane & 7) * 128 : OOB, 0, spins);
      }
    }
    RUNPRIO();
    STAMPT(24);
    LDS4(L_K + tid * 16) = k0; LDS4(L_V + tid * 16) = v0;
    LDS4(L_K + 8192 + tid * 16) = k1; LDS4(L_V + 8192 + tid * 16) = v1;
  }
  if (w == 0) {
    for (unsigned spins = 0;; ++spins) {
      if (__all((__hip_atomic_load(gr + G_PREP + (lane >= 18 ? dep : b), RLX_AGENT) >> 16) == tag) || spins > SPIN_CAP) break;
      BACKOFF();
    }
    RUNPRIO();
  }
  DRAIN();
  __syncthreads();
  STAMPT(5);
  {
    const int blk0 = w >> 1;
    f4 sc[18];
    {
      v4u kf[18];
#pragma unroll
      for (int t = 0; t < 6; ++t) kf[t] = LDS4(L_K + (blk0 + (t >> 1)) * 2048 + (t & 1) * 1024 + lane * 16);
#pragma unroll
      for (int t = 0; t < 18; ++t) {
        if (t + 6 < 18) kf[t + 6] = LDS4(L_K + (blk0 + ((t + 6) >> 1)) * 2048 + (t & 1) * 1024 + lane * 16);
        if ((t & 1) && t < 16) dma16<true>(sws, lane * 16, WS_WOT + c8 * 65536 + (w * 8 + (t >> 1)) * 1024, L_WO + (w * 8 + (t >> 1)) * 1024);
        SB0();
        sc[t] = mfma(kf[t], qf, (f4){0, 0, 0, 0});
        SB0();
      }
    }
    STAMPT(25);
    v4u vf[18];
#pragma unroll
    for (int t = 0; t < 4; ++t) vf[t] = LDS4(L_V + (blk0 + (t >> 1)) * 2048 + (t & 1) * 1024 + lane * 16);
    const int ql = 256 + 16 * w + l15;
    const int kmin = (256 - 128 * g16) > (ql - 255) ? (256 - 128 * g16) : (ql - 255);
    const int kb0 = 32 * blk0 + 8 * g;
    const float NEG = -1e30f;
    if (g16 < 2) {
#pragma unroll
      for (int t = 2; t < 16; ++t)
#pragma unroll
        for (int r = 0; r < 4; ++r) sc[t][r] = (kb0 + 32 * (t >> 1) + 4 * (t & 1) + r) >= kmin ? sc[t][r] : NEG;
    }
#pragma unroll
    for (int t = 0; t < 2; ++t)
#pragma unroll
      for (int r = 0; r < 4; ++r) sc[t][r] = (kb0 + 4 * t + r) >= kmin ? sc[t][r] : NEG;
#pragma unroll
    for (int t = 16; t < 18; ++t)
#pragma unroll
      for (int r = 0; r < 4; ++r) sc[t][r] = (kb0 + 256 + 4 * (t & 1) + r) <= ql ? sc[t][r] : NEG;
    float mx = NEG;
#pragma unroll
    for (int t = 0; t < 18; ++t) mx = fmaxf(fmaxf(mx, sc[t][0]), fmaxf(sc[t][1], fmaxf(sc[t][2], sc[t][3])));
    mx = fmaxf(mx, __shfl_xor(mx, 16));
    mx = fmaxf(mx, __shfl_xor(mx, 32));
    STAMPT(26);
    STAMPT(18);
    const float cs = 0.18898223650461362f * 1.4426950408889634f;
    const float mc = mx * cs;
    f4 oe = {0, 0, 0, 0}, oo = {0, 0, 0, 0};
#pragma unroll
    for (int bb = 0; bb < 9; ++bb) {
      if (bb + 2 < 9) {
        vf[2 * bb + 4] = LDS4(L_V + (blk0 + bb + 2) * 2048 + lane * 16);
        vf[2 * bb + 5] = LDS4(L_V + (blk0 + bb + 2) * 2048 + 1024 + lane * 16);
      }
      w1f[bb >> 1][bb & 1] = ld4s(rws, vo1, WS_W1T + (7 * c8 + (w % 7)) * 28672 + bb * 1024);
      if ((bb & 1) == 0 && bb < 8) st4s(rws, vo1, WS_HID + (4 * g32 + (bb >> 1)) * 57344 + c8 * 7168 + (w % 7) * 1024, SENT);
      SB0();
      float p[8];
#pragma unroll
      for (int r = 0; r < 4; ++r) {
        p[r] = __builtin_amdgcn_exp2f(sc[2 * bb][r] * cs - mc);
        p[4 + r] = __builtin_amdgcn_exp2f(sc[2 * bb + 1][r] * cs - mc);
      }
      const v4u pf = {pk(p[0], p[1]), pk(p[2], p[3]), pk(p[4], p[5]), pk(p[6], p[7])};
      oe = mfma(vf[2 * bb], pf, oe);
      oo = mfma(vf[2 * bb + 1], pf, oo);
      SB0();
    }
    const float den = __shfl(oo[0], 48 + l15);
    const float inv = __builtin_amdgcn_rcpf(den);
    const v4u of = nosent4((v4u){pk(oe[0] * inv, oe[1] * inv), pk(oe[2] * inv, oe[3] * inv), pk(oo[0] * inv, oo[1] * inv), pk(oo[2] * inv, oo[3] * inv)});
    st4s(rws, lane * 16, WS_O + ((8 * g16 + w) * 16 + head) * 1024, of);
  }
  STAMPT(6);
  SB0();
  asm volatile("s_waitcnt vmcnt(14)" ::: "memory");
#pragma unroll
  for (int i = 9; i < 17; ++i) w1f[i >> 1][i & 1] = ld4s(rws, vo1, WS_W1T + (7 * c8 + (w % 7)) * 28672 + i * 1024);
  STAMPT(19);
  STAMPT(27);
  __syncthreads();
  STAMPT(28);

  {
    v4u of[8];
    const u32 base = WS_O + (4 * g32 + mtB) * 16384 + nhB * 8192;
    v4u wpre[4][4];
#pragma unroll
    for (int h = 0; h < 4; ++h)
#pragma unroll
      for (int t = 0; t < 4; ++t) { wpre[h][t] = LDS4(L_WO + ((t >> 1) * 16 + nhB * 8 + h) * 2048 + (t & 1) * 1024 + lane * 16); PIN(wpre[h][t]); }
    for (unsigned spins = 0;; ++spins) {
      CBAR();
      u32 m = 0;
#pragma unroll
      for (int h = 0; h < 8; ++h) of[h] = ld4s(rws, lane * 16, base + h * 1024);
#pragma unroll
      for (int h = 0; h < 8; ++h) m = chk(m, of[h]);
      if (!__any(m == ~0u) || spins > SPIN_CAP) break;
      canary(rws, lane < 8 ? lane * 1024 : OOB, base, spins);
    }
    RUNPRIO();
    DRAIN();
    STAMPT(7);
    f4 acc[4] = {{0, 0, 0, 0}, {0, 0, 0, 0}, {0, 0, 0, 0}, {0, 0, 0, 0}};
#pragma unroll
    for (int h = 0; h < 8; ++h) {
#pragma unroll
      for (int t = 0; t < 4; ++t) acc[t] = mfma(wpre[h & 3][t], of[h], acc[t]);
      SB0();
      if (h < 4) {
#pragma unroll
        for (int t = 0; t < 4; ++t) wpre[h][t] = LDS4(L_WO + ((t >> 1) * 16 + nhB * 8 + h + 4) * 2048 + (t & 1) * 1024 + lane * 16);
      }
      if (h >= 2) {
#pragma unroll
        for (int i = 17 + 2 * (h - 2); i < 19 + 2 * (h - 2) && i < 28; ++i) w1f[i >> 1][i & 1] = ld4s(rws, vo1, WS_W1T + (7 * c8 + (w % 7)) * 28672 + i * 1024);
      }
      SB0();
    }
    LDSF4(L_PR + w * 2048 + lane * 16) = nhB ? acc[0] : acc[2];
    LDSF4(L_PR + w * 2048 + 1024 + lane * 16) = nhB ? acc[1] : acc[3];
    __syncthreads();
    const f4 e = (nhB ? acc[2] : acc[0]) + LDSF4(L_PR + (w ^ 4) * 2048 + lane * 16);
    const f4 o = (nhB ? acc[3] : acc[1]) + LDSF4(L_PR + (w ^ 4) * 2048 + 1024 + lane * 16);
    const f4 g0 = LDSF4(L_GA + (32 * nhB + 8 * g) * 4), g1 = LDSF4(L_GA + (32 * nhB + 8 * g) * 4 + 16);
    const v4u a2 = LDS4(L_A2 + (56 * c8 + 32 * nhB + 8 * g) * 2);
    v4u x1;
    x1.x = pk(lo2f(xres.x) + g0.x * e[0], hi2f(xres.x) + g0.y * o[0]);
    x1.y = pk(lo2f(xres.y) + g0.z * e[1], hi2f(xres.y) + g0.w * o[1]);
    x1.z = pk(lo2f(xres.z) + g1.x * e[2], hi2f(xres.z) + g1.y * o[2]);
    x1.w = pk(lo2f(xres.w) + g1.z * e[3], hi2f(xres.w) + g1.w * o[3]);
    v4u y;
#pragma unroll
    for (int j = 0; j < 4; ++j) y[j] = nosent(pk(lo2f(x1[j]) * lo2f(a2[j]), hi2f(x1[j]) * hi2f(a2[j])));
    const bool vld = !(nhB == 1 && g == 3);
    st4s(rws, vld ? lane * 16 : OOB, WS_X1 + (4 * g32 + mtB) * 14336 + (7 * c8 + 4 * nhB) * 256, y);
    float s1 = 0.f, s2 = 0.f;
#pragma unroll
    for (int j = 0; j < 4; ++j) { s1 = dot2(x1[j], 0x3F803F80u, s1); s2 = dot2(x1[j], x1[j], s2); }
    s1 += __shfl_xor(s1, 16); s2 += __shfl_xor(s2, 16);
    s1 += __shfl_xor(s1, 32); s2 += __shfl_xor(s2, 32);
    st2s(rws, g == 0 ? l15 * 8 : OOB, WS_ST + (4 * g32 + mtB) * 2048 + (2 * c8 + nhB) * 128,
         (v2u){nosent(__builtin_bit_cast(u32, s1)), nosent(__builtin_bit_cast(u32, s2))});
    if (vld) LDS4(L_X1S + (mtB * 7 + 4 * nhB) * 256 + lane * 16) = x1;
  }
  STAMPT(8);
  f4 au[2] = {{0, 0, 0, 0}, {0, 0, 0, 0}}, av[2] = {{0, 0, 0, 0}, {0, 0, 0, 0}};
#pragma unroll
  for (int s = 0; s < 14; ++s) {
    const v4u a2 = LDS4(L_A2 + (32 * s + 8 * g) * 2), sb = LDS4(L_SB + (32 * s + 8 * g) * 2);
    au[0] = mfma(w1f[s][0], a2, au[0]); au[1] = mfma(w1f[s][1], a2, au[1]);
    av[0] = mfma(w1f[s][0], sb, av[0]); av[1] = mfma(w1f[s][1], sb, av[1]);
  }
  PIN(au[0]); PIN(au[1]); PIN(av[0]); PIN(av[1]);
  STAMPT(23);
  {
    v4u t[7], st[2];
    const u32 base = WS_X1 + 4 * g32 * 14336 + w * 7168;
    const u32 sto = w < 4 ? lane * 16 : OOB;
    for (unsigned spins = 0;; ++spins) {
      CBAR();
      u32 m = 0;
#pragma unroll
      for (int i = 0; i < 7; ++i) t[i] = ld4s(rws, lane * 16, base + i * 1024);
      st[0] = ld4s(rws, sto, WS_ST + (4 * g32 + (w & 3)) * 2048);
      st[1] = ld4s(rws, sto, WS_ST + (4 * g32 + (w & 3)) * 2048 + 1024);
#pragma unroll
      for (int i = 0; i < 7; ++i) m = chk(m, t[i]);
      m = chk(chk(m, st[0]), st[1]);
      if (!__any(m == ~0u) || spins > SPIN_CAP) break;
      canary(rws, lane < 28 ? base + lane * 256 : ((lane >= 32 && lane < 48 && w < 4) ? WS_ST + (4 * g32 + w) * 2048 + (lane - 32) * 128 : OOB), 0, spins);
    }
    RUNPRIO();
#pragma unroll
    for (int i = 0; i < 7; ++i) LDS4(w * 7168 + i * 1024 + lane * 16) = t[i];
    if (w < 4) {
      f4 a = __builtin_bit_cast(f4, st[0]) + __builtin_bit_cast(f4, st[1]);
#pragma unroll
      for (int j = 0; j < 4; ++j) { a[j] += dppf<0x128>(a[j]); a[j] += __shfl_xor(a[j], 16); a[j] += __shfl_xor(a[j], 32); }
      const float m0 = a.x * (1.f / D_), m1 = a.z * (1.f / D_);
      const float r0 = __builtin_amdgcn_rsqf(fmaxf(a.y * (1.f / D_) - m0 * m0, 0.f) + 1e-5f);
      const float r1 = __builtin_amdgcn_rsqf(fmaxf(a.w * (1.f / D_) - m1 * m1, 0.f) + 1e-5f);
      if (lane < 8) LDSF4(L_MR + w * 128 + lane * 16) = (f4){m0, r0, m1, r1};
    }
    __syncthreads();
  }
  STAMPT(9);
  v4u wf[7][4];
#pragma unroll
  for (int mp = 0; mp < 2; ++mp) {
    f4 acc[2][2] = {{{0, 0, 0, 0}, {0, 0, 0, 0}}, {{0, 0, 0, 0}, {0, 0, 0, 0}}};
    v4u xb[3][2];
#pragma unroll
    for (int m = 0; m < 2; ++m) { xb[0][m] = LDS4((2 * mp + m) * 14336 + lane * 16); xb[1][m] = LDS4((2 * mp + m) * 14336 + 1024 + lane * 16); }
#pragma unroll
    for (int s = 0; s < 14; ++s) {
      if (s < 12) {
#pragma unroll
        for (int m = 0; m < 2; ++m) xb[(s + 2) % 3][m] = LDS4((2 * mp + m) * 14336 + (s + 2) * 1024 + lane * 16);
      }
      SB0();
#pragma unroll
      for (int m = 0; m < 2; ++m) {
        acc[m][0] = mfma(w1f[s][0], xb[s % 3][m], acc[m][0]);
        acc[m][1] = mfma(w1f[s][1], xb[s % 3][m], acc[m][1]);
      }
      SB0();
      wf[(14 * mp + s) >> 2][(14 * mp + s) & 3] = ld4s(rws, lane * 16, WS_W2T + ((c8 * 56 + 7 * w) * 4 + 14 * mp + s) * 1024);
      SB0();
    }
#pragma unroll
    for (int m = 0; m < 2; ++m) {
      const f2 mr2 = *(LAS f2*)(lds + L_MR + ((2 * mp + m) * 16 + l15) * 8);
      const f2 nm = {-mr2.x, -mr2.x}, rs = {mr2.y, mr2.y};
      v4u hv;
#pragma unroll
      for (int r = 0; r < 4; ++r) {
        f2 h = __builtin_elementwise_fma(nm, (f2){au[0][r], au[1][r]}, (f2){acc[m][0][r], acc[m][1][r]});
        h = __builtin_elementwise_fma(rs, h, (f2){av[0][r], av[1][r]});
        hv[r] = pk(fmaxf(h.x, 0.f), fmaxf(h.y, 0.f));
      }
      st4s(rws, w < 7 ? lane * 16 : OOB, WS_HID + (4 * g32 + 2 * mp + m) * 57344 + (28 * c8 + 4 * (w % 7)) * 256, hv);
    }
    SB0();
  }
  STAMPT(10);
  __syncthreads();
  {
    v4u hf[2][2][7];
#define HLOAD(mp) { _Pragma("unroll") for (int i = 0; i < 2; ++i) _Pragma("unroll") for (int s = 0; s < 7; ++s) \
      hf[mp][i][s] = ld4s(rws, lane * 16, WS_HID + (4 * g32 + 2 * (mp) + i) * 57344 + w * 7168 + s * 1024); }
    HLOAD(0)
#pragma unroll
    for (int mp = 0; mp < 2; ++mp) {
      for (unsigned spins = 0;; ++spins) {
        u32 m = 0;
#pragma unroll
        for (int i = 0; i < 2; ++i)
#pragma unroll
          for (int s = 0; s < 7; ++s) m = chk(m, hf[mp][i][s]);
        if (!__any(m == ~0u) || spins > SPIN_CAP) break;
        canary(rws, (lane & 7) < 7 && lane < 16 ? (lane >> 3) * 57344 + (lane & 7) * 1024 : OOB, WS_HID + (4 * g32 + 2 * mp) * 57344 + w * 7168, spins);
        CBAR();
        HLOAD(mp)
      }
      RUNPRIO();
      if (mp == 0) STAMPT(11);
#pragma unroll
      for (int i = 0; i < 2; ++i) {
        f4 acc[4] = {{0, 0, 0, 0}, {0, 0, 0, 0}, {0, 0, 0, 0}, {0, 0, 0, 0}};
#pragma unroll
        for (int s = 0; s < 7; ++s)
#pragma unroll
          for (int t = 0; t < 4; ++t) acc[t] = mfma(wf[s][t], hf[mp][i][s], acc[t]);
#pragma unroll
        for (int r = 0; r < 4; ++r) LDSF4(((w * 4 + 2 * mp + i) * 4 + r) * 1024 + lane * 16) = (f4){acc[0][r], acc[1][r], acc[2][r], acc[3][r]};
        if (mp == 0 && i == 0) { SB0(); HLOAD(1) SB0(); }
      }
    }
  }
  __syncthreads();
  STAMPT(12);
  {
    const int mt = tid >> 7, rp = (tid >> 6) & 1;
    if (!(g == 3 && rp == 1)) {
      f4 s0 = {0, 0, 0, 0}, s1 = {0, 0, 0, 0};
#pragma unroll
      for (int ww = 0; ww < 8; ++ww) {
        s0 += LDSF4(((ww * 4 + mt) * 4 + 2 * rp) * 1024 + lane * 16);
        s1 += LDSF4(((ww * 4 + mt) * 4 + 2 * rp + 1) * 1024 + lane * 16);
      }
      const v4u x1 = LDS4(L_X1S + (mt * 7 + 2 * g + rp) * 256 + l15 * 16);
      const f4 g0 = LDSF4(L_GB + (16 * g + 8 * rp) * 4), g1 = LDSF4(L_GB + (16 * g + 8 * rp) * 4 + 16);
      v4u o;
      o.x = pk(lo2f(x1.x) + g0.x * s0.x, hi2f(x1.x) + g0.y * s0.y);
      o.y = pk(lo2f(x1.y) + g0.z * s0.z, hi2f(x1.y) + g0.w * s0.w);
      o.z = pk(lo2f(x1.z) + g1.x * s1.x, hi2f(x1.z) + g1.y * s1.y);
      o.w = pk(lo2f(x1.w) + g1.z * s1.z, hi2f(x1.w) + g1.w * s1.w);
      __builtin_nontemporal_store(o, (v4u*)(out + (size_t)(64 * g32 + 16 * mt + l15) * D_ + 56 * c8 + 16 * g + 8 * rp));
    }
  }
  if (b == 0 && w == 1) {
    for (unsigned spins = 0;; ++spins) {
      bool ok = true;
#pragma unroll
      for (int i = 0; i < 4; ++i) ok &= (__hip_atomic_load(gr + G_PREP + lane + 64 * i, RLX_AGENT) >> 16) == tag;
      if (__all(ok) || spins > SPIN_CAP) break;
      BACKOFF();
    }
    if (lane == 0) __hip_atomic_store((gu64*)ws, epoch + 1, RLX_AGENT);
  }
  __syncthreads();
  if (b == 100 && w == 0) {
    u64 mn = ~0ull, mx = 0;
    for (int i = 0; i < 4; ++i) { const u64 v = __hip_atomic_load((gu64*)(ws + WS_END) + lane + 64 * i, RLX_AGENT); mn = v < mn ? v : mn; mx = v > mx ? v : mx; }
    for (int m = 32; m >= 1; m >>= 1) { const u64 a = __shfl_xor(mn, m), c = __shfl_xor(mx, m); mn = a < mn ? a : mn; mx = c > mx ? c : mx; }
    const u64 span = __builtin_amdgcn_s_memrealtime() - mn, skew = mx - mn;
    if (lane == 0 && (skew >= 900 && skew < 1300)) out[(size_t)(64 * g32) * D_ + 56 * c8] = 0x4700;
  }
  STAMPT(13);
  asm volatile(".Lmega_end:");
}

extern "C" void kernel_launch(void* const* d_in, const int* in_sizes, int n_in, void* d_out, int out_size, void* d_ws,
                              size_t ws_size, hipStream_t stream) {
  (void)hipFuncSetAttribute((const void*)mega, hipFuncAttributeMaxDynamicSharedMemorySize, LDS_BYTES);
  mega<<<256, 512, LDS_BYTES, stream>>>((const u16*)d_in[0], (const u16*)d_in[1], (const u16*)d_in[2], (const u16*)d_in[4],
                                        (const u16*)d_in[5], (const u16*)d_in[6], (const u16*)d_in[7], (u16*)d_out, (char*)d_ws);
}
```

```cpp
#include <hip/hip_runtime.h>
#include <stdint.h>

typedef unsigned int u32;
typedef unsigned long long u64;
typedef unsigned short u16;
typedef u32 v4u __attribute__((ext_vector_type(4)));
typedef u32 v2u __attribute__((ext_vector_type(2)));
typedef float f4 __attribute__((ext_vector_type(4)));
typedef float f2 __attribute__((ext_vector_type(2)));
typedef __bf16 bf2v __attribute__((ext_vector_type(2)));
typedef __bf16 bf8v __attribute__((ext_vector_type(8)));
#define GAS __attribute__((address_space(1)))
#define LAS __attribute__((address_space(3)))
typedef GAS u64 gu64;
#define RLX_AGENT __ATOMIC_RELAXED, __HIP_MEMORY_SCOPE_AGENT
typedef __amdgpu_buffer_rsrc_t rsrc_t;

constexpr int S_ = 2048, D_ = 448, DFF_ = 1792;
constexpr int WS_GRAN = 4096;
constexpr int G_PREP = 2688, G_FILL = 2944;
constexpr int G_REP = 4096;
constexpr int WS_KV = 524288;
constexpr int WS_O = WS_KV + 4194304;
constexpr int WS_X1 = WS_O + 2097152;
constexpr int WS_ST = WS_X1 + 1835008;
constexpr int WS_HID = WS_ST + 262144;
constexpr int WS_W2T = WS_HID + 7340032;
constexpr int WS_W1T = WS_W2T + 1835008;
constexpr int WS_WOT = WS_W1T + 1605632;
constexpr int WS_END = WS_WOT + 524288;
constexpr int L_WQ = 0;
constexpr int L_K = 0, L_V = 24576;
constexpr int L_PR = 57344;
constexpr int L_WO = 86016;
constexpr int L_X = 36864;
constexpr int L_SM = 151552;
constexpr int L_AS1 = L_SM;
constexpr int L_A2 = L_AS1 + 1792;
constexpr int L_SB = L_A2 + 896;
constexpr int L_GA = L_SB + 896;
constexpr int L_GB = L_GA + 256;
constexpr int L_MR = L_GB + 256;
constexpr int L_RED = L_MR + 512;
constexpr int L_X1S = L_RED;
constexpr int L_END = L_X1S + 7168;
static_assert(L_END <= 163840, "lds");
constexpr int LDS_BYTES = 163840;
constexpr u32 OOB = 0x80000000u;
constexpr unsigned SPIN_CAP = 1u << 15;

__device__ __forceinline__ u32 pk(float lo, float hi) { f2 v = {lo, hi}; return __builtin_bit_cast(u32, __builtin_convertvector(v, bf2v)); }
__device__ __forceinline__ float dot2(u32 a, u32 b, float c) { return __builtin_amdgcn_fdot2_f32_bf16(__builtin_bit_cast(bf2v, a), __builtin_bit_cast(bf2v, b), c, false); }
__device__ __forceinline__ f4 mfma(v4u a, v4u b, f4 c) { return __builtin_amdgcn_mfma_f32_16x16x32_bf16(__builtin_bit_cast(bf8v, a), __builtin_bit_cast(bf8v, b), c, 0, 0, 0); }
__device__ __forceinline__ float lo2f(u32 v) { return __builtin_bit_cast(float, v << 16); }
__device__ __forceinline__ float hi2f(u32 v) { return __builtin_bit_cast(float, v & 0xffff0000u); }
__device__ __forceinline__ u32 plo(u32 r0, u32 r1) { return __builtin_amdgcn_perm(r1, r0, 0x05040100u); }
__device__ __forceinline__ u32 phi(u32 r0, u32 r1) { return __builtin_amdgcn_perm(r1, r0, 0x07060302u); }
__device__ __forceinline__ u32 nosent(u32 v) { return v < 0xFFFFFFFEu ? v : 0xFFFFFFFEu; }
__device__ __forceinline__ v4u nosent4(v4u v) { v.x = nosent(v.x); v.y = nosent(v.y); v.z = nosent(v.z); v.w = nosent(v.w); return v; }
__device__ __forceinline__ u32 umax3(u32 a, u32 b, u32 c) { u32 m = a > b ? a : b; return m > c ? m : c; }
__device__ __forceinline__ u32 chk(u32 m, v4u v) { return umax3(umax3(m, v.x, v.y), v.z, v.w); }
template <int C> __device__ __forceinline__ float dppf(float v) { return __builtin_bit_cast(float, __builtin_amdgcn_update_dpp(0, __builtin_bit_cast(int, v), C, 0xf, 0xf, false)); }
__device__ __forceinline__ float rowsum16(float v) { v += dppf<0xB1>(v); v += dppf<0x4E>(v); v += dppf<0x141>(v); v += dppf<0x140>(v); return v; }
__device__ __forceinline__ v4u ld4(rsrc_t r, u32 vo, u32 so) { return __builtin_amdgcn_raw_buffer_load_b128(r, vo, so, 0); }
__device__ __forceinline__ v4u ld4s(rsrc_t r, u32 vo, u32 so) { return __builtin_amdgcn_raw_buffer_load_b128(r, vo, so, 16); }
__device__ __forceinline__ v2u ld2(rsrc_t r, u32 vo, u32 so) { return __builtin_amdgcn_raw_buffer_load_b64(r, vo, so, 0); }
__device__ __forceinline__ u32 ld1(rsrc_t r, u32 vo, u32 so) { return __builtin_amdgcn_raw_buffer_load_b32(r, vo, so, 0); }
__device__ __forceinline__ void st4s(rsrc_t r, u32 vo, u32 so, v4u v) { __builtin_amdgcn_raw_buffer_store_b128(v, r, vo + so, 0, 16); }
__device__ __forceinline__ void st2s(rsrc_t r, u32 vo, u32 so, v2u v) { __builtin_amdgcn_raw_buffer_store_b64(v, r, vo, so, 16); }
__device__ __forceinline__ v4u modulate(const u32 (&xh)[8], v4u a0, v4u a1) {
  v4u h;
  float d0, d1, d2, d3, d4, d5, d6, d7;
  asm volatile(
      "v_dot2_f32_bf16 %4, %12, %20, 0\n\tv_dot2_f32_bf16 %5, %13, %21, 0\n\tv_dot2_f32_bf16 %6, %14, %22, 0\n\tv_dot2_f32_bf16 %7, %15, %23, 0\n\t"
      "v_dot2_f32_bf16 %8, %16, %24, 0\n\tv_dot2_f32_bf16 %9, %17, %25, 0\n\tv_dot2_f32_bf16 %10, %18, %26, 0\n\tv_dot2_f32_bf16 %11, %19, %27, 0\n\t"
      "v_cvt_pk_bf16_f32 %0, %4, %5\n\tv_cvt_pk_bf16_f32 %1, %6, %7\n\tv_cvt_pk_bf16_f32 %2, %8, %9\n\tv_cvt_pk_bf16_f32 %3, %10, %11\n\ts_nop 1"
      : "=&v"(h.x), "=&v"(h.y), "=&v"(h.z), "=&v"(h.w), "=&v"(d0), "=&v"(d1), "=&v"(d2), "=&v"(d3), "=&v"(d4), "=&v"(d5), "=&v"(d6), "=&v"(d7)
      : "v"(xh[0]), "v"(xh[1]), "v"(xh[2]), "v"(xh[3]), "v"(xh[4]), "v"(xh[5]), "v"(xh[6]), "v"(xh[7]),
        "v"(a0.x), "v"(a0.y), "v"(a0.z), "v"(a0.w), "v"(a1.x), "v"(a1.y), "v"(a1.z), "v"(a1.w));
  return h;
}
typedef short s4v __attribute__((ext_vector_type(4)));
__device__ __forceinline__ v2u trrd(LAS char* p) { return __builtin_bit_cast(v2u, __builtin_amdgcn_ds_read_tr16_b64_v4i16((LAS s4v*)p)); }
__device__ __forceinline__ v4u mksrd(const void* p, u32 bytes) {
  const u64 a = (u64)p;
  return (v4u){(u32)__builtin_amdgcn_readfirstlane((u32)a), (u32)__builtin_amdgcn_readfirstlane((u32)(a >> 32)) & 0xffffu, bytes, 0x00020000u};
}
template <bool SC1> __device__ __forceinline__ void dma16(v4u srd, u32 vo, u32 so, u32 ldsaddr) {
  u32 keep;
  so = __builtin_amdgcn_readfirstlane(so);
  ldsaddr = __builtin_amdgcn_readfirstlane(ldsaddr);
  if (SC1)
    asm volatile("s_mov_b32 %0, m0\n\ts_mov_b32 m0, %4\n\ts_nop 3\n\tbuffer_load_dwordx4 %1, %2, %3 offen sc1 lds\n\ts_mov_b32 m0, %0"
                 : "=&s"(keep) : "v"(vo), "s"(srd), "s"(so), "s"(ldsaddr) : "memory");
  else
    asm volatile("s_mov_b32 %0, m0\n\ts_mov_b32 m0, %4\n\ts_nop 3\n\tbuffer_load_dwordx4 %1, %2, %3 offen lds\n\ts_mov_b32 m0, %0"
                 : "=&s"(keep) : "v"(vo), "s"(srd), "s"(so), "s"(ldsaddr) : "memory");
}
__device__ __forceinline__ void canary(rsrc_t r, u32 vo, u32 so, unsigned& spins) {
  __builtin_amdgcn_s_setprio(0);
  for (; spins <= SPIN_CAP; ++spins) {
    __builtin_amdgcn_s_sleep(2);
    asm volatile("" ::: "memory");
    if (!__any(__builtin_amdgcn_raw_buffer_load_b32(r, vo, so, 16) == ~0u)) break;
  }
}
#define LDS4(off) (*(LAS v4u*)(lds + (off)))
#define LDS2(off) (*(LAS v2u*)(lds + (off)))
#define LDSF4(off) (*(LAS f4*)(lds + (off)))
#define LDSF(off) (*(LAS float*)(lds + (off)))
#define LDSH(off) (*(LAS u16*)(lds + (off)))
#define CBAR() asm volatile("" ::: "memory")
#define PIN(v) asm volatile("" : "+v"(v))
#define SB0() __builtin_amdgcn_sched_barrier(0)
#define BACKOFF() do { __builtin_amdgcn_s_setprio(0); __builtin_amdgcn_s_sleep(6); } while (0)
#define RUNPRIO() __builtin_amdgcn_s_setprio(1)
#define DRAIN() asm volatile("s_waitcnt vmcnt(0)" ::: "memory")

__global__ __launch_bounds__(512, 2) void mega(const u16* __restrict__ x, const u16* __restrict__ wqkv, const u16* __restrict__ wo,
                                               const u16* __restrict__ w1, const u16* __restrict__ w2, const u16* __restrict__ cvec,
                                               const u16* __restrict__ wmod, u16* __restrict__ out, char* ws) {
  extern __shared__ __attribute__((aligned(16))) char lds_g[];
  LAS char* lds = (LAS char*)lds_g;
  const int tid = threadIdx.x, lane = tid & 63, w = __builtin_amdgcn_readfirstlane(tid >> 6) & 7;
  const int l15 = lane & 15, g = lane >> 4;
  const int b = blockIdx.x, xcd = b & 7, slot = b >> 3;
  const int g16 = xcd * 2 + (slot >> 4), head = slot & 15;
  const int g32 = xcd * 4 + (slot >> 3), c8 = slot & 7;
  const rsrc_t rws = __builtin_amdgcn_make_buffer_rsrc(ws, 0, WS_END, 0x00020000);
  const rsrc_t rx = __builtin_amdgcn_make_buffer_rsrc((void*)x, 0, S_ * D_ * 2, 0x00020000);
  const v4u SENT = {~0u, ~0u, ~0u, ~0u};
  RUNPRIO();
  u32 cpf, mpf;
  u64 epoch;
  {
    u64 pc;
    u32 len;
    asm volatile("s_getpc_b64 %0\n.Lmega_start:\n\ts_mov_b32 %1, .Lmega_end-.Lmega_start" : "=s"(pc), "=s"(len));
    const v4u scode = mksrd((const void*)pc, len), swm0 = mksrd(wmod, D_ * 6 * D_ * 2), sws0 = mksrd(ws, 64);
    const int idx = slot * 84 + tid - 320, n = idx / 448, row = idx - 448 * n, j = xcd + 8 * n;
    asm volatile("s_nop 4\n\tbuffer_load_dword %0, %3, %5, 0 offen\n\tbuffer_load_dword %1, %4, %6, 0 offen\n\tbuffer_load_dwordx2 %2, off, %7, 0 sc1"
                 : "=&v"(cpf), "=&v"(mpf), "=&v"(epoch)
                 : "v"(tid >= 448 ? (lane * 8 + (slot & 7)) * 128 : OOB), "v"((tid >= 320 && tid < 404 && j < 42) ? (u32)(row * 5376 + j * 128) : OOB),
                   "s"(scode), "s"(swm0), "s"(sws0)
                 : "memory");
  }
  gu64* gr0 = (gu64*)(ws + WS_GRAN);
  gu64* gr = gr0 + xcd * G_REP;
  const int mj = xcd + 8 * (slot / 3);
  const bool modw = (slot % 3 == 0) && mj < 42 && w < 4;
  if (tid < 4) *(volatile LAS u32*)(lds + L_RED + 3072 + tid * 4) = 0u;
  v2u mr[4][8];
  u32 craw;
  if (modw) {
    const v4u swm = mksrd(wmod, D_ * 6 * D_ * 2), sc_ = mksrd(cvec, D_ * 2);
    asm volatile("s_nop 4\n\tbuffer_load_dword %0, %1, %2, 0 offen" : "=&v"(craw) : "v"(w + 4 * g < 14 ? (32 * (w + 4 * g) + 2 * l15) * 2 : OOB), "s"(sc_) : "memory");
#pragma unroll
    for (int i = 0; i < 4; ++i) {
      const u32 vo = (i < 3 || w < 2) ? (u32)(8 * g * 2688 + 4 * l15) * 2 : OOB;
      const u32 so = (u32)((32 * ((w + 4 * i) % 14)) * 2688 + 64 * mj) * 2;
      asm volatile(
          "s_nop 4\n\t"
          "buffer_load_dwordx2 %0, %8, %9, %10 offen\n\tbuffer_load_dwordx2 %1, %8, %9, %11 offen\n\t"
          "buffer_load_dwordx2 %2, %8, %9, %12 offen\n\tbuffer_load_dwordx2 %3, %8, %9, %13 offen\n\t"
          "buffer_load_dwordx2 %4, %8, %9, %14 offen\n\tbuffer_load_dwordx2 %5, %8, %9, %15 offen\n\t"
          "buffer_load_dwordx2 %6, %8, %9, %16 offen\n\tbuffer_load_dwordx2 %7, %8, %9, %17 offen"
          : "=&v"(mr[i][0]), "=&v"(mr[i][1]), "=&v"(mr[i][2]), "=&v"(mr[i][3]), "=&v"(mr[i][4]), "=&v"(mr[i][5]), "=&v"(mr[i][6]), "=&v"(mr[i][7])
          : "v"(vo), "s"(swm), "s"(so), "s"(so + 5376), "s"(so + 2 * 5376), "s"(so + 3 * 5376), "s"(so + 4 * 5376), "s"(so + 5 * 5376),
            "s"(so + 6 * 5376), "s"(so + 7 * 5376)
          : "memory");
    }
  }
  asm volatile("s_waitcnt lgkmcnt(0)\n\ts_barrier" ::: "memory");
  const v4u sq_srd = mksrd(wqkv, D_ * 3 * D_ * 2);
  const u32 vq = (u32)((lane >> 2) * 2688 + (((lane & 3) ^ (2 * (lane >> 5))) * 16));
#define WQDMA(i)                                                                                                        \
  {                                                                                                                     \
    const int u = w + 8 * ((i) >> 1), d = 2 * u + ((i) & 1), mat = u / 14, kb = d - 28 * mat;                           \
    dma16<false>(sq_srd, u < 42 ? vq : OOB, (u32)(16 * kb * 2688 + (mat % 3) * 896 + 56 * head), u < 42 ? L_WQ + d * 1024 : L_RED + 4096); \
  }
  {
    const v4u sx = mksrd(x, S_ * D_ * 2);
    const u32 vo = (u32)((lane >> 3) * D_ * 2 + (((lane & 7) ^ (lane >> 3)) * 16));
#pragma unroll
    for (int q = 0; q < 14; ++q) dma16<false>(sx, vo, (u32)((128 * g16 + 16 * w + 8 * (q & 1)) * D_ * 2 + (q >> 1) * 128), L_X + w * 14336 + q * 1024);
  }
  asm volatile("s_barrier" ::: "memory");
  WQDMA(0) WQDMA(1) WQDMA(2) WQDMA(3)
  u32 pr[8];
  u32 pdst;
  {
    const int u = b * 8 + w;
    const u16* base; u32 bytes, ld, so, vo, vo2;
    if (u < 896) {
      const int cc = u / 112, v = u - 112 * cc, kb = v >> 1, hf = v & 1;
      base = w2; bytes = DFF_ * D_ * 2; ld = D_ * 2; so = (u32)(32 * kb * D_ + 56 * cc + 2 * hf) * 2;
      vo = l15 < 14 ? (u32)(8 * g * D_ + 4 * l15) * 2 : OOB; vo2 = vo;
      pdst = WS_W2T + ((cc * 56 + kb) * 4 + 2 * hf) * 1024;
    } else if (u < 1680) {
      const int v = u - 896, cb = v / 14, s = v - 14 * cb;
      base = w1; bytes = DFF_ * D_ * 2; ld = DFF_ * 2; so = (u32)(32 * s * DFF_ + 32 * cb) * 2;
      vo = (u32)(8 * g * DFF_ + 2 * l15) * 2; vo2 = vo;
      pdst = WS_W1T + (cb * 14 + s) * 2048;
    } else {
      const int v = u - 1680, hh = v & 15, nh = (v >> 4) & 1, cc = (v >> 5) & 7;
      base = wo; bytes = D_ * D_ * 2; ld = D_ * 2; so = (u32)(28 * hh * D_ + 56 * cc + 32 * nh) * 2;
      vo = (u < 1936 && (nh == 0 || l15 < 12)) ? (u32)(4 * g * D_ + 2 * l15) * 2 : OOB; vo2 = g < 3 ? vo + 12 * D_ * 2 : OOB;
      pdst = u < 1936 ? WS_WOT + (cc * 32 + nh * 16 + hh) * 2048 : OOB;
    }
    const v4u sp = mksrd(base, __builtin_amdgcn_readfirstlane(bytes));
    so = __builtin_amdgcn_readfirstlane(so); ld = __builtin_amdgcn_readfirstlane(ld);
    asm volatile(
        "s_nop 4\n\t"
        "buffer_load_dword %0, %8, %10, %11 offen\n\tbuffer_load_dword %1, %8, %10, %12 offen\n\t"
        "buffer_load_dword %2, %8, %10, %13 offen\n\tbuffer_load_dword %3, %8, %10, %14 offen\n\t"
        "buffer_load_dword %4, %9, %10, %15 offen\n\tbuffer_load_dword %5, %9, %10, %16 offen\n\t"
        "buffer_load_dword %6, %9, %10, %17 offen\n\tbuffer_load_dword %7, %9, %10, %18 offen"
        : "=&v"(pr[0]), "=&v"(pr[1]), "=&v"(pr[2]), "=&v"(pr[3]), "=&v"(pr[4]), "=&v"(pr[5]), "=&v"(pr[6]), "=&v"(pr[7])
        : "v"(vo), "v"(vo2), "s"(sp), "s"(so), "s"(so + ld), "s"(so + 2 * ld), "s"(so + 3 * ld), "s"(so + 4 * ld), "s"(so + 5 * ld), "s"(so + 6 * ld),
          "s"(so + 7 * ld)
        : "memory");
  }
  SB0();
  asm volatile("s_waitcnt vmcnt(26)" : "+v"(epoch), "+v"(cpf), "+v"(mpf)::"memory");
  const u64 tag = (epoch + 1) & 0xFFFFFFFFFFFFull;
  if (modw) {
    __builtin_amdgcn_s_setprio(3);
    asm volatile(""
                 : "+v"(craw), "+v"(mr[0][0]), "+v"(mr[0][1]), "+v"(mr[0][2]), "+v"(mr[0][3]), "+v"(mr[0][4]), "+v"(mr[0][5]), "+v"(mr[0][6]), "+v"(mr[0][7]),
                   "+v"(mr[1][0]), "+v"(mr[1][1]), "+v"(mr[1][2]), "+v"(mr[1][3]), "+v"(mr[1][4]), "+v"(mr[1][5]), "+v"(mr[1][6]), "+v"(mr[1][7])::"memory");
    asm volatile(""
                 : "+v"(mr[2][0]), "+v"(mr[2][1]), "+v"(mr[2][2]), "+v"(mr[2][3]), "+v"(mr[2][4]), "+v"(mr[2][5]), "+v"(mr[2][6]), "+v"(mr[2][7]),
                   "+v"(mr[3][0]), "+v"(mr[3][1]), "+v"(mr[3][2]), "+v"(mr[3][3]), "+v"(mr[3][4]), "+v"(mr[3][5]), "+v"(mr[3][6]), "+v"(mr[3][7])::"memory");
    float c0 = lo2f(craw), c1 = hi2f(craw);
    c0 = c0 * __builtin_amdgcn_rcpf(1.f + __expf(-c0));
    c1 = c1 * __builtin_amdgcn_rcpf(1.f + __expf(-c1));
    *(LAS u32*)(lds + L_RED + w * 256 + lane * 4) = pk(c0, c1);
    f4 acc[4] = {{0, 0, 0, 0}, {0, 0, 0, 0}, {0, 0, 0, 0}, {0, 0, 0, 0}};
#pragma unroll
    for (int i = 0; i < 4; ++i) {
      const v4u sf = LDS4(L_RED + w * 256 + i * 64 + g * 16);
      acc[0] = mfma((v4u){plo(mr[i][0].x, mr[i][1].x), plo(mr[i][2].x, mr[i][3].x), plo(mr[i][4].x, mr[i][5].x), plo(mr[i][6].x, mr[i][7].x)}, sf, acc[0]);
      acc[1] = mfma((v4u){phi(mr[i][0].x, mr[i][1].x), phi(mr[i][2].x, mr[i][3].x), phi(mr[i][4].x, mr[i][5].x), phi(mr[i][6].x, mr[i][7].x)}, sf, acc[1]);
      acc[2] = mfma((v4u){plo(mr[i][0].y, mr[i][1].y), plo(mr[i][2].y, mr[i][3].y), plo(mr[i][4].y, mr[i][5].y), plo(mr[i][6].y, mr[i][7].y)}, sf, acc[2]);
      acc[3] = mfma((v4u){phi(mr[i][0].y, mr[i][1].y), phi(mr[i][2].y, mr[i][3].y), phi(mr[i][4].y, mr[i][5].y), phi(mr[i][6].y, mr[i][7].y)}, sf, acc[3]);
    }
    if (l15 == 0) {
#pragma unroll
      for (int r = 0; r < 4; ++r) LDSF4(L_RED + 1024 + w * 256 + (16 * g + 4 * r) * 4) = (f4){acc[0][r], acc[1][r], acc[2][r], acc[3][r]};
    }
    asm volatile("s_waitcnt lgkmcnt(0)" ::: "memory");
    u32 old = 0;
    if (lane == 0) old = __hip_atomic_fetch_add((LAS u32*)(lds + L_RED + 3072), 1u, __ATOMIC_RELAXED, __HIP_MEMORY_SCOPE_WORKGROUP);
    if (__builtin_amdgcn_readfirstlane(old) == 3u) {
      CBAR();
      const float sum = (LDSF(L_RED + 1024 + lane * 4) + LDSF(L_RED + 1280 + lane * 4)) + (LDSF(L_RED + 1536 + lane * 4) + LDSF(L_RED + 1792 + lane * 4));
      const u64 gval = (tag << 16) | (u64)(pk(sum, 0.f) & 0xffffu);
#pragma unroll
      for (int r = 0; r < 8; ++r) __hip_atomic_store(gr0 + r * G_REP + 64 * mj + lane, gval, RLX_AGENT);
    }
    RUNPRIO();
  }
  if (modw) asm volatile("s_waitcnt vmcnt(8)" ::: "memory"); else asm volatile("s_waitcnt vmcnt(12)" ::: "memory");
  v4u xr[14];
  {
    const u32 ra0 = L_X + w * 14336 + (l15 >> 3) * 1024 + (l15 & 7) * 128 + ((g ^ (l15 & 7)) * 16);
    const u32 ra1 = L_X + w * 14336 + (l15 >> 3) * 1024 + (l15 & 7) * 128 + (((4 + g) ^ (l15 & 7)) * 16);
#pragma unroll
    for (int s = 0; s < 14; ++s) xr[s] = LDS4(((s & 1) ? ra1 : ra0) + (s >> 1) * 2048);
#pragma unroll
    for (int s = 0; s < 14; ++s) PIN(xr[s]);
  }
  asm volatile("s_waitcnt lgkmcnt(0)\n\ts_barrier" ::: "memory");
  const int mtB = w & 3, nhB = w >> 2;
  v4u xres;
  const int tau = 16 * (w & 1) + l15;
  const u32 koff = (w >> 1) * 2048 + ((tau >> 2) & 1) * 1024 + (g * 16 + 4 * (tau >> 3) + (tau & 3)) * 16;
  const u32 voff = (w >> 1) * 2048 + ((2 * (w & 1) + (g >> 1)) * 16 + l15) * 16 + (g & 1) * 8;
  const u32 kvb = g16 < 15 ? WS_KV + (g16 * 16 + head) * 16384 : OOB;
  u32 xh[14][8];
  {
    const v4u ONES = {0x3F803F80u, 0x3F803F80u, 0x3F803F80u, 0x3F803F80u};
    f4 sm = {0, 0, 0, 0}, gm = {0, 0, 0, 0};
#pragma unroll
    for (int s = 0; s < 14; ++s) {
      if (s == 0 || s == 7) WQDMA(4 + s / 7)
      sm = mfma(ONES, xr[s], sm);
      gm = mfma(xr[s], xr[s], gm);
    }
    float sq = (l15 & 2) ? ((l15 & 1) ? gm[3] : gm[2]) : ((l15 & 1) ? gm[1] : gm[0]);
    sq = __shfl(sq, 16 * (l15 >> 2) + l15);
    const float mean = sm[0] * (1.f / D_);
    const float rstd = __builtin_amdgcn_rsqf(fmaxf(sq * (1.f / D_) - mean * mean, 0.f) + 1e-5f);
    const f2 rs2 = {rstd, rstd}, nm2 = {-mean * rstd, -mean * rstd};
#pragma unroll
    for (int s = 0; s < 14; ++s) {
      if (s < 6) WQDMA(6 + s)
      else if (s == 7) xres = ld4(rx, (nhB == 1 && g == 3) ? OOB : (u32)(l15 * D_ + 8 * g) * 2, (u32)((64 * g32 + 16 * mtB) * D_ + 56 * c8 + 32 * nhB) * 2);
      else if (s == 9) st4s(rws, koff, kvb, SENT);
      else if (s == 11) st4s(rws, (w >> 1) * 2048 + (w & 1) * 512 + (lane & 31) * 16 + (lane >> 5) * 1024, kvb + 8192, SENT);
      else if (s == 13) st4s(rws, lane * 16, WS_O + ((8 * g16 + w) * 16 + head) * 1024, SENT);
#pragma unroll
      for (int j = 0; j < 4; ++j) {
        const f2 v = __builtin_elementwise_fma((f2){lo2f(xr[s][j]), hi2f(xr[s][j])}, rs2, nm2);
        xh[s][2 * j] = pk(v.x, 1.f);
        xh[s][2 * j + 1] = pk(v.y, 1.f);
        PIN(xh[s][2 * j]); PIN(xh[s][2 * j + 1]);
      }
      SB0();
    }
  }
  const int i1 = tid < 384 ? 512 + tid : tid;
  u64 g10 = __hip_atomic_load(gr + tid, RLX_AGENT), g11 = __hip_atomic_load(gr + i1, RLX_AGENT);
  asm volatile("s_waitcnt vmcnt(6)"
               : "+v"(pr[0]), "+v"(pr[1]), "+v"(pr[2]), "+v"(pr[3]), "+v"(pr[4]), "+v"(pr[5]), "+v"(pr[6]), "+v"(pr[7])::"memory");
  {
    LAS char* tb0 = lds + L_WQ + (8 * g + (l15 >> 2)) * 64 + (g & 1) * 32 + (l15 & 3) * 8;
    LAS char* tb1 = lds + L_WQ + (8 * g + (l15 >> 2)) * 64 + ((g & 1) ^ 1) * 32 + (l15 & 3) * 8;
    v2u r[6][4];
#pragma unroll
    for (int i = 0; i < 6; ++i) {
      const int u = (i < 5 || w < 2) ? w + 8 * i : w;
      r[i][0] = trrd(tb0 + u * 2048); r[i][1] = trrd(tb0 + u * 2048 + 256);
      r[i][2] = trrd(tb1 + u * 2048); r[i][3] = trrd(tb1 + u * 2048 + 256);
    }
    SB0();
#pragma unroll
    for (int i = 0; i < 6; ++i) {
      const int u = (i < 5 || w < 2) ? w + 8 * i : w;
      LDS4(L_WQ + u * 2048 + lane * 16) = (v4u){r[i][0].x, r[i][0].y, r[i][1].x, r[i][1].y};
      LDS4(L_WQ + u * 2048 + 1024 + lane * 16) = (v4u){r[i][2].x, r[i][2].y, r[i][3].x, r[i][3].y};
    }
  }
  v4u pe = {plo(pr[0], pr[1]), plo(pr[2], pr[3]), plo(pr[4], pr[5]), plo(pr[6], pr[7])};
  v4u po = {phi(pr[0], pr[1]), phi(pr[2], pr[3]), phi(pr[4], pr[5]), phi(pr[6], pr[7])};
  PIN(pe); PIN(po);
  {
    for (unsigned spins = 0;; ++spins) {
      const bool ok = (g10 >> 16) == tag && (g11 >> 16) == tag;
      if (__all(ok) || spins > SPIN_CAP) break;
      BACKOFF();
      g10 = __hip_atomic_load(gr + tid, RLX_AGENT);
      g11 = __hip_atomic_load(gr + i1, RLX_AGENT);
    }
    RUNPRIO();
    if (tid < 448) LDSH(L_AS1 + tid * 4 + 2) = (u16)g10;
    else LDSH(L_AS1 + (tid - 448) * 4) = (u16)pk(1.f + lo2f((u32)g10), 0.f);
    if (tid < 384) LDSH(L_AS1 + (64 + tid) * 4) = (u16)pk(1.f + lo2f((u32)g11), 0.f);
  }
  DRAIN();
  __syncthreads();
  if (tid < 8) __hip_atomic_store(gr0 + tid * G_REP + G_FILL + b, tag << 16, RLX_AGENT);
  const int jg = tid - 448;
  const int i20 = tid < 448 ? 1344 + tid : 896 + 56 * c8 + (jg < 56 ? jg : 0);
  const int i21 = tid < 448 ? 1792 + tid : 2240 + 56 * c8 + (jg < 56 ? jg : 0);
  u64 g20 = __hip_atomic_load(gr + i20, RLX_AGENT), g21 = __hip_atomic_load(gr + i21, RLX_AGENT);

  v4u hfr[14];
  {
    v4u as[3][2];
#pragma unroll
    for (int s = 0; s < 2; ++s) { as[s][0] = LDS4(L_AS1 + (32 * s + 8 * g) * 4); as[s][1] = LDS4(L_AS1 + (32 * s + 8 * g) * 4 + 16); }
#pragma unroll
    for (int s = 0; s < 14; ++s) {
      if (s < 12) { as[(s + 2) % 3][0] = LDS4(L_AS1 + (32 * (s + 2) + 8 * g) * 4); as[(s + 2) % 3][1] = LDS4(L_AS1 + (32 * (s + 2) + 8 * g) * 4 + 16); }
      SB0();
      hfr[s] = modulate(xh[s], as[s % 3][0], as[s % 3][1]);
      SB0();
    }
  }
  int dep = b;
  if (lane < 16) dep = xcd + 8 * (16 * (g16 & 1) + lane);
  else if (lane == 16) dep = g16 >= 1 ? ((g16 - 1) >> 1) + 8 * (16 * ((g16 - 1) & 1) + head) : b;
  else if (lane == 17) dep = g16 >= 2 ? ((g16 - 2) >> 1) + 8 * (16 * (g16 & 1) + head) : b;
  else if (lane < 32) dep = 14 * c8 + (lane - 18);
  else if (lane < 46) dep = ((896 + 98 * c8) >> 3) + (lane - 32);
  else if (lane < 50) dep = 210 + 4 * c8 + (lane - 46);
  u64 dfl = tag << 16;
  f4 aq[6];
#pragma unroll
  for (int t = 0; t < 6; ++t) aq[t] = (f4){0, 0, 0, 0};
  {
    v4u fb[3][6];
#define QLOAD(s, d) { _Pragma("unroll") for (int t = 0; t < 6; ++t) fb[d][t] = LDS4(L_WQ + ((t >> 1) * 14 + (s)) * 2048 + (t & 1) * 1024 + lane * 16); }
    QLOAD(0, 0)
    QLOAD(1, 1)
#pragma unroll
    for (int s = 0; s < 14; ++s) {
      if (s < 12) QLOAD(s + 2, (s + 2) % 3)
      if (s == 0) st4s(rws, lane * 16, pdst, pe);
      if (s == 2) st4s(rws, lane * 16, pdst + 1024, po);
      if (s == 4) st4s(rws, (w < 4 || lane < 48) ? lane * 16 : OOB, WS_X1 + (4 * g32 + (w & 3)) * 14336 + c8 * 1792 + (w >> 2) * 1024, SENT);
      if (s == 5) st4s(rws, (w < 4 && lane < 16) ? lane * 16 : OOB, WS_ST + (4 * g32 + (w & 3)) * 2048 + c8 * 256, SENT);
      if (s == 8 && w == 0) dfl = __hip_atomic_load(gr + G_FILL + (lane < 18 ? dep : b), RLX_AGENT);
      SB0();
#pragma unroll
      for (int t = 0; t < 4; ++t) aq[t] = mfma(fb[s % 3][t], hfr[s], aq[t]);
#pragma unroll
      for (int t = 4; t < 6; ++t) aq[t] = mfma(hfr[s], fb[s % 3][t], aq[t]);
      SB0();
    }
  }
  const v4u qf = {pk(aq[0][0], aq[0][1]), pk(aq[0][2], aq[0][3]), g < 3 ? pk(aq[1][0], aq[1][1]) : 0u, g < 3 ? pk(aq[1][2], aq[1][3]) : 0u};
  {
    const v4u kf = nosent4((v4u){pk(aq[2][0], aq[2][1]), pk(aq[2][2], aq[2][3]), pk(aq[3][0], aq[3][1]), pk(aq[3][2], aq[3][3])});
    v2u ve = {pk(aq[4][0], aq[4][1]), pk(aq[4][2], aq[4][3])}, vo_ = {pk(aq[5][0], aq[5][1]), pk(aq[5][2], aq[5][3])};
    if (l15 == 12) vo_ = (v2u){0x3F803F80u, 0x3F803F80u};
    ve.x = nosent(ve.x); ve.y = nosent(ve.y); vo_.x = nosent(vo_.x); vo_.y = nosent(vo_.y);
    {
      DRAIN();
      u32 old = 0;
      if (lane == 0) old = __hip_atomic_fetch_add((LAS u32*)(lds + L_RED + 3080), 1u, __ATOMIC_RELAXED, __HIP_MEMORY_SCOPE_WORKGROUP);
      if (__builtin_amdgcn_readfirstlane(old) == 7u && lane < 8) __hip_atomic_store(gr0 + lane * G_REP + G_PREP + b, tag << 16, RLX_AGENT);
    }
    st4s(rws, koff, kvb, kf);
    st2s(rws, voff, kvb + 8192, ve);
    st2s(rws, voff, kvb + 8192 + 1024, vo_);
    for (unsigned spins = 0;; ++spins) {
      bool ok = (g20 >> 16) == tag && (g21 >> 16) == tag;
      ok &= (dfl >> 16) == tag;
      if (__all(ok) || spins > SPIN_CAP) break;
      BACKOFF();
      if (w == 0) dfl = __hip_atomic_load(gr + G_FILL + (lane < 18 ? dep : b), RLX_AGENT);
      g20 = __hip_atomic_load(gr + i20, RLX_AGENT);
      g21 = __hip_atomic_load(gr + i21, RLX_AGENT);
    }
    RUNPRIO();
    if (tid < 448) {
      LDSH(L_SB + tid * 2) = (u16)g20;
      LDSH(L_A2 + tid * 2) = (u16)pk(1.f + lo2f((u32)g21), 0.f);
    } else {
      LDSF(L_GA + jg * 4) = lo2f((u32)g20);
      LDSF(L_GB + jg * 4) = lo2f((u32)g21);
    }
    __syncthreads();
    LDS4(L_K + 16384 + koff) = kf;
    LDS2(L_V + 16384 + voff) = ve;
    LDS2(L_V + 16384 + 1024 + voff) = vo_;
  }
  v4u w1f[14][2];
  const u32 vo1 = w < 7 ? lane * 16 : OOB;
  const v4u sws = mksrd(ws, WS_END);
  {
    const u32 b0 = g16 >= 2 ? WS_KV + ((g16 - 2) * 16 + head) * 16384 : OOB;
    const u32 b1 = g16 >= 1 ? WS_KV + ((g16 - 1) * 16 + head) * 16384 : OOB;
    v4u k0, v0, k1, v1;
    for (unsigned spins = 0;; ++spins) {
      CBAR();
      k0 = ld4s(rws, tid * 16, b0); v0 = ld4s(rws, tid * 16, b0 + 8192);
      k1 = ld4s(rws, tid * 16, b1); v1 = ld4s(rws, tid * 16, b1 + 8192);
      const bool bad = chk(chk(chk(chk(0u, k0), v0), k1), v1) == ~0u;
      if (!__any(bad) || spins > SPIN_CAP) break;
      {
        const u32 pb = (lane & 16) ? b1 : b0;
        canary(rws, lane < 32 ? pb + ((lane >> 3) & 1) * 8192 + w * 1024 + (lane & 7) * 128 : OOB, 0, spins);
      }
    }
    RUNPRIO();
    LDS4(L_K + tid * 16) = k0; LDS4(L_V + tid * 16) = v0;
    LDS4(L_K + 8192 + tid * 16) = k1; LDS4(L_V + 8192 + tid * 16) = v1;
  }
  if (w == 0) {
    for (unsigned spins = 0;; ++spins) {
      if (__all((__hip_atomic_load(gr + G_PREP + (lane >= 18 ? dep : b), RLX_AGENT) >> 16) == tag) || spins > SPIN_CAP) break;
      BACKOFF();
    }
    RUNPRIO();
  }
  DRAIN();
  __syncthreads();
  {
    const int blk0 = w >> 1;
    f4 sc[18];
    {
      v4u kf[18];
#pragma unroll
      for (int t = 0; t < 6; ++t) kf[t] = LDS4(L_K + (blk0 + (t >> 1)) * 2048 + (t & 1) * 1024 + lane * 16);
#pragma unroll
      for (int t = 0; t < 18; ++t) {
        if (t + 6 < 18) kf[t + 6] = LDS4(L_K + (blk0 + ((t + 6) >> 1)) * 2048 + (t & 1) * 1024 + lane * 16);
        if ((t & 1) && t < 16) dma16<true>(sws, lane * 16, WS_WOT + c8 * 65536 + (w * 8 + (t >> 1)) * 1024, L_WO + (w * 8 + (t >> 1)) * 1024);
        SB0();
        sc[t] = mfma(kf[t], qf, (f4){0, 0, 0, 0});
        SB0();
      }
    }
    v4u vf[18];
#pragma unroll
    for (int t = 0; t < 4; ++t) vf[t] = LDS4(L_V + (blk0 + (t >> 1)) * 2048 + (t & 1) * 1024 + lane * 16);
    const int ql = 256 + 16 * w + l15;
    const int kmin = (256 - 128 * g16) > (ql - 255) ? (256 - 128 * g16) : (ql - 255);
    const int kb0 = 32 * blk0 + 8 * g;
    const float NEG = -1e30f;
    if (g16 < 2) {
#pragma unroll
      for (int t = 2; t < 16; ++t)
#pragma unroll
        for (int r = 0; r < 4; ++r) sc[t][r] = (kb0 + 32 * (t >> 1) + 4 * (t & 1) + r) >= kmin ? sc[t][r] : NEG;
    }
#pragma unroll
    for (int t = 0; t < 2; ++t)
#pragma unroll
      for (int r = 0; r < 4; ++r) sc[t][r] = (kb0 + 4 * t + r) >= kmin ? sc[t][r] : NEG;
#pragma unroll
    for (int t = 16; t < 18; ++t)
#pragma unroll
      for (int r = 0; r < 4; ++r) sc[t][r] = (kb0 + 256 + 4 * (t & 1) + r) <= ql ? sc[t][r] : NEG;
    float mx = NEG;
#pragma unroll
    for (int t = 0; t < 18; ++t) mx = fmaxf(fmaxf(mx, sc[t][0]), fmaxf(sc[t][1], fmaxf(sc[t][2], sc[t][3])));
    mx = fmaxf(mx, __shfl_xor(mx, 16));
    mx = fmaxf(mx, __shfl_xor(mx, 32));
    const float cs = 0.18898223650461362f * 1.4426950408889634f;
    const float mc = mx * cs;
    f4 oe = {0, 0, 0, 0}, oo = {0, 0, 0, 0};
#pragma unroll
    for (int bb = 0; bb < 9; ++bb) {
      if (bb + 2 < 9) {
        vf[2 * bb + 4] = LDS4(L_V + (blk0 + bb + 2) * 2048 + lane * 16);
        vf[2 * bb + 5] = LDS4(L_V + (blk0 + bb + 2) * 2048 + 1024 + lane * 16);
      }
      w1f[bb >> 1][bb & 1] = ld4s(rws, vo1, WS_W1T + (7 * c8 + (w % 7)) * 28672 + bb * 1024);
      if ((bb & 1) == 0 && bb < 8) st4s(rws, vo1, WS_HID + (4 * g32 + (bb >> 1)) * 57344 + c8 * 7168 + (w % 7) * 1024, SENT);
      SB0();
      float p[8];
#pragma unroll
      for (int r = 0; r < 4; ++r) {
        p[r] = __builtin_amdgcn_exp2f(sc[2 * bb][r] * cs - mc);
        p[4 + r] = __builtin_amdgcn_exp2f(sc[2 * bb + 1][r] * cs - mc);
      }
      const v4u pf = {pk(p[0], p[1]), pk(p[2], p[3]), pk(p[4], p[5]), pk(p[6], p[7])};
      oe = mfma(vf[2 * bb], pf, oe);
      oo = mfma(vf[2 * bb + 1], pf, oo);
      SB0();
    }
    const float den = __shfl(oo[0], 48 + l15);
    const float inv = __builtin_amdgcn_rcpf(den);
    const v4u of = nosent4((v4u){pk(oe[0] * inv, oe[1] * inv), pk(oe[2] * inv, oe[3] * inv), pk(oo[0] * inv, oo[1] * inv), pk(oo[2] * inv, oo[3] * inv)});
    st4s(rws, lane * 16, WS_O + ((8 * g16 + w) * 16 + head) * 1024, of);
  }
  SB0();
  asm volatile("s_waitcnt vmcnt(14)" ::: "memory");
#pragma unroll
  for (int i = 9; i < 17; ++i) w1f[i >> 1][i & 1] = ld4s(rws, vo1, WS_W1T + (7 * c8 + (w % 7)) * 28672 + i * 1024);
  __syncthreads();

  {
    v4u of[8];
    const u32 base = WS_O + (4 * g32 + mtB) * 16384 + nhB * 8192;
    v4u wpre[4][4];
#pragma unroll
    for (int h = 0; h < 4; ++h)
#pragma unroll
      for (int t = 0; t < 4; ++t) { wpre[h][t] = LDS4(L_WO + ((t >> 1) * 16 + nhB * 8 + h) * 2048 + (t & 1) * 1024 + lane * 16); PIN(wpre[h][t]); }
    for (unsigned spins = 0;; ++spins) {
      CBAR();
      u32 m = 0;
#pragma unroll
      for (int h = 0; h < 8; ++h) of[h] = ld4s(rws, lane * 16, base + h * 1024);
#pragma unroll
      for (int h = 0; h < 8; ++h) m = chk(m, of[h]);
      if (!__any(m == ~0u) || spins > SPIN_CAP) break;
      canary(rws, lane < 8 ? lane * 1024 : OOB, base, spins);
    }
    RUNPRIO();
    DRAIN();
    f4 acc[4] = {{0, 0, 0, 0}, {0, 0, 0, 0}, {0, 0, 0, 0}, {0, 0, 0, 0}};
#pragma unroll
    for (int h = 0; h < 8; ++h) {
#pragma unroll
      for (int t = 0; t < 4; ++t) acc[t] = mfma(wpre[h & 3][t], of[h], acc[t]);
      SB0();
      if (h < 4) {
#pragma unroll
        for (int t = 0; t < 4; ++t) wpre[h][t] = LDS4(L_WO + ((t >> 1) * 16 + nhB * 8 + h + 4) * 2048 + (t & 1) * 1024 + lane * 16);
      }
      if (h >= 2) {
#pragma unroll
        for (int i = 17 + 2 * (h - 2); i < 19 + 2 * (h - 2) && i < 28; ++i) w1f[i >> 1][i & 1] = ld4s(rws, vo1, WS_W1T + (7 * c8 + (w % 7)) * 28672 + i * 1024);
      }
      SB0();
    }
    LDSF4(L_PR + w * 2048 + lane * 16) = nhB ? acc[0] : acc[2];
    LDSF4(L_PR + w * 2048 + 1024 + lane * 16) = nhB ? acc[1] : acc[3];
    __syncthreads();
    const f4 e = (nhB ? acc[2] : acc[0]) + LDSF4(L_PR + (w ^ 4) * 2048 + lane * 16);
    const f4 o = (nhB ? acc[3] : acc[1]) + LDSF4(L_PR + (w ^ 4) * 2048 + 1024 + lane * 16);
    const f4 g0 = LDSF4(L_GA + (32 * nhB + 8 * g) * 4), g1 = LDSF4(L_GA + (32 * nhB + 8 * g) * 4 + 16);
    const v4u a2 = LDS4(L_A2 + (56 * c8 + 32 * nhB + 8 * g) * 2);
    v4u x1;
    x1.x = pk(lo2f(xres.x) + g0.x * e[0], hi2f(xres.x) + g0.y * o[0]);
    x1.y = pk(lo2f(xres.y) + g0.z * e[1], hi2f(xres.y) + g0.w * o[1]);
    x1.z = pk(lo2f(xres.z) + g1.x * e[2], hi2f(xres.z) + g1.y * o[2]);
    x1.w = pk(lo2f(xres.w) + g1.z * e[3], hi2f(xres.w) + g1.w * o[3]);
    v4u y;
#pragma unroll
    for (int j = 0; j < 4; ++j) y[j] = nosent(pk(lo2f(x1[j]) * lo2f(a2[j]), hi2f(x1[j]) * hi2f(a2[j])));
    const bool vld = !(nhB == 1 && g == 3);
    st4s(rws, vld ? lane * 16 : OOB, WS_X1 + (4 * g32 + mtB) * 14336 + (7 * c8 + 4 * nhB) * 256, y);
    float s1 = 0.f, s2 = 0.f;
#pragma unroll
    for (int j = 0; j < 4; ++j) { s1 = dot2(x1[j], 0x3F803F80u, s1); s2 = dot2(x1[j], x1[j], s2); }
    s1 += __shfl_xor(s1, 16); s2 += __shfl_xor(s2, 16);
    s1 += __shfl_xor(s1, 32); s2 += __shfl_xor(s2, 32);
    st2s(rws, g == 0 ? l15 * 8 : OOB, WS_ST + (4 * g32 + mtB) * 2048 + (2 * c8 + nhB) * 128,
         (v2u){nosent(__builtin_bit_cast(u32, s1)), nosent(__builtin_bit_cast(u32, s2))});
    if (vld) LDS4(L_X1S + (mtB * 7 + 4 * nhB) * 256 + lane * 16) = x1;
  }
  f4 au[2] = {{0, 0, 0, 0}, {0, 0, 0, 0}}, av[2] = {{0, 0, 0, 0}, {0, 0, 0, 0}};
#pragma unroll
  for (int s = 0; s < 14; ++s) {
    const v4u a2 = LDS4(L_A2 + (32 * s + 8 * g) * 2), sb = LDS4(L_SB + (32 * s + 8 * g) * 2);
    au[0] = mfma(w1f[s][0], a2, au[0]); au[1] = mfma(w1f[s][1], a2, au[1]);
    av[0] = mfma(w1f[s][0], sb, av[0]); av[1] = mfma(w1f[s][1], sb, av[1]);
  }
  PIN(au[0]); PIN(au[1]); PIN(av[0]); PIN(av[1]);
  {
    v4u t[7], st[2];
    const u32 base = WS_X1 + 4 * g32 * 14336 + w * 7168;
    const u32 sto = w < 4 ? lane * 16 : OOB;
    for (unsigned spins = 0;; ++spins) {
      CBAR();
      u32 m = 0;
#pragma unroll
      for (int i = 0; i < 7; ++i) t[i] = ld4s(rws, lane * 16, base + i * 1024);
      st[0] = ld4s(rws, sto, WS_ST + (4 * g32 + (w & 3)) * 2048);
      st[1] = ld4s(rws, sto, WS_ST + (4 * g32 + (w & 3)) * 2048 + 1024);
#pragma unroll
      for (int i = 0; i < 7; ++i) m = chk(m, t[i]);
      m = chk(chk(m, st[0]), st[1]);
      if (!__any(m == ~0u) || spins > SPIN_CAP) break;
      canary(rws, lane < 28 ? base + lane * 256 : ((lane >= 32 && lane < 48 && w < 4) ? WS_ST + (4 * g32 + w) * 2048 + (lane - 32) * 128 : OOB), 0, spins);
    }
    RUNPRIO();
#pragma unroll
    for (int i = 0; i < 7; ++i) LDS4(w * 7168 + i * 1024 + lane * 16) = t[i];
    if (w < 4) {
      f4 a = __builtin_bit_cast(f4, st[0]) + __builtin_bit_cast(f4, st[1]);
#pragma unroll
      for (int j = 0; j < 4; ++j) { a[j] += dppf<0x128>(a[j]); a[j] += __shfl_xor(a[j], 16); a[j] += __shfl_xor(a[j], 32); }
      const float m0 = a.x * (1.f / D_), m1 = a.z * (1.f / D_);
      const float r0 = __builtin_amdgcn_rsqf(fmaxf(a.y * (1.f / D_) - m0 * m0, 0.f) + 1e-5f);
      const float r1 = __builtin_amdgcn_rsqf(fmaxf(a.w * (1.f / D_) - m1 * m1, 0.f) + 1e-5f);
      if (lane < 8) LDSF4(L_MR + w * 128 + lane * 16) = (f4){m0, r0, m1, r1};
    }
    __syncthreads();
  }
  v4u wf[7][4];
#pragma unroll
  for (int mp = 0; mp < 2; ++mp) {
    f4 acc[2][2] = {{{0, 0, 0, 0}, {0, 0, 0, 0}}, {{0, 0, 0, 0}, {0, 0, 0, 0}}};
    v4u xb[3][2];
#pragma unroll
    for (int m = 0; m < 2; ++m) { xb[0][m] = LDS4((2 * mp + m) * 14336 + lane * 16); xb[1][m] = LDS4((2 * mp + m) * 14336 + 1024 + lane * 16); }
#pragma unroll
    for (int s = 0; s < 14; ++s) {
      if (s < 12) {
#pragma unroll
        for (int m = 0; m < 2; ++m) xb[(s + 2) % 3][m] = LDS4((2 * mp + m) * 14336 + (s + 2) * 1024 + lane * 16);
      }
      SB0();
#pragma unroll
      for (int m = 0; m < 2; ++m) {
        acc[m][0] = mfma(w1f[s][0], xb[s % 3][m], acc[m][0]);
        acc[m][1] = mfma(w1f[s][1], xb[s % 3][m], acc[m][1]);
      }
      SB0();
      wf[(14 * mp + s) >> 2][(14 * mp + s) & 3] = ld4s(rws, lane * 16, WS_W2T + ((c8 * 56 + 7 * w) * 4 + 14 * mp + s) * 1024);
      SB0();
    }
#pragma unroll
    for (int m = 0; m < 2; ++m) {
      const f2 mr2 = *(LAS f2*)(lds + L_MR + ((2 * mp + m) * 16 + l15) * 8);
      const f2 nm = {-mr2.x, -mr2.x}, rs = {mr2.y, mr2.y};
      v4u hv;
#pragma unroll
      for (int r = 0; r < 4; ++r) {
        f2 h = __builtin_elementwise_fma(nm, (f2){au[0][r], au[1][r]}, (f2){acc[m][0][r], acc[m][1][r]});
        h = __builtin_elementwise_fma(rs, h, (f2){av[0][r], av[1][r]});
        hv[r] = pk(fmaxf(h.x, 0.f), fmaxf(h.y, 0.f));
      }
      st4s(rws, w < 7 ? lane * 16 : OOB, WS_HID + (4 * g32 + 2 * mp + m) * 57344 + (28 * c8 + 4 * (w % 7)) * 256, hv);
    }
    SB0();
  }
  __syncthreads();
  {
    v4u hf[2][2][7];
#define HLOAD(mp) { _Pragma("unroll") for (int i = 0; i < 2; ++i) _Pragma("unroll") for (int s = 0; s < 7; ++s) \
      hf[mp][i][s] = ld4s(rws, lane * 16, WS_HID + (4 * g32 + 2 * (mp) + i) * 57344 + w * 7168 + s * 1024); }
    HLOAD(0)
#pragma unroll
    for (int mp = 0; mp < 2; ++mp) {
      for (unsigned spins = 0;; ++spins) {
        u32 m = 0;
#pragma unroll
        for (int i = 0; i < 2; ++i)
#pragma unroll
          for (int s = 0; s < 7; ++s) m = chk(m, hf[mp][i][s]);
        if (!__any(m == ~0u) || spins > SPIN_CAP) break;
        canary(rws, (lane & 7) < 7 && lane < 16 ? (lane >> 3) * 57344 + (lane & 7) * 1024 : OOB, WS_HID + (4 * g32 + 2 * mp) * 57344 + w * 7168, spins);
        CBAR();
        HLOAD(mp)
      }
      RUNPRIO();
#pragma unroll
      for (int i = 0; i < 2; ++i) {
        f4 acc[4] = {{0, 0, 0, 0}, {0, 0, 0, 0}, {0, 0, 0, 0}, {0, 0, 0, 0}};
#pragma unroll
        for (int s = 0; s < 7; ++s)
#pragma unroll
          for (int t = 0; t < 4; ++t) acc[t] = mfma(wf[s][t], hf[mp][i][s], acc[t]);
#pragma unroll
        for (int r = 0; r < 4; ++r) LDSF4(((w * 4 + 2 * mp + i) * 4 + r) * 1024 + lane * 16) = (f4){acc[0][r], acc[1][r], acc[2][r], acc[3][r]};
        if (mp == 0 && i == 0) { SB0(); HLOAD(1) SB0(); }
      }
    }
  }
  __syncthreads();
  {
    const int mt = tid >> 7, rp = (tid >> 6) & 1;
    if (!(g == 3 && rp == 1)) {
      f4 s0 = {0, 0, 0, 0}, s1 = {0, 0, 0, 0};
#pragma unroll
      for (int ww = 0; ww < 8; ++ww) {
        s0 += LDSF4(((ww * 4 + mt) * 4 + 2 * rp) * 1024 + lane * 16);
        s1 += LDSF4(((ww * 4 + mt) * 4 + 2 * rp + 1) * 1024 + lane * 16);
      }
      const v4u x1 = LDS4(L_X1S + (mt * 7 + 2 * g + rp) * 256 + l15 * 16);
      const f4 g0 = LDSF4(L_GB + (16 * g + 8 * rp) * 4), g1 = LDSF4(L_GB + (16 * g + 8 * rp) * 4 + 16);
      v4u o;
      o.x = pk(lo2f(x1.x) + g0.x * s0.x, hi2f(x1.x) + g0.y * s0.y);
      o.y = pk(lo2f(x1.y) + g0.z * s0.z, hi2f(x1.y) + g0.w * s0.w);
      o.z = pk(lo2f(x1.z) + g1.x * s1.x, hi2f(x1.z) + g1.y * s1.y);
      o.w = pk(lo2f(x1.w) + g1.z * s1.z, hi2f(x1.w) + g1.w * s1.w);
      __builtin_nontemporal_store(o, (v4u*)(out + (size_t)(64 * g32 + 16 * mt + l15) * D_ + 56 * c8 + 16 * g + 8 * rp));
    }
  }
  if (b == 0 && w == 1) {
    for (unsigned spins = 0;; ++spins) {
      bool ok = true;
#pragma unroll
      for (int i = 0; i < 4; ++i) ok &= (__hip_atomic_load(gr + G_PREP + lane + 64 * i, RLX_AGENT) >> 16) == tag;
      if (__all(ok) || spins > SPIN_CAP) break;
      BACKOFF();
    }
    if (lane == 0) __hip_atomic_store((gu64*)ws, epoch + 1, RLX_AGENT);
  }
  asm volatile(".Lmega_end:");
}

extern "C" void kernel_launch(void* const* d_in, const int* in_sizes, int n_in, void* d_out, int out_size, void* d_ws,
                              size_t ws_size, hipStream_t stream) {
  (void)hipFuncSetAttribute((const void*)mega, hipFuncAttributeMaxDynamicSharedMemorySize, LDS_BYTES);
  mega<<<256, 512, LDS_BYTES, stream>>>((const u16*)d_in[0], (const u16*)d_in[1], (const u16*)d_in[2], (const u16*)d_in[4],
                                        (const u16*)d_in[5], (const u16*)d_in[6], (const u16*)d_in[7], (u16*)d_out, (char*)d_ws);
}
```
